```python
import torch

def _softmax(x, axis=-1):
    return torch.softmax(x, dim=axis)
def _gelu(x):
    return torch.nn.functional.gelu(x, approximate="tanh")
def _silu(x):
    return torch.nn.functional.silu(x)
def _sigmoid(x):
    return torch.sigmoid(x)
def _layernorm(x, axis=-1, eps=1e-5):
    m = x.mean(dim=axis, keepdim=True)
    v = x.var(dim=axis, keepdim=True, unbiased=False)
    return (x - m) / torch.sqrt(v + eps)
def _scatter_add(tgt, idx, src, axis=0):
    return tgt.clone().index_add_(axis, idx, src)
def _segment_sum(src, seg, n):
    out = torch.zeros((n,) + src.shape[1:], dtype=src.dtype)
    return out.index_add_(0, seg, src)
def _pad(x, pw):
    flat = tuple(v for pair in reversed(pw) for v in pair)
    return torch.nn.functional.pad(x, flat)

kernel_name = 'recurrent_scan__linear_kernel_hedgehog__mixer_s_mlp__prompt'

def setup_inputs():

    _w = (lambda a: (lambda t: t.to(torch.bfloat16) if t.is_floating_point() else t)(torch.as_tensor(a)))
    def _A_setup():
        B, S, D, H = 1, 2048, 448, 16
        Dh = D // H
        return {
            "x": _w((lambda *s: torch.randn(*s, dtype=torch.bfloat16) * 0.05)(B, S, D)),
            "wqkv": _w((lambda *s: torch.randn(*s, dtype=torch.bfloat16) * 0.2)(D, 3 * D)),
            "w_phi": _w((lambda *s: torch.randn(*s, dtype=torch.bfloat16) * 0.2)(H, Dh, Dh) * (Dh ** -0.5)),
            "wo": _w((lambda *s: torch.randn(*s, dtype=torch.bfloat16) * 0.05)(D, D)),
        }
    _A_in = _A_setup()

    def _B_setup():
        B, S, D, F = 1, 2048, 448, 1792
        return {
            "x":  _w((lambda *s: torch.randn(*s, dtype=torch.bfloat16) * 0.05)(B, S, D)),
            "w1": _w((lambda *s: torch.randn(*s, dtype=torch.bfloat16) * 0.8)(D, D) * (D ** -0.5)),
            "w2": _w((lambda *s: torch.randn(*s, dtype=torch.bfloat16) * 0.8)(D, D) * (D ** -0.5)),
            "w3": _w((lambda *s: torch.randn(*s, dtype=torch.bfloat16) * 0.8)(D, F) * (D ** -0.5)),
            "w4": _w((lambda *s: torch.randn(*s, dtype=torch.bfloat16) * 0.8)(F, D) * (F ** -0.5)),
        }
    _B_in = _B_setup()

    return {
        "A_x": _A_in["x"],
        "A_wqkv": _A_in["wqkv"],
        "A_w_phi": _A_in["w_phi"],
        "A_wo": _A_in["wo"],
        "B_x": _B_in["x"],
        "B_w1": _B_in["w1"],
        "B_w2": _B_in["w2"],
        "B_w3": _B_in["w3"],
        "B_w4": _B_in["w4"],
    }

def reference(A_x, A_wqkv, A_w_phi, A_wo, B_x, B_w1, B_w2, B_w3, B_w4):
    def _A_ref(x, wqkv, w_phi, wo):
        B, S, D = x.shape
        H = 16; Dh = D // H
        qkv = (lambda x, *d: x.permute(*d))((x @ wqkv).reshape(B, S, 3, H, Dh), 2, 0, 3, 1, 4)
        q, k, v = qkv[0], qkv[1], qkv[2]
        pq = torch.einsum("bhsd,hdf->bhsf", q, w_phi)
        pk = torch.einsum("bhsd,hdf->bhsf", k, w_phi)
        phi_q = torch.concatenate([_softmax(pq, axis=-1), _softmax(-pq, axis=-1)], axis=-1)
        phi_k = torch.concatenate([_softmax(pk, axis=-1), _softmax(-pk, axis=-1)], axis=-1)
        kv = torch.einsum("bhsf,bhsd->bhfd", phi_k, v)
        z  = torch.einsum("bhsf->bhf", phi_k)
        num = torch.einsum("bhqf,bhfd->bhqd", phi_q, kv)
        den = torch.einsum("bhqf,bhf->bhq", phi_q, z)[..., None] + 1e-6
        o = num / den
        return (lambda x, *d: x.permute(*d))(o, 0, 2, 1, 3).reshape(B, S, D) @ wo

    def _B_ref(x, w1, w2, w3, w4):
        B, S, D = x.shape
        h = _gelu(_layernorm(x, axis=-1) @ w1)
        c = D // 4
        g0 = _pad(h[:, :, 0*c:1*c], ((0, 0), (1, 0), (0, 0)))[:, :S, :]
        g1 = _pad(h[:, :, 1*c:2*c], ((0, 0), (0, 1), (0, 0)))[:, 1:, :]
        g2 = _pad(h[:, :, 2*c:3*c], ((0, 0), (2, 0), (0, 0)))[:, :S, :]
        g3 = _pad(h[:, :, 3*c:   ], ((0, 0), (0, 2), (0, 0)))[:, 2:, :]
        hs = torch.concatenate([g0, g1, g2, g3], axis=-1) @ w2
        x1 = x + hs
        h2 = _gelu(_layernorm(x1, axis=-1) @ w3) @ w4
        return x1 + h2

    x = A_x
    xn = _layernorm(x, axis=-1)
    s = _A_ref(xn, A_wqkv, A_w_phi, A_wo)
    g = _silu(_B_ref(xn, B_w1, B_w2, B_w3, B_w4))
    return x + s * g
```

```cpp
#include <hip/hip_runtime.h>
#include <hip/hip_bf16.h>
#include <stdint.h>

#define LAS __attribute__((address_space(3)))
#define DI __device__ __forceinline__
typedef short s16x4 __attribute__((ext_vector_type(4)));
typedef __bf16 bf16x8 __attribute__((ext_vector_type(8)));
typedef __bf16 bf16x2 __attribute__((ext_vector_type(2)));
typedef float f32x4 __attribute__((ext_vector_type(4)));
typedef float f32x2 __attribute__((ext_vector_type(2)));
typedef unsigned u32x4 __attribute__((ext_vector_type(4)));
typedef unsigned u32x2 __attribute__((ext_vector_type(2)));

constexpr int S = 2048, D = 448;
constexpr float LOG2E = 1.4426950408889634f;
constexpr size_t OFF_H = 0;
constexpr size_t OFF_X1 = OFF_H + 128 * 14 * 1024;
constexpr size_t OFF_H3 = OFF_X1 + 128 * 14 * 1024;
constexpr size_t OFF_PQ = OFF_H3 + 128 * 56 * 1024;
constexpr size_t OFF_O = OFF_PQ + 128 * 32 * 1024;
constexpr size_t OFF_KVP = OFF_O + 128 * 16 * 1024;
constexpr size_t OFF_KV = OFF_KVP + 16 * 16 * 8192;
constexpr size_t OFF_ST0 = OFF_KV + 65536;
constexpr size_t OFF_ST1 = OFF_ST0 + 2048 * 8;
constexpr size_t OFF_END = OFF_ST1 + 7 * 2048 * 8;
constexpr size_t OFF_STAMP = 64u << 20;

struct Params {
  const uint16_t *x, *wqkv, *wphi, *wo, *w1, *w2, *w3, *w4;
  uint16_t* out;
  char* ws;
};

DI f32x4 mfma(u32x4 a, u32x4 b, f32x4 c) {
  return __builtin_amdgcn_mfma_f32_16x16x32_bf16(__builtin_bit_cast(bf16x8, a), __builtin_bit_cast(bf16x8, b), c, 0, 0, 0);
}
DI unsigned pk2(float a, float b) {
  f32x2 v = {a, b};
  return __builtin_bit_cast(unsigned, __builtin_convertvector(v, bf16x2));
}
DI float bflo(unsigned u) { return __builtin_bit_cast(float, u << 16); }
DI float bfhi(unsigned u) { return __builtin_bit_cast(float, u & 0xffff0000u); }
DI u32x2 trd(unsigned addr) {
  s16x4 v = __builtin_amdgcn_ds_read_tr16_b64_v4i16((LAS s16x4*)(uintptr_t)addr);
  return __builtin_bit_cast(u32x2, v);
}
DI u32x4 trfrag(unsigned a0, unsigned a1) {
  u32x2 lo = trd(a0), hi = trd(a1);
  return u32x4{lo.x, lo.y, hi.x, hi.y};
}
DI void glds16(const void* gsrc, unsigned lds_dst) {
  unsigned keep;
  asm volatile("s_mov_b32 %0, m0\n\ts_mov_b32 m0, %2\n\ts_nop 0\n\tglobal_load_lds_dwordx4 %1, off\n\ts_mov_b32 m0, %0"
               : "=&s"(keep) : "v"(gsrc), "s"(lds_dst) : "memory");
}
#define WAIT_VM0() asm volatile("s_waitcnt vmcnt(0)" ::: "memory")
DI float xsum(float v) {
  v += __shfl_xor(v, 16);
  v += __shfl_xor(v, 32);
  return v;
}
DI float xmax(float v) { v = fmaxf(v, __shfl_xor(v, 16)); return fmaxf(v, __shfl_xor(v, 32)); }
DI float xmin(float v) { v = fminf(v, __shfl_xor(v, 16)); return fminf(v, __shfl_xor(v, 32)); }
DI float gelu(float y) {
  const float c0 = -2.f * 0.7978845608028654f * LOG2E, c1 = c0 * 0.044715f;
  float u = y * fmaf(y * y, c1, c0);
  return y * __builtin_amdgcn_rcpf(1.f + __builtin_amdgcn_exp2f(u));
}
DI float dot2(unsigned a, unsigned b, float c) {
  return __builtin_amdgcn_fdot2_f32_bf16(__builtin_bit_cast(bf16x2, a), __builtin_bit_cast(bf16x2, b), c, false);
}
#define STAMP_AT(p, k, i) do {} while (0)

DI void dual_softmax(const f32x4& p0, const f32x4& p1, int g, u32x4& fp, u32x4& fm) {
  const bool pad = (g == 3);
  float mx = fmaxf(fmaxf(p0[0], p0[1]), fmaxf(p0[2], p0[3]));
  float mn = fminf(fminf(p0[0], p0[1]), fminf(p0[2], p0[3]));
  float mx1 = fmaxf(fmaxf(p1[0], p1[1]), fmaxf(p1[2], p1[3]));
  float mn1 = fminf(fminf(p1[0], p1[1]), fminf(p1[2], p1[3]));
  mx = pad ? mx : fmaxf(mx, mx1);
  mn = pad ? mn : fminf(mn, mn1);
  mx = xmax(mx) * LOG2E;
  mn = xmin(mn) * LOG2E;
  float ep[8], em[8], sp = 0.f, sm = 0.f;
#pragma unroll
  for (int i = 0; i < 4; ++i) {
    ep[i] = __builtin_amdgcn_exp2f(fmaf(p0[i], LOG2E, -mx));
    em[i] = __builtin_amdgcn_exp2f(fmaf(p0[i], -LOG2E, mn));
    float a = __builtin_amdgcn_exp2f(fmaf(p1[i], LOG2E, -mx));
    float b = __builtin_amdgcn_exp2f(fmaf(p1[i], -LOG2E, mn));
    ep[4 + i] = pad ? 0.f : a;
    em[4 + i] = pad ? 0.f : b;
  }
#pragma unroll
  for (int i = 0; i < 8; ++i) { sp += ep[i]; sm += em[i]; }
  float rp = __builtin_amdgcn_rcpf(xsum(sp)), rm = __builtin_amdgcn_rcpf(xsum(sm));
#pragma unroll
  for (int i = 0; i < 4; ++i) {
    fp[i] = pk2(ep[2 * i] * rp, ep[2 * i + 1] * rp);
    fm[i] = pk2(em[2 * i] * rm, em[2 * i + 1] * rm);
  }
}

__global__ __launch_bounds__(256, 1) void k1(Params p) {
  extern __shared__ __attribute__((aligned(16))) char smem[];
  unsigned long long stv[8]; (void)stv;
  STAMP_AT(p, 0, 0);
  const unsigned L0 = (unsigned)(uintptr_t)(LAS char*)smem;
  const int tid = threadIdx.x, w = __builtin_amdgcn_readfirstlane(tid >> 6), lane = tid & 63, g = lane >> 4, tl = lane & 15;
  const int b = blockIdx.x, r = b >> 3, grp = 2 * (b & 7) + (r >> 4), j = r & 15;
  const int role = j < 8 ? 0 : (j < 12 ? 1 : 2);
  {
    const int rr = lane >> 4, cp = lane & 15;
    const int pitch = role == 2 ? 896 : 2688;
    const char* base = role == 2 ? (const char*)p.w1 : (const char*)p.wqkv;
#pragma unroll 4
    for (int i = 0; i < 28; ++i) {
      const int row = (i * 4 + w) * 4 + rr;
      const int c = cp ^ (((row & 3) << 2) | ((row >> 2) & 2));
      int off;
      if (role == 0) { int cc = c & 7; if (cc == 7) cc = 6; off = 896 + (c >> 3) * 896 + 112 * j + cc * 16; }
      else { int cc = c > 13 ? 13 : c; off = 224 * (role == 1 ? j - 8 : j - 12) + cc * 16; }
      glds16(base + (size_t)row * pitch + off, L0 + (i * 4 + w) * 1024);
    }
  }
  const int tok0 = grp * 128 + w * 32;
  u32x4 xf[2][14];
#pragma unroll
  for (int s = 0; s < 14; ++s)
#pragma unroll
    for (int m = 0; m < 2; ++m)
      xf[m][s] = *(const u32x4*)((const char*)p.x + (size_t)(tok0 + m * 16 + tl) * 896 + s * 64 + g * 16);
  STAMP_AT(p, 0, 4);
  int toff[8];
#pragma unroll
  for (int t = 0; t < 8; ++t) {
    int o0 = (t >> 2) * 128 + ((t >> 1) & 1) * 56 + (t & 1) * 32;
    int o1 = (t >> 1) * 56 + (t & 1) * 32;
    int o2 = t * 32;
    toff[t] = role == 0 ? o0 : (role == 1 ? o1 : o2);
  }
  u32x4 wp[4][2];
  if (role != 2) {
    const int nh = role == 0 ? 2 : 4, h0 = role == 0 ? 2 * j : 4 * (j - 8);
#pragma unroll
    for (int hh = 0; hh < 4; ++hh)
#pragma unroll
      for (int T = 0; T < 2; ++T) {
        unsigned e[8];
#pragma unroll
        for (int jj = 0; jj < 8; ++jj) {
          const int d = 16 * (jj >> 2) + 4 * g + (jj & 3), f = 16 * T + tl;
          const bool ok = hh < nh && d < 28 && f < 28;
          e[jj] = ok ? p.wphi[((h0 + hh) * 28 + d) * 28 + f] : 0;
        }
        wp[hh][T] = u32x4{e[0] | e[1] << 16, e[2] | e[3] << 16, e[4] | e[5] << 16, e[6] | e[7] << 16};
      }
  }
  WAIT_VM0();
  __syncthreads();
  STAMP_AT(p, 0, 1);
  f32x4 acc[8][2], cs[2];
#pragma unroll
  for (int t = 0; t < 8; ++t) { acc[t][0] = f32x4{0, 0, 0, 0}; acc[t][1] = f32x4{0, 0, 0, 0}; }
  cs[0] = cs[1] = f32x4{0, 0, 0, 0};
  const u32x4 ones = {0x3f803f80u, 0x3f803f80u, 0x3f803f80u, 0x3f803f80u};
  unsigned ta[8][2];
  {
    const int q = tl >> 2, pp = tl & 3;
#pragma unroll
    for (int t = 0; t < 8; ++t)
#pragma unroll
      for (int u = 0; u < 2; ++u) {
        const int row = 8 * g + 4 * u + q, by = toff[t] + 8 * pp;
        ta[t][u] = L0 + row * 256 + (((by >> 4) ^ (((row & 3) << 2) | ((row >> 2) & 2))) << 4) + (by & 8);
      }
  }
  {
    const int q = tl >> 2, pp = tl & 3;
    unsigned ca[2][2];
#pragma unroll
    for (int t = 0; t < 2; ++t)
#pragma unroll
      for (int u = 0; u < 2; ++u) {
        const int tt = 2 * w + t;
        const int o0 = (tt >> 2) * 128 + ((tt >> 1) & 1) * 56 + (tt & 1) * 32, o1 = (tt >> 1) * 56 + (tt & 1) * 32, o2 = tt * 32;
        const int row = 8 * g + 4 * u + q, by = (role == 0 ? o0 : (role == 1 ? o1 : o2)) + 8 * pp;
        ca[t][u] = L0 + row * 256 + (((by >> 4) ^ (((row & 3) << 2) | ((row >> 2) & 2))) << 4) + (by & 8);
      }
#pragma unroll
    for (int s = 0; s < 14; ++s) {
      cs[0] = mfma(trfrag(ca[0][0] + s * 8192, ca[0][1] + s * 8192), ones, cs[0]);
      cs[1] = mfma(trfrag(ca[1][0] + s * 8192, ca[1][1] + s * 8192), ones, cs[1]);
    }
  }
  STAMP_AT(p, 0, 3);
  float sm[2] = {0, 0}, sq[2] = {0, 0};
#pragma unroll
  for (int s = 0; s < 14; ++s) {
#pragma unroll
    for (int t = 0; t < 8; ++t) {
      u32x4 a = trfrag(ta[t][0] + s * 8192, ta[t][1] + s * 8192);
      acc[t][0] = mfma(a, xf[0][s], acc[t][0]);
      acc[t][1] = mfma(a, xf[1][s], acc[t][1]);
    }
#pragma unroll
    for (int m = 0; m < 2; ++m)
#pragma unroll
      for (int i = 0; i < 4; ++i) { sm[m] = dot2(xf[m][s][i], 0x3f803f80u, sm[m]); sq[m] = dot2(xf[m][s][i], xf[m][s][i], sq[m]); }
  }
  __syncthreads();
  LAS float* csl = (LAS float*)(smem + 131072);
  if (tl == 0) { *(LAS f32x4*)(csl + (2 * w) * 16 + 4 * g) = cs[0]; *(LAS f32x4*)(csl + (2 * w + 1) * 16 + 4 * g) = cs[1]; }
  float mean[2], r1[2], r12[2];
#pragma unroll
  for (int m = 0; m < 2; ++m) {
    mean[m] = xsum(sm[m]) * (1.f / D);
    float var = fmaxf(xsum(sq[m]) * (1.f / D) - mean[m] * mean[m], 0.f);
    r1[m] = __builtin_amdgcn_rsqf(var + 1e-5f);
    r12[m] = r1[m] * __builtin_amdgcn_rsqf(var * r1[m] * r1[m] + 1e-5f);
  }
  __syncthreads();
  STAMP_AT(p, 0, 2);
  f32x4 csv[8];
#pragma unroll
  for (int t = 0; t < 8; ++t) csv[t] = *(LAS f32x4*)(csl + t * 16 + 4 * g);
  if (role == 2) {
    const int jp = j - 12;
#pragma unroll
    for (int m = 0; m < 2; ++m) {
      const int mt = (tok0 >> 4) + m;
      if (jp == 0 && g == 0) *(f32x2*)(p.ws + OFF_ST0 + (size_t)(mt * 16 + tl) * 8) = f32x2{mean[m], r1[m]};
#pragma unroll
      for (int t = 0; t < 7; ++t) {
        float y[4];
#pragma unroll
        for (int i = 0; i < 4; ++i) y[i] = gelu((acc[t][m][i] - mean[m] * csv[t][i]) * r12[m]);
        const int gt = 7 * jp + t;
        *(u32x2*)(p.ws + OFF_H + ((size_t)mt * 14 + (gt >> 1)) * 1024 + lane * 16 + (gt & 1) * 8) = u32x2{pk2(y[0], y[1]), pk2(y[2], y[3])};
      }
    }
  } else {
    u32x4 fr[4][2];
#pragma unroll
    for (int hh = 0; hh < 4; ++hh)
#pragma unroll
      for (int m = 0; m < 2; ++m) {
        float y[8];
#pragma unroll
        for (int i = 0; i < 8; ++i) y[i] = (acc[2 * hh + (i >> 2)][m][i & 3] - mean[m] * csv[2 * hh + (i >> 2)][i & 3]) * r1[m];
        fr[hh][m] = u32x4{pk2(y[0], y[1]), pk2(y[2], y[3]), pk2(y[4], y[5]), pk2(y[6], y[7])};
      }
    const f32x4 z4 = {0, 0, 0, 0};
    if (role == 1) {
#pragma unroll
      for (int hh = 0; hh < 4; ++hh)
#pragma unroll
        for (int m = 0; m < 2; ++m) {
          f32x4 p0 = mfma(wp[hh][0], fr[hh][m], z4), p1 = mfma(wp[hh][1], fr[hh][m], z4);
          u32x4 fp, fm;
          dual_softmax(p0, p1, g, fp, fm);
          char* dst = p.ws + OFF_PQ + (((size_t)((tok0 >> 4) + m) * 16 + 4 * (j - 8) + hh) * 2) * 1024 + lane * 16;
          *(u32x4*)dst = fp;
          *(u32x4*)(dst + 1024) = fm;
        }
    } else {
#pragma unroll
      for (int hh = 0; hh < 2; ++hh)
#pragma unroll
        for (int m = 0; m < 2; ++m) {
          f32x4 p0 = mfma(wp[hh][0], fr[hh][m], z4), p1 = mfma(wp[hh][1], fr[hh][m], z4);
          u32x4 fp, fm;
          dual_softmax(p0, p1, g, fp, fm);
          u32x4 fv = fr[2 + hh][m];
          if (g == 3) { fv[2] = 0x00003f80u; fv[3] = 0u; }
          const int row = w * 32 + m * 16 + tl, sw = ((row & 3) << 2) | ((row >> 2) & 2);
          LAS char* rb = (LAS char*)smem + hh * 32768 + row * 256 + (g & 1) * 8;
#pragma unroll
          for (int T = 0; T < 2; ++T) {
            const int c = 2 * T + (g >> 1);
            *(LAS u32x2*)(rb + (((c) ^ sw) << 4)) = u32x2{fp[2 * T], fp[2 * T + 1]};
            *(LAS u32x2*)(rb + (((c + 4) ^ sw) << 4)) = u32x2{fm[2 * T], fm[2 * T + 1]};
            *(LAS u32x2*)(rb + (((c + 8) ^ sw) << 4)) = u32x2{fv[2 * T], fv[2 * T + 1]};
          }
        }
      __syncthreads();
      const int hh = w >> 1, sg = w & 1, q = tl >> 2, pp = tl & 3;
      f32x4 kv[2][2];
      kv[0][0] = kv[0][1] = kv[1][0] = kv[1][1] = z4;
#pragma unroll
      for (int s = 0; s < 4; ++s) {
        u32x4 af[2], bf[2];
#pragma unroll
        for (int T = 0; T < 2; ++T) {
          unsigned aa[2], ba[2];
#pragma unroll
          for (int u = 0; u < 2; ++u) {
            const int row = 32 * s + 8 * g + 4 * u + q, sw = ((row & 3) << 2) | ((row >> 2) & 2);
            const unsigned rb = L0 + hh * 32768 + row * 256 + (pp & 1) * 8;
            aa[u] = rb + (((sg * 4 + 2 * T + (pp >> 1)) ^ sw) << 4);
            ba[u] = rb + (((8 + 2 * T + (pp >> 1)) ^ sw) << 4);
          }
          af[T] = trfrag(aa[0], aa[1]);
          bf[T] = trfrag(ba[0], ba[1]);
        }
#pragma unroll
        for (int Td = 0; Td < 2; ++Td)
#pragma unroll
          for (int Tf = 0; Tf < 2; ++Tf) kv[Td][Tf] = mfma(af[Tf], bf[Td], kv[Td][Tf]);
      }
      char* dst = p.ws + OFF_KVP + ((size_t)grp * 16 + 2 * j + hh) * 8192 + sg * 4096 + lane * 16;
#pragma unroll
      for (int Td = 0; Td < 2; ++Td)
#pragma unroll
        for (int Tf = 0; Tf < 2; ++Tf) *(f32x4*)(dst + (Td * 2 + Tf) * 1024) = kv[Td][Tf];
    }
  }
  STAMP_AT(p, 0, 7);
}

__global__ __launch_bounds__(256, 1) void k2(Params p) {
  extern __shared__ __attribute__((aligned(16))) char smem[];
  unsigned long long stv[8]; (void)stv;
  STAMP_AT(p, 1, 0);
  const unsigned L0 = (unsigned)(uintptr_t)(LAS char*)smem;
  const int tid = threadIdx.x, w = __builtin_amdgcn_readfirstlane(tid >> 6), lane = tid & 63, g = lane >> 4, tl = lane & 15;
  const int b = blockIdx.x, r = b >> 3;
  if (r >= 28) {
    const int it = ((b & 7) * 4 + (r - 28)) * 256 + tid;
    if (it < 4096) {
      const int h = it >> 8, st = (it >> 6) & 3, l = it & 63;
      f32x4 a0 = {0, 0, 0, 0}, a1 = {0, 0, 0, 0};
#pragma unroll
      for (int gg = 0; gg < 16; ++gg) {
        const char* src = p.ws + OFF_KVP + ((size_t)gg * 16 + h) * 8192 + st * 2048 + l * 16;
        a0 += *(const f32x4*)src;
        a1 += *(const f32x4*)(src + 1024);
      }
      *(u32x4*)(p.ws + OFF_KV + (size_t)it * 16) = u32x4{pk2(a0[0], a0[1]), pk2(a0[2], a0[3]), pk2(a1[0], a1[1]), pk2(a1[2], a1[3])};
    }
    STAMP_AT(p, 1, 7);
    return;
  }
  const int tt = 4 * (b & 7) + r / 7, cslice = r % 7;
  {
    const int rr = lane >> 3, cp = lane & 7;
#pragma unroll
    for (int i = 0; i < 14; ++i) {
      const int row = (i * 4 + w) * 8 + rr;
      const int c = cp ^ (((row >> 1) & 3) << 1);
      glds16((const char*)p.w2 + (size_t)row * 896 + cslice * 128 + c * 16, L0 + (i * 4 + w) * 1024);
    }
  }
  STAMP_AT(p, 1, 4);
  const int th = w & 1, kh = w >> 1;
  const __amdgpu_buffer_rsrc_t rh = __builtin_amdgcn_make_buffer_rsrc(p.ws + OFF_H, 0, 128 * 14 * 1024, 0x00020000);
  u32x4 hf[2][7];
#pragma unroll
  for (int s = 0; s < 7; ++s)
#pragma unroll
    for (int m = 0; m < 2; ++m) {
      const int P = kh * 7 + s, tok = tt * 64 + th * 32 + m * 16 + tl;
      const int c0 = 32 * P / 112, c1 = (32 * P + 16) / 112;
      const int s0 = c0 == 0 ? -1 : (c0 == 1 ? 1 : (c0 == 2 ? -2 : 2));
      const int s1 = c1 == 0 ? -1 : (c1 == 1 ? 1 : (c1 == 2 ? -2 : 2));
      const int t0 = tok + s0, t1 = tok + s1;
      const unsigned o0 = ((t0 >> 4) * 14 + P) * 1024 + ((t0 & 15) + 16 * g) * 16;
      const unsigned o1 = ((t1 >> 4) * 14 + P) * 1024 + ((t1 & 15) + 16 * g) * 16 + 8;
      u32x2 lo = __builtin_amdgcn_raw_buffer_load_b64(rh, o0, 0, 0), hi = __builtin_amdgcn_raw_buffer_load_b64(rh, o1, 0, 0);
      hf[m][s] = u32x4{lo.x, lo.y, hi.x, hi.y};
    }
  const int ftok = tt * 64 + th * 32 + kh * 16 + tl;
  u32x2 xv[4];
#pragma unroll
  for (int t = 0; t < 4; ++t) xv[t] = *(const u32x2*)((const char*)p.x + (size_t)ftok * 896 + cslice * 128 + t * 32 + g * 8);
  const f32x2 st0 = *(const f32x2*)(p.ws + OFF_ST0 + (size_t)ftok * 8);
  WAIT_VM0();
  __syncthreads();
  STAMP_AT(p, 1, 1);
  f32x4 acc[4][2];
#pragma unroll
  for (int t = 0; t < 4; ++t) acc[t][0] = acc[t][1] = f32x4{0, 0, 0, 0};
  {
    const int q = tl >> 2, pp = tl & 3, row = 4 * g + q, sw = ((row >> 1) & 3) << 1;
#pragma unroll
    for (int s = 0; s < 7; ++s)
#pragma unroll
      for (int t = 0; t < 4; ++t) {
        const unsigned a = L0 + ((kh * 7 + s) * 32 + row) * 128 + (((2 * t + (pp >> 1)) ^ sw) << 4) + (pp & 1) * 8;
        u32x4 af = trfrag(a, a + 16 * 128);
        acc[t][0] = mfma(af, hf[0][s], acc[t][0]);
        acc[t][1] = mfma(af, hf[1][s], acc[t][1]);
      }
  }
  LAS f32x4* ex = (LAS f32x4*)(smem + 65536);
#pragma unroll
  for (int t = 0; t < 4; ++t) ex[(w * 4 + t) * 64 + lane] = kh ? acc[t][0] : acc[t][1];
  __syncthreads();
  float sum = 0, sq = 0;
  unsigned o[8];
#pragma unroll
  for (int t = 0; t < 4; ++t) {
    f32x4 v = (kh ? acc[t][1] : acc[t][0]) + ex[((w ^ 2) * 4 + t) * 64 + lane];
    float x0 = bflo(xv[t].x), x1 = bfhi(xv[t].x), x2 = bflo(xv[t].y), x3 = bfhi(xv[t].y);
    o[2 * t] = pk2(fmaf(x0 - st0.x, st0.y, v[0]), fmaf(x1 - st0.x, st0.y, v[1]));
    o[2 * t + 1] = pk2(fmaf(x2 - st0.x, st0.y, v[2]), fmaf(x3 - st0.x, st0.y, v[3]));
    sum = dot2(o[2 * t], 0x3f803f80u, sum); sum = dot2(o[2 * t + 1], 0x3f803f80u, sum);
    sq = dot2(o[2 * t], o[2 * t], sq); sq = dot2(o[2 * t + 1], o[2 * t + 1], sq);
  }
  char* dst = p.ws + OFF_X1 + ((size_t)(ftok >> 4) * 14 + 2 * cslice) * 1024 + lane * 16;
  *(u32x4*)dst = u32x4{o[0], o[1], o[2], o[3]};
  *(u32x4*)(dst + 1024) = u32x4{o[4], o[5], o[6], o[7]};
  sum = xsum(sum); sq = xsum(sq);
  if (g == 0) *(f32x2*)(p.ws + OFF_ST1 + ((size_t)cslice * 2048 + ftok) * 8) = f32x2{sum, sq};
  STAMP_AT(p, 1, 7);
}

__global__ __launch_bounds__(256, 1) void k3(Params p) {
  extern __shared__ __attribute__((aligned(16))) char smem[];
  unsigned long long stv[8]; (void)stv;
  STAMP_AT(p, 2, 0);
  const unsigned L0 = (unsigned)(uintptr_t)(LAS char*)smem;
  const int tid = threadIdx.x, w = __builtin_amdgcn_readfirstlane(tid >> 6), lane = tid & 63, g = lane >> 4, tl = lane & 15;
  const int b = blockIdx.x, r = b >> 3;
  if (r >= 28) {
    const int mt = ((b & 7) * 4 + (r - 28)) * 4 + w;
    const f32x4 z4 = {0, 0, 0, 0};
#pragma unroll 4
    for (int h = 0; h < 16; ++h) {
      const char* pq = p.ws + OFF_PQ + ((size_t)mt * 16 + h) * 2048 + lane * 16;
      const char* kv = p.ws + OFF_KV + (size_t)h * 4096 + lane * 16;
      u32x4 fp = *(const u32x4*)pq, fm = *(const u32x4*)(pq + 1024);
      f32x4 n0 = mfma(*(const u32x4*)kv, fp, z4), n1 = mfma(*(const u32x4*)(kv + 1024), fp, z4);
      n0 = mfma(*(const u32x4*)(kv + 2048), fm, n0);
      n1 = mfma(*(const u32x4*)(kv + 3072), fm, n1);
      const float rd = __builtin_amdgcn_rcpf(__shfl(n1[0], 48 + tl) + 1e-6f);
      u32x4 o = {pk2(n0[0] * rd, n0[1] * rd), pk2(n0[2] * rd, n0[3] * rd), pk2(n1[0] * rd, n1[1] * rd), pk2(n1[2] * rd, n1[3] * rd)};
      if (g == 3) { o[2] = 0; o[3] = 0; }
      *(u32x4*)(p.ws + OFF_O + ((size_t)mt * 16 + h) * 1024 + lane * 16) = o;
    }
    STAMP_AT(p, 2, 7);
    return;
  }
  const int grp = 2 * (b & 7) + r / 14, cslice = r % 14;
  {
    const int rr = lane >> 4, cp = lane & 15;
#pragma unroll 4
    for (int i = 0; i < 28; ++i) {
      const int row = (i * 4 + w) * 4 + rr;
      const int c = cp ^ ((row & 7) << 1);
      glds16((const char*)p.w3 + (size_t)row * 3584 + cslice * 256 + c * 16, L0 + (i * 4 + w) * 1024);
    }
  }
  const int mt0 = grp * 8 + w * 2;
  u32x4 xf[2][14];
#pragma unroll
  for (int s = 0; s < 14; ++s)
#pragma unroll
    for (int m = 0; m < 2; ++m) xf[m][s] = *(const u32x4*)(p.ws + OFF_X1 + ((size_t)(mt0 + m) * 14 + s) * 1024 + lane * 16);
  STAMP_AT(p, 2, 4);
  float mean[2], rs[2];
#pragma unroll
  for (int m = 0; m < 2; ++m) {
    f32x2 a = {0, 0};
#pragma unroll
    for (int c = 0; c < 7; ++c) a += *(const f32x2*)(p.ws + OFF_ST1 + ((size_t)c * 2048 + (mt0 + m) * 16 + tl) * 8);
    mean[m] = a.x * (1.f / D);
    rs[m] = __builtin_amdgcn_rsqf(fmaxf(a.y * (1.f / D) - mean[m] * mean[m], 0.f) + 1e-5f);
  }
  WAIT_VM0();
  __syncthreads();
  STAMP_AT(p, 2, 1);
  f32x4 acc[8][2], cs[2];
#pragma unroll
  for (int t = 0; t < 8; ++t) acc[t][0] = acc[t][1] = f32x4{0, 0, 0, 0};
  cs[0] = cs[1] = f32x4{0, 0, 0, 0};
  const u32x4 ones = {0x3f803f80u, 0x3f803f80u, 0x3f803f80u, 0x3f803f80u};
  {
    const int q = tl >> 2, pp = tl & 3, row = 4 * g + q, sw = (row & 7) << 1;
    unsigned ta[8];
#pragma unroll
    for (int t = 0; t < 8; ++t) ta[t] = L0 + row * 256 + (((2 * t + (pp >> 1)) ^ sw) << 4) + (pp & 1) * 8;
    {
      const unsigned c0 = L0 + row * 256 + (((4 * w + (pp >> 1)) ^ sw) << 4) + (pp & 1) * 8, c1 = L0 + row * 256 + (((4 * w + 2 + (pp >> 1)) ^ sw) << 4) + (pp & 1) * 8;
#pragma unroll
      for (int s = 0; s < 14; ++s) {
        cs[0] = mfma(trfrag(c0 + s * 8192, c0 + s * 8192 + 4096), ones, cs[0]);
        cs[1] = mfma(trfrag(c1 + s * 8192, c1 + s * 8192 + 4096), ones, cs[1]);
      }
    }
    STAMP_AT(p, 2, 3);
#pragma unroll
    for (int s = 0; s < 14; ++s)
#pragma unroll
      for (int t = 0; t < 8; ++t) {
        u32x4 a = trfrag(ta[t] + s * 8192, ta[t] + s * 8192 + 4096);
        acc[t][0] = mfma(a, xf[0][s], acc[t][0]);
        acc[t][1] = mfma(a, xf[1][s], acc[t][1]);
      }
  }
  LAS float* csl = (LAS float*)(smem + 131072);
  if (tl == 0) { *(LAS f32x4*)(csl + (2 * w) * 16 + 4 * g) = cs[0]; *(LAS f32x4*)(csl + (2 * w + 1) * 16 + 4 * g) = cs[1]; }
  __syncthreads();
  STAMP_AT(p, 2, 2);
#pragma unroll
  for (int P = 0; P < 4; ++P) {
    const f32x4 c0 = *(LAS f32x4*)(csl + (2 * P) * 16 + 4 * g), c1 = *(LAS f32x4*)(csl + (2 * P + 1) * 16 + 4 * g);
#pragma unroll
    for (int m = 0; m < 2; ++m) {
      float y[8];
#pragma unroll
      for (int i = 0; i < 4; ++i) {
        y[i] = gelu((acc[2 * P][m][i] - mean[m] * c0[i]) * rs[m]);
        y[4 + i] = gelu((acc[2 * P + 1][m][i] - mean[m] * c1[i]) * rs[m]);
      }
      *(u32x4*)(p.ws + OFF_H3 + ((size_t)(mt0 + m) * 56 + cslice * 4 + P) * 1024 + lane * 16) =
          u32x4{pk2(y[0], y[1]), pk2(y[2], y[3]), pk2(y[4], y[5]), pk2(y[6], y[7])};
    }
  }
  STAMP_AT(p, 2, 7);
}

__global__ __launch_bounds__(256, 1) void k4(Params p) {
  extern __shared__ __attribute__((aligned(16))) char smem[];
  unsigned long long stv[8]; (void)stv;
  STAMP_AT(p, 3, 0);
  const unsigned L0 = (unsigned)(uintptr_t)(LAS char*)smem;
  const int tid = threadIdx.x, w = __builtin_amdgcn_readfirstlane(tid >> 6), lane = tid & 63, g = lane >> 4, tl = lane & 15;
  const int b = blockIdx.x, r = b >> 3;
  if (r >= 28) return;
  const int tt = 4 * (b & 7) + r / 7, cslice = r % 7;
  const int nstage = w == 3 ? 8 : 10;
  const int ns = w == 2 ? 8 : nstage;
  const int ks0 = w == 3 ? 0 : 20 * w;
  const char* wbase = (w == 3 ? (const char*)p.wo : (const char*)p.w4) + cslice * 128;
  const int krows = w == 3 ? 28 : 32, kmax = w == 3 ? 447 : 1791;
  const char* abase = p.ws + (w == 3 ? OFF_O : OFF_H3) + (size_t)(tt * 4) * (w == 3 ? 16 : 56) * 1024 + lane * 16;
  const size_t amt = (size_t)(w == 3 ? 16 : 56) * 1024;
  const unsigned ring = L0 + w * 16384;
  const int rr = lane >> 3, cp = lane & 7;
  auto issueW = [&](int st) {
#pragma unroll
    for (int i = 0; i < 8; ++i) {
      const int lr = (i & 3) * 8 + rr;
      int row = (ks0 + 2 * st + (i >> 2)) * krows + lr;
      row = row > kmax ? kmax : row;
      const int c = cp ^ (((lr >> 1) & 3) << 1);
      glds16(wbase + (size_t)row * 896 + c * 16, ring + (st & 1) * 8192 + i * 1024);
    }
  };
  u32x4 af[2][2][4];
  auto issueA = [&](int st, u32x4 (&d)[2][4]) {
#pragma unroll
    for (int k = 0; k < 2; ++k)
#pragma unroll
      for (int m = 0; m < 4; ++m) d[k][m] = *(const u32x4*)(abase + m * amt + (size_t)(ks0 + 2 * st + k) * 1024);
  };
  f32x4 acc[4][4];
#pragma unroll
  for (int t = 0; t < 4; ++t)
#pragma unroll
    for (int m = 0; m < 4; ++m) acc[t][m] = f32x4{0, 0, 0, 0};
  const int q = tl >> 2, pp = tl & 3, row = 4 * g + q, sw = ((row >> 1) & 3) << 1;
  unsigned ta[4];
#pragma unroll
  for (int t = 0; t < 4; ++t) ta[t] = ring + row * 128 + (((2 * t + (pp >> 1)) ^ sw) << 4) + (pp & 1) * 8;
  auto comp = [&](int buf, u32x4 (&d)[2][4]) {
#pragma unroll
    for (int k = 0; k < 2; ++k)
#pragma unroll
      for (int t = 0; t < 4; ++t) {
        u32x4 a = trfrag(ta[t] + buf * 8192 + k * 4096, ta[t] + buf * 8192 + k * 4096 + 2048);
#pragma unroll
        for (int m = 0; m < 4; ++m) acc[t][m] = mfma(a, d[k][m], acc[t][m]);
      }
  };
  const int ftok = tt * 64 + w * 16 + tl;
  u32x2 xv[4];
#pragma unroll
  for (int t = 0; t < 4; ++t) xv[t] = *(const u32x2*)((const char*)p.x + (size_t)ftok * 896 + cslice * 128 + t * 32 + g * 8);
  u32x4 x1v[2];
  x1v[0] = *(const u32x4*)(p.ws + OFF_X1 + ((size_t)(ftok >> 4) * 14 + 2 * cslice) * 1024 + lane * 16);
  x1v[1] = *(const u32x4*)(p.ws + OFF_X1 + ((size_t)(ftok >> 4) * 14 + 2 * cslice + 1) * 1024 + lane * 16);
  issueW(0);
  issueA(0, af[0]);
  for (int st = 0; st < ns; st += 2) {
    __builtin_amdgcn_s_waitcnt(0x0F70);
    WAIT_VM0();
    issueW(st + 1);
    issueA(st + 1, af[1]);
    comp(0, af[0]);
    __builtin_amdgcn_s_waitcnt(0x0070);
    WAIT_VM0();
    if (st + 2 < ns) { issueW(st + 2); issueA(st + 2, af[0]); }
    comp(1, af[1]);
    __builtin_amdgcn_s_waitcnt(0xC07F);
  }
  STAMP_AT(p, 3, 2);
  LAS f32x4* ex = (LAS f32x4*)(smem + 65536);
#pragma unroll
  for (int m = 0; m < 4; ++m)
#pragma unroll
    for (int t = 0; t < 4; ++t) ex[((w * 4 + m) * 4 + t) * 64 + lane] = acc[t][m];
  __syncthreads();
#pragma unroll
  for (int t = 0; t < 4; ++t) {
    f32x4 h2 = ex[((0 * 4 + w) * 4 + t) * 64 + lane] + ex[((1 * 4 + w) * 4 + t) * 64 + lane] + ex[((2 * 4 + w) * 4 + t) * 64 + lane];
    f32x4 sv = ex[((3 * 4 + w) * 4 + t) * 64 + lane];
    const unsigned a0 = x1v[t >> 1][(t & 1) * 2], a1 = x1v[t >> 1][(t & 1) * 2 + 1];
    float bv[4] = {bflo(a0) + h2[0], bfhi(a0) + h2[1], bflo(a1) + h2[2], bfhi(a1) + h2[3]};
    float xx[4] = {bflo(xv[t].x), bfhi(xv[t].x), bflo(xv[t].y), bfhi(xv[t].y)};
    float o[4];
#pragma unroll
    for (int i = 0; i < 4; ++i) o[i] = fmaf(sv[i], bv[i] * __builtin_amdgcn_rcpf(1.f + __builtin_amdgcn_exp2f(-LOG2E * bv[i])), xx[i]);
    *(u32x2*)((char*)p.out + (size_t)ftok * 896 + cslice * 128 + t * 32 + g * 8) = u32x2{pk2(o[0], o[1]), pk2(o[2], o[3])};
  }
  STAMP_AT(p, 3, 7);
}

extern "C" void kernel_launch(void* const* d_in, const int* in_sizes, int n_in, void* d_out, int out_size, void* d_ws, size_t ws_size,
                              hipStream_t stream) {
  Params p;
  p.x = (const uint16_t*)d_in[0]; p.wqkv = (const uint16_t*)d_in[1]; p.wphi = (const uint16_t*)d_in[2]; p.wo = (const uint16_t*)d_in[3];
  p.w1 = (const uint16_t*)d_in[5]; p.w2 = (const uint16_t*)d_in[6]; p.w3 = (const uint16_t*)d_in[7]; p.w4 = (const uint16_t*)d_in[8];
  p.out = (uint16_t*)d_out; p.ws = (char*)d_ws;
  constexpr int LDS = 132 * 1024;
  hipFuncSetAttribute((const void*)k1, hipFuncAttributeMaxDynamicSharedMemorySize, LDS);
  hipFuncSetAttribute((const void*)k2, hipFuncAttributeMaxDynamicSharedMemorySize, LDS);
  hipFuncSetAttribute((const void*)k3, hipFuncAttributeMaxDynamicSharedMemorySize, LDS);
  hipFuncSetAttribute((const void*)k4, hipFuncAttributeMaxDynamicSharedMemorySize, LDS);
  k1<<<256, 256, LDS, stream>>>(p);
  k2<<<256, 256, LDS, stream>>>(p);
  k3<<<256, 256, LDS, stream>>>(p);
  k4<<<256, 256, LDS, stream>>>(p);
}
```

```cpp
#include <hip/hip_runtime.h>
#include <stdint.h>

#define LAS __attribute__((address_space(3)))
#define GAS __attribute__((address_space(1)))
#define DI __device__ __forceinline__
typedef short s16x4 __attribute__((ext_vector_type(4)));
typedef __bf16 bf16x8 __attribute__((ext_vector_type(8)));
typedef __bf16 bf16x2 __attribute__((ext_vector_type(2)));
typedef float f32x16 __attribute__((ext_vector_type(16)));
typedef float f32x4 __attribute__((ext_vector_type(4)));
typedef float f32x2 __attribute__((ext_vector_type(2)));
typedef unsigned u32x4 __attribute__((ext_vector_type(4)));
typedef unsigned u32x2 __attribute__((ext_vector_type(2)));
typedef unsigned long long u64;
typedef __amdgpu_buffer_rsrc_t srd_t;

constexpr int D = 448;
constexpr float LOG2E = 1.4426950408889634f;
constexpr unsigned OFF_FLG = 0;
constexpr unsigned OFF_H = 65536;
constexpr unsigned OFF_X1 = OFF_H + 64 * 28 * 1024;
constexpr unsigned OFF_QKV = OFF_X1 + 64 * 28 * 1024;
constexpr unsigned OFF_H3 = OFF_QKV + 64 * 84 * 1024;
constexpr unsigned OFF_O = OFF_H3 + 64 * 112 * 1024;
constexpr unsigned OFF_KVP = OFF_O + 64 * 32 * 1024;
constexpr unsigned OFF_KV = OFF_KVP + 256 * 8192;
constexpr unsigned OFF_ST0 = OFF_KV + 65536;
constexpr unsigned OFF_ST1 = OFF_ST0 + 2048 * 8;
constexpr unsigned WS_USED = OFF_ST1 + 14 * 2048 * 8;
constexpr size_t OFF_STAMP = 64u << 20;
constexpr int FL_E = 0, FL_H = 32, FL_Q = FL_H + 16 * 32, FL_X = FL_Q + 16 * 32, FL_KV = FL_X + 16 * 32, FL_KR = FL_KV + 16 * 32,
              FL_3 = FL_KR + 16 * 32, FL_O = FL_3 + 16 * 32;
constexpr unsigned R0 = 0, R1 = 57344, R2 = 114688, SCR = R2 + 32768, LDS_BYTES = 163840;
constexpr unsigned CODE_BYTES = 36864;
constexpr unsigned SPIN_CAP = 1u << 18;

struct Params {
  const uint16_t *x, *wqkv, *wphi, *wo, *w1, *w2, *w3, *w4;
  uint16_t* out;
  char* ws;
};

template <int I> struct IC { static constexpr int v = I; };
template <int B, int E, class F> DI void sfor(F&& f) {
  if constexpr (B < E) { f(IC<B>{}); sfor<B + 1, E>(f); }
}
DI f32x16 mfma(u32x4 a, u32x4 b, f32x16 c) {
  return __builtin_amdgcn_mfma_f32_32x32x16_bf16(__builtin_bit_cast(bf16x8, a), __builtin_bit_cast(bf16x8, b), c, 0, 0, 0);
}
DI unsigned pk2(float a, float b) {
  f32x2 v = {a, b};
  return __builtin_bit_cast(unsigned, __builtin_convertvector(v, bf16x2));
}
DI float u2f(unsigned u) { return __builtin_bit_cast(float, u); }
DI unsigned f2u(float f) { return __builtin_bit_cast(unsigned, f); }
DI float bflo(unsigned u) { return __builtin_bit_cast(float, u << 16); }
DI float bfhi(unsigned u) { return __builtin_bit_cast(float, u & 0xffff0000u); }
DI u32x4 trfrag(unsigned a0, unsigned a1) {
  u32x2 lo = __builtin_bit_cast(u32x2, __builtin_amdgcn_ds_read_tr16_b64_v4i16((LAS s16x4*)(uintptr_t)a0));
  u32x2 hi = __builtin_bit_cast(u32x2, __builtin_amdgcn_ds_read_tr16_b64_v4i16((LAS s16x4*)(uintptr_t)a1));
  return u32x4{lo.x, lo.y, hi.x, hi.y};
}
template <class T> DI T ldsr(unsigned a) { return *(LAS T*)(uintptr_t)a; }
template <class T> DI void ldsw(unsigned a, T v) { *(LAS T*)(uintptr_t)a = v; }
DI srd_t mk(const void* p, unsigned bytes) { return __builtin_amdgcn_make_buffer_rsrc((void*)p, 0, (int)bytes, 0x00020000); }
DI void dma16(unsigned voff, srd_t r, unsigned soff, unsigned lds) {
  asm volatile("s_mov_b32 m0, %3\n\ts_nop 0\n\tbuffer_load_dwordx4 %0, %1, %2 offen lds" ::"v"(voff), "s"(r), "s"(soff), "s"(lds) : "memory");
}
template <int IMM> DI void ld16s(u32x4& d, unsigned voff, srd_t r, unsigned soff) {
  asm volatile("buffer_load_dwordx4 %0, %1, %2, %3 offen offset:%c4 sc1" : "=v"(d) : "v"(voff), "s"(r), "s"(soff), "i"(IMM) : "memory");
}
template <int IMM> DI void ld8s(u32x2& d, unsigned voff, srd_t r, unsigned soff) {
  asm volatile("buffer_load_dwordx2 %0, %1, %2, %3 offen offset:%c4 sc1" : "=v"(d) : "v"(voff), "s"(r), "s"(soff), "i"(IMM) : "memory");
}
template <int IMM> DI void ld8p(u32x2& d, unsigned voff, srd_t r, unsigned soff) {
  asm volatile("buffer_load_dwordx2 %0, %1, %2, %3 offen offset:%c4" : "=v"(d) : "v"(voff), "s"(r), "s"(soff), "i"(IMM) : "memory");
}
template <int IMM = 0> DI void st16s(u32x4 v, srd_t r, unsigned voff, unsigned soff) {
  asm volatile("buffer_store_dwordx4 %0, %1, %2, %3 offen offset:%c4 sc1\n\ts_nop 1" ::"v"(v), "v"(voff), "s"(r), "s"(soff), "i"(IMM) : "memory");
}
DI void st8s(u32x2 v, srd_t r, unsigned voff, unsigned soff) {
  asm volatile("buffer_store_dwordx2 %0, %1, %2, %3 offen sc1" ::"v"(v), "v"(voff), "s"(r), "s"(soff) : "memory");
}
template <int N> DI void wvm() { asm volatile("s_waitcnt vmcnt(%c0)" ::"i"(N) : "memory"); }
template <class T> DI void tie(T& a) { asm volatile("" : "+v"(a)); }
template <int N, class T> DI void wvm(T& a) { asm volatile("s_waitcnt vmcnt(%c1)" : "+v"(a) : "i"(N) : "memory"); }
DI void wlg4(u32x4& a, u32x4& b, u32x4& c, u32x4& d) { asm volatile("s_waitcnt lgkmcnt(0)" : "+v"(a), "+v"(b), "+v"(c), "+v"(d)::"memory"); }
template <int N> DI void wvm4(u32x4& a, u32x4& b, u32x4& c, u32x4& d) {
  asm volatile("s_waitcnt vmcnt(%c4)" : "+v"(a), "+v"(b), "+v"(c), "+v"(d) : "i"(N) : "memory");
}
DI void bar() { asm volatile("s_waitcnt lgkmcnt(0)\n\ts_barrier" ::: "memory"); }
#define SB() __builtin_amdgcn_sched_barrier(0)
DI void halves(float v, float& lo, float& hi) {
  auto r = __builtin_amdgcn_permlane32_swap(f2u(v), f2u(v), false, false);
  lo = u2f(r[0]); hi = u2f(r[1]);
}
DI float swap_add(float v) { float a, b; halves(v, a, b); return a + b; }
DI float swap_max(float v) { float a, b; halves(v, a, b); return fmaxf(a, b); }
DI float swap_hi(float v) { float a, b; halves(v, a, b); return b; }
DI float gelu(float y) {
  const float c0 = -2.f * 0.7978845608028654f * LOG2E, c1 = c0 * 0.044715f;
  float u = y * fmaf(y * y, c1, c0);
  return y * __builtin_amdgcn_rcpf(1.f + __builtin_amdgcn_exp2f(u));
}
DI float dot2(unsigned a, unsigned b, float c) {
  return __builtin_amdgcn_fdot2_f32_bf16(__builtin_bit_cast(bf16x2, a), __builtin_bit_cast(bf16x2, b), c, false);
}
constexpr unsigned ONE2 = 0x3f803f80u;
DI f32x16 zero16() { f32x16 z; for (int i = 0; i < 16; ++i) z[i] = 0.f; return z; }
DI u32x4 pack8(const f32x16& a, int u) {
  return u32x4{pk2(a[8 * u], a[8 * u + 1]), pk2(a[8 * u + 2], a[8 * u + 3]), pk2(a[8 * u + 4], a[8 * u + 5]), pk2(a[8 * u + 6], a[8 * u + 7])};
}
DI void poll(GAS u64* fl, int idx, u64 tag) {
#pragma unroll 1
  for (unsigned spins = 0; spins < SPIN_CAP; ++spins) {
    const u64 v = __hip_atomic_load(fl + idx, __ATOMIC_RELAXED, __HIP_MEMORY_SCOPE_AGENT);
    if (__all(v == tag)) break;
    __builtin_amdgcn_s_sleep(1);
  }
  asm volatile("" ::: "memory");
}
DI u64 read_tag(const char* ws) {
  u64 e;
  asm volatile("s_load_dwordx2 %0, %1, 0x0\n\ts_waitcnt lgkmcnt(0)" : "=s"(e) : "s"(ws) : "memory");
  return e + 0x9E3779B97F4A7C15ull;
}
DI void publish(GAS u64* fl, int idx, u64 tag) {
  if (threadIdx.x == 0) __hip_atomic_store(fl + idx, tag, __ATOMIC_RELAXED, __HIP_MEMORY_SCOPE_AGENT);
}

#define ST(i) do {} while (0)

template <int NU, int TS, class Pre>
DI void half_gemm(f32x16& c0, f32x16& c1, unsigned a0, u32x4* xf, Pre&& pre) {
  const unsigned a1 = a0 ^ 64u;
  u32x4 wf[3][2];
  wf[0][0] = trfrag(a0, a0 + TS); wf[0][1] = trfrag(a1, a1 + TS);
  wf[1][0] = trfrag(a0 + 2048, a0 + 2048 + TS); wf[1][1] = trfrag(a1 + 2048, a1 + 2048 + TS);
  sfor<0, NU>([&](auto UU) {
    constexpr int U = UU.v;
    pre(UU);
    if constexpr (U + 2 < NU) wf[(U + 2) % 3][0] = trfrag(a0 + (U + 2) * 2048, a0 + (U + 2) * 2048 + TS);
    c0 = mfma(wf[U % 3][0], xf[U], c0);
    SB();
    if constexpr (U + 2 < NU) wf[(U + 2) % 3][1] = trfrag(a1 + (U + 2) * 2048, a1 + (U + 2) * 2048 + TS);
    c1 = mfma(wf[U % 3][1], xf[U], c1);
    SB();
  });
}
template <int NU> DI f32x16 colsum(unsigned a) {
  const u32x4 ones = {ONE2, ONE2, ONE2, ONE2};
  f32x16 c = zero16();
  sfor<0, NU>([&](auto UU) { c = mfma(trfrag(a + UU.v * 2048, a + UU.v * 2048 + 1024), ones, c); });
  return c;
}
DI void cs_write(unsigned dst, const f32x16& c, int lane) {
  if ((lane & 31) == 0) {
    const unsigned d = dst + (lane >> 5) * 16;
    sfor<0, 4>([&](auto A) { ldsw<f32x4>(d + A.v * 32, f32x4{c[4 * A.v], c[4 * A.v + 1], c[4 * A.v + 2], c[4 * A.v + 3]}); });
  }
}
DI void dual_softmax(const f32x16& p, bool hi, f32x16& ep, f32x16& em) {
  float mx = p[0], mn = p[0];
#pragma unroll
  for (int i = 1; i < 12; ++i) { mx = fmaxf(mx, p[i]); mn = fminf(mn, p[i]); }
  float mx1 = fmaxf(fmaxf(p[12], p[13]), fmaxf(p[14], p[15])), mn1 = fminf(fminf(p[12], p[13]), fminf(p[14], p[15]));
  mx = hi ? mx : fmaxf(mx, mx1);
  mn = hi ? mn : fminf(mn, mn1);
  mx = swap_max(mx) * LOG2E;
  mn = -swap_max(-mn) * LOG2E;
  float sp = 0.f, sm = 0.f;
#pragma unroll
  for (int i = 0; i < 16; ++i) {
    float a = __builtin_amdgcn_exp2f(fmaf(p[i], LOG2E, -mx)), b = __builtin_amdgcn_exp2f(fmaf(p[i], -LOG2E, mn));
    if (i >= 12) { a = hi ? 0.f : a; b = hi ? 0.f : b; }
    ep[i] = a; em[i] = b; sp += a; sm += b;
  }
  const float rp = __builtin_amdgcn_rcpf(swap_add(sp)), rm = __builtin_amdgcn_rcpf(swap_add(sm));
#pragma unroll
  for (int i = 0; i < 16; ++i) { ep[i] *= rp; em[i] *= rm; }
}
DI unsigned piece(int c, int t) { return (unsigned)((c >> 4) * 1024 + (t + 32 * ((c >> 2) & 1)) * 16 + ((c >> 3) & 1) * 8); }

constexpr int AW = 5;
constexpr int p4_younger(int s) {
  int n = 0;
  if (s < AW) n += 4 * (AW - 1 - s) + 4;
  for (int e = (s < AW ? 0 : s - AW + 1); e < s; ++e) n += (e + 10 <= 17 ? 4 : 0) + (e + AW <= 17 ? 4 : 0) + (e == 4 ? 1 : 0);
  if (s >= AW && s - AW == 4) n += 1;
  return n;
}

__global__ __launch_bounds__(256, 1) void fused(Params p) {
  extern __shared__ __attribute__((aligned(16))) char smem[];
  ST(0);
  const int tid = threadIdx.x, lane = tid & 63, t32 = lane & 31, h = lane >> 5;
  const int w = __builtin_amdgcn_readfirstlane(tid >> 6);
  const int b = blockIdx.x, xc = b & 7, r = b >> 3;
  const bool mainwg = r < 28;
  const srd_t sWS = mk(p.ws, WS_USED);
  GAS u64* fl = (GAS u64*)(p.ws + OFF_FLG);
  const int rr = lane >> 3, cp = lane & 7;
  const unsigned wsw = (unsigned)((cp ^ (((rr >> 1) & 1) << 2)) << 4);
  const unsigned wv896 = rr * 896 + wsw;
  {
    u64 pc;
    asm volatile("s_getpc_b64 %0" : "=s"(pc));
    const srd_t sC = mk((const void*)(pc & ~127ull), CODE_BYTES);
    asm volatile("s_mov_b32 m0, %3\n\ts_nop 0\n\tbuffer_load_dword %0, %1, %2 offen lds" ::"v"(lane * 128), "s"(sC),
                 "s"(((r * 4 + w) % ((CODE_BYTES + 8191) / 8192)) * 8192), "s"(R1 + w * 1024) : "memory");
  }
  const int q = (lane & 15) >> 2, pp = lane & 3, blk = (lane >> 4) & 1;
  const unsigned acol = (unsigned)((((2 * blk + (pp >> 1)) ^ ((q >> 1) << 2)) << 4) + (pp & 1) * 8);
  const unsigned aperm = (4 * h + q) * 128 + acol, anat = (8 * h + q) * 128 + acol;
  const int tt = 4 * xc + r / 7, s7 = r % 7;
  const int g4 = 4 * (xc >> 1) + r / 7;
  const int th = w & 1, kh = w >> 1;
  const int ftok = tt * 64 + th * 32 + t32;
  const int kg = 2 * xc + (r >> 4), kh16 = r & 15;
  u64 tag = 0;
  u32x2 xv[4];
  u32x4 x1p[2];
  u32x4 xf[28];
  const srd_t sX = mk(p.x, 2048 * 896);

  if (mainwg) {
    const bool wtype = (xc & 1) == 0;
    const srd_t sQ = mk(p.wqkv, 448 * 2688);
    const srd_t sA = wtype ? mk(p.w1, 448 * 896) : sQ;
    const unsigned pitchA = wtype ? 896 : 2688;
    const unsigned colA = wtype ? s7 : 2 * s7, colB = wtype ? 14 + s7 : 2 * s7 + 1;
    const unsigned wvA = rr * pitchA + wsw, wvB = rr * 2688 + wsw;
    const unsigned xv0 = rr * 896 + ((cp ^ (rr >> 1)) << 4), xv1 = xv0 ^ 64u;
    const unsigned xs = (g4 * 128 + w * 32) * 896, ring = R2 + w * 12288;
    auto XC = [&](auto C) {
      sfor<0, 4>([&](auto I) { dma16((I.v & 1) ? xv1 : xv0, sX, xs + I.v * 8 * 896 + C.v * 128, ring + (C.v % 3) * 4096 + I.v * 1024); });
    };
    auto WB = [&](int k) { dma16(wvB, sQ, (4 * k + w) * 8 * 2688 + colB * 128, R1 + (4 * k + w) * 1024); };
    XC(IC<0>{}); XC(IC<1>{});
    sfor<0, 14>([&](auto K) { dma16(wvA, sA, (4 * K.v + w) * 8 * pitchA + colA * 128, R0 + (4 * K.v + w) * 1024); });
    XC(IC<2>{});
    tag = read_tag(p.ws);
    wvm<4>();
    bar();
    ST(1);
    f32x16 csp = colsum<14>(R0 + (aperm ^ (th * 64)) + kh * 14 * 2048);
    f32x16 c0 = zero16(), c1 = zero16();
    float sm = 0.f, sq = 0.f;
    const unsigned xr = ring + t32 * 128 + ((h ^ ((t32 >> 1) & 7)) << 4);
    half_gemm<28, 512>(c0, c1, R0 + anat, xf, [&](auto UU) {
      constexpr int U = UU.v, C = U / 4;
      if constexpr (U % 4 == 0) {
        if constexpr (C >= 2) wvm<(C <= 4 ? 8 : C == 5 ? 9 : 10)>();
        sfor<0, 4>([&](auto I) { xf[U + I.v] = ldsr<u32x4>((xr ^ (I.v << 5)) + (C % 3) * 4096); });
        wlg4(xf[U], xf[U + 1], xf[U + 2], xf[U + 3]);
        if constexpr (C + 3 <= 6) XC(IC<C + 3>{});
        else sfor<5 * (C - 4), (C == 6 ? 14 : 5 * (C - 3))>([&](auto K) { WB(K.v); });
      }
      sfor<0, 4>([&](auto I) { sm = dot2(xf[U][I.v], ONE2, sm); sq = dot2(xf[U][I.v], xf[U][I.v], sq); });
      tie(sm); tie(sq);
    });
    sfor<0, 4>([&](auto A) { ld8p<A.v * 16>(xv[A.v], ftok * 896 + h * 8, sX, s7 * 128 + kh * 64); });
    cs_write(ring, csp, lane);
    const float mean = swap_add(sm) * (1.f / D);
    const float var = fmaxf(swap_add(sq) * (1.f / D) - mean * mean, 0.f);
    const float r1 = __builtin_amdgcn_rsqf(var + 1e-5f);
    const float r12 = r1 * __builtin_amdgcn_rsqf(var * r1 * r1 + 1e-5f);
    bar();
    ST(2);
    const unsigned T32 = 4 * g4 + w;
    auto epi = [&](f32x16& c, unsigned cs, float sc, bool act) {
      sfor<0, 4>([&](auto A) {
        const f32x4 s = ldsr<f32x4>(cs + A.v * 32 + h * 16) + ldsr<f32x4>(cs + 2 * 12288 + A.v * 32 + h * 16);
        sfor<0, 4>([&](auto I) {
          const float y = (c[4 * A.v + I.v] - mean * s[I.v]) * sc;
          c[4 * A.v + I.v] = act ? gelu(y) : y;
        });
      });
    };
    {
      const unsigned so = wtype ? OFF_H + (T32 * 28 + 4 * colA) * 1024 : OFF_QKV + (T32 * 84 + 4 * colA) * 1024;
      if (wtype) { epi(c0, R2, r12, true); epi(c1, R2 + 12288, r12, true); }
      else { epi(c0, R2, r1, false); epi(c1, R2 + 12288, r1, false); }
      st16s(pack8(c0, 0), sWS, lane * 16, so); st16s<1024>(pack8(c0, 1), sWS, lane * 16, so);
      st16s<2048>(pack8(c1, 0), sWS, lane * 16, so); st16s<3072>(pack8(c1, 1), sWS, lane * 16, so);
      st8s(u32x2{f2u(mean), f2u(r1)}, sWS,
           (wtype && s7 == 0 && h == 0) ? (g4 * 128 + w * 32 + t32) * 8 : 0x80000000u, OFF_ST0);
    }
    wvm<5>();
    bar();
    ST(3);
    csp = colsum<14>(R1 + (aperm ^ (th * 64)) + kh * 14 * 2048);
    c0 = zero16(); c1 = zero16();
    const srd_t sW2 = mk(p.w2, 448 * 896);
    half_gemm<28, 512>(c0, c1, R1 + anat, xf, [&](auto UU) {
      constexpr int U = UU.v;
      if constexpr (U == 6) { wvm<0>(); bar(); if (wtype) publish(fl, FL_H + g4 * 32 + s7, tag); }
      if constexpr (U >= 7 && U < 21) dma16(wv896, sW2, (4 * (U - 7) + w) * 8 * 896 + s7 * 128, R0 + (4 * (U - 7) + w) * 1024);
    });
    cs_write(ring + 128, csp, lane);
    bar();
    ST(4);
    {
      const unsigned so = OFF_QKV + (T32 * 84 + 4 * colB) * 1024;
      epi(c0, R2 + 128, r1, false); epi(c1, R2 + 12288 + 128, r1, false);
      st16s(pack8(c0, 0), sWS, lane * 16, so); st16s<1024>(pack8(c0, 1), sWS, lane * 16, so);
      st16s<2048>(pack8(c1, 0), sWS, lane * 16, so); st16s<3072>(pack8(c1, 1), sWS, lane * 16, so);
    }
    wvm<0>();
    bar();
    publish(fl, FL_Q + g4 * 32 + (xc & 1) * 7 + s7, tag);
    ST(5);
    if (w == 0) {
      const int gq = tt >> 1, gn = (tt & 1) ? min(gq + 1, 15) : max(gq - 1, 0);
      poll(fl, FL_H + ((lane & 8) ? gn : gq) * 32 + min(lane & 7, 6), tag);
    }
    bar();
    ST(6);
    const srd_t sH = mk(p.ws + OFF_H, 64 * 28 * 1024);
    const srd_t sW3 = mk(p.w3, 448 * 3584);
    const unsigned wv3 = rr * 3584 + wsw, col3 = (7 * (xc & 1) + s7) * 256;
    u32x2 st0;
    {
      const int ta = ftok - 1 - kh, tb = ftok + 1 + kh;
      const unsigned oa = (unsigned)((ta >> 5) * 28 * 1024 + ((ta & 31) + 32 * h) * 16), ob = (unsigned)((tb >> 5) * 28 * 1024 + ((tb & 31) + 32 * h) * 16);
      sfor<0, 14>([&](auto U) { ld16s<(U.v & 3) * 1024>(xf[U.v], U.v < 7 ? oa : ob, sH, (kh * 14 + (U.v & ~3)) * 1024); });
      ld8s<0>(st0, ftok * 8, sWS, OFF_ST0);
    }
    c0 = zero16(); c1 = zero16();
    half_gemm<14, 1024>(c0, c1, R0 + aperm + kh * 14 * 2048, xf, [&](auto UU) {
      constexpr int U = UU.v;
      wvm<14>(xf[U]);
      dma16(wv3, sW3, (4 * U + w) * 8 * 3584 + col3, R1 + (4 * U + w) * 1024);
    });
    {
      const f32x16 snd = kh ? c0 : c1;
      sfor<0, 4>([&](auto A) { ldsw<f32x4>(SCR + w * 4096 + A.v * 1024 + lane * 16, f32x4{snd[4 * A.v], snd[4 * A.v + 1], snd[4 * A.v + 2], snd[4 * A.v + 3]}); });
    }
    bar();
    {
      f32x16 own = kh ? c1 : c0;
      wvm<14>(st0);
      sfor<0, 4>([&](auto A) { tie(xv[A.v]); });
      const float m0 = u2f(st0.x), r0 = u2f(st0.y);
      sfor<0, 4>([&](auto A) {
        const f32x4 o = ldsr<f32x4>(SCR + (w ^ 2) * 4096 + A.v * 1024 + lane * 16);
        own[4 * A.v] += o[0] + (bflo(xv[A.v].x) - m0) * r0;
        own[4 * A.v + 1] += o[1] + (bfhi(xv[A.v].x) - m0) * r0;
        own[4 * A.v + 2] += o[2] + (bflo(xv[A.v].y) - m0) * r0;
        own[4 * A.v + 3] += o[3] + (bfhi(xv[A.v].y) - m0) * r0;
      });
      x1p[0] = pack8(own, 0); x1p[1] = pack8(own, 1);
      const unsigned so = OFF_X1 + ((2 * tt + th) * 28 + 4 * s7 + 2 * kh) * 1024;
      st16s(x1p[0], sWS, lane * 16, so); st16s<1024>(x1p[1], sWS, lane * 16, so);
      float s1 = 0.f, s2 = 0.f;
      sfor<0, 8>([&](auto I) { s1 = dot2(x1p[I.v >> 2][I.v & 3], ONE2, s1); s2 = dot2(x1p[I.v >> 2][I.v & 3], x1p[I.v >> 2][I.v & 3], s2); });
      s1 = swap_add(s1); s2 = swap_add(s2);
      st8s(u32x2{f2u(s1), f2u(s2)}, sWS, h == 0 ? ftok * 8 : 0x80000000u,
           OFF_ST1 + (2 * s7 + kh) * 2048 * 8);
    }
    wvm<0>();
    bar();
    publish(fl, FL_X + (tt >> 1) * 32 + (tt & 1) * 7 + s7, tag);
    ST(7);
  } else {
    tag = read_tag(p.ws);
  }

  poll(fl, FL_Q + kg * 32 + min(lane & 15, 13), tag);
  const unsigned qkvs = OFF_QKV + (4 * kg + w) * 84 * 1024;
  u32x4 wp[2];
  {
    const srd_t sP = mk(p.wphi, 16 * 1568);
    if (w < 2) dma16(lane * 16, sP, kh16 * 1568 + w * 1024, R2 + 24576 + w * 1024);
    u32x2 kp[4], vp[4];
    sfor<0, 4>([&](auto I) {
      const int d0 = min(8 * I.v + 4 * h, 24);
      ld8s<0>(kp[I.v], piece(448 + 28 * kh16 + d0, t32), sWS, qkvs);
      ld8s<0>(vp[I.v], piece(896 + 28 * kh16 + d0, t32), sWS, qkvs);
    });
    if (mainwg) {
      const srd_t sW3 = mk(p.w3, 448 * 3584);
      const unsigned wv3 = rr * 3584 + wsw, col3 = (7 * (xc & 1) + s7) * 256;
      sfor<0, 14>([&](auto K) { dma16(wv3, sW3, (4 * K.v + w) * 8 * 3584 + col3 + 128, R0 + (4 * K.v + w) * 1024); });
      wvm<14>();
    } else {
      wvm<0>();
    }
    sfor<0, 4>([&](auto I) { tie(kp[I.v]); tie(vp[I.v]); });
    bar();
    const unsigned pa = R2 + 24576 + (4 * h + q) * 56 + (16 * blk + 4 * pp) * 2;
    wp[0] = trfrag(pa, pa + 8 * 56);
    wp[1] = trfrag(pa + 16 * 56, pa + 24 * 56);
    if (h) { kp[3] = u32x2{0u, 0u}; vp[3] = u32x2{0x3f80u, 0u}; }
    f32x16 pk = mfma(wp[0], u32x4{kp[0].x, kp[0].y, kp[1].x, kp[1].y}, zero16());
    pk = mfma(wp[1], u32x4{kp[2].x, kp[2].y, kp[3].x, kp[3].y}, pk);
    f32x16 ep, em;
    dual_softmax(pk, h != 0, ep, em);
    const int row = w * 32 + t32;
    const unsigned pr = R2 + row * 128 + h * 8, sw = ((row >> 1) & 1) << 2;
    sfor<0, 4>([&](auto A) {
      ldsw<u32x2>(pr + ((A.v ^ sw) << 4), u32x2{pk2(ep[4 * A.v], ep[4 * A.v + 1]), pk2(ep[4 * A.v + 2], ep[4 * A.v + 3])});
      ldsw<u32x2>(pr + (((4 + A.v) ^ sw) << 4), u32x2{pk2(em[4 * A.v], em[4 * A.v + 1]), pk2(em[4 * A.v + 2], em[4 * A.v + 3])});
      ldsw<u32x2>(R2 + 16384 + row * 64 + A.v * 16 + h * 8, vp[A.v]);
    });
    bar();
    if (w < 2) {
      f32x16 kv = zero16();
      const unsigned aa = R2 + (anat ^ (w * 64)), ba = R2 + 16384 + (8 * h + q) * 64 + blk * 32 + pp * 8;
      sfor<0, 8>([&](auto U) { kv = mfma(trfrag(aa + U.v * 2048, aa + U.v * 2048 + 512), trfrag(ba + U.v * 1024, ba + U.v * 1024 + 256), kv); });
      asm volatile("s_nop 15" : "+v"(kv));
      const unsigned so = OFF_KVP + ((kg * 16 + kh16) * 2 + w) * 4096;
      sfor<0, 4>([&](auto A) {
        st16s<A.v * 1024>(__builtin_bit_cast(u32x4, f32x4{kv[4 * A.v], kv[4 * A.v + 1], kv[4 * A.v + 2], kv[4 * A.v + 3]}), sWS, lane * 16, so);
      });
    }
    wvm<0>();
    bar();
    publish(fl, FL_KV + kh16 * 32 + kg, tag);
  }
  ST(8);
  u32x2 qp[4];
  auto loadq = [&]() { sfor<0, 4>([&](auto I) { ld8s<0>(qp[I.v], piece(28 * kh16 + min(8 * I.v + 4 * h, 24), t32), sWS, qkvs); }); };
  const srd_t sW4 = mk(p.w4, 1792 * 896);
  const srd_t sWo = mk(p.wo, 448 * 896);
  auto slot = [&](int ww, int j) -> unsigned {
    return j < 2 ? R2 + (2 * ww + j) * 4096 : j < 5 ? R1 + (3 * ww + j - 2) * 4096 : j < 8 ? R0 + (3 * ww + j - 5) * 4096 : (ww < 2 ? R1 : R0) + (12 + (ww & 1)) * 4096;
  };
  auto W4 = [&](auto SS, auto II) {
    constexpr int s = SS.v, i = II.v;
    if constexpr (s < 14) dma16(wv896, sW4, (448 * w + 32 * s + 8 * i) * 896 + s7 * 128, slot(w, s % 9) + i * 1024);
    else dma16(wv896, sWo, (28 * (4 * w + s - 14) + 8 * i) * 896 + s7 * 128, slot(w, s % 9) + i * 1024);
  };

  if (mainwg) {
    cs_write(SCR + w * 128, colsum<28>((kh ? R0 : R1) + (aperm ^ (th * 64))), lane);
    if (w == 0) poll(fl, FL_X + g4 * 32 + min(lane & 15, 13), tag);
    bar();
    ST(9);
    const unsigned T32 = 4 * g4 + w;
    u32x2 sp[7];
    sfor<0, 7>([&](auto I) { ld8s<0>(sp[I.v], (T32 * 32 + t32) * 8 + h * 7 * 2048 * 8, sWS, OFF_ST1 + I.v * 2048 * 8); });
    constexpr int XW = 10;
    auto X1 = [&](auto U) { ld16s<(U.v & 3) * 1024>(xf[U.v], lane * 16, sWS, OFF_X1 + (T32 * 28 + (U.v & ~3)) * 1024); };
    sfor<0, XW>(X1);
    f32x16 c0 = zero16(), c1 = zero16(), c2 = zero16(), c3 = zero16();
    half_gemm<28, 1024>(c0, c1, R1 + aperm, xf, [&](auto UU) {
      constexpr int U = UU.v;
      wvm<(U <= 26 ? XW - 1 : XW - 2)>(xf[U]);
      if constexpr (U + XW < 28) X1(IC<U + XW>{});
      else if constexpr (U < 26) W4(IC<(U - 18) / 4>{}, IC<(U - 18) % 4>{});
    });
    sfor<0, 7>([&](auto I) { tie(sp[I.v]); });
    float s1 = 0.f, s2 = 0.f;
    sfor<0, 7>([&](auto I) { s1 += u2f(sp[I.v].x); s2 += u2f(sp[I.v].y); });
    const float mean = swap_add(s1) * (1.f / D);
    const float rs = __builtin_amdgcn_rsqf(fmaxf(swap_add(s2) * (1.f / D) - mean * mean, 0.f) + 1e-5f);
    bar();
    ST(10);
    auto epi3 = [&](f32x16& c, int tile) {
      sfor<0, 4>([&](auto A) {
        const f32x4 s = ldsr<f32x4>(SCR + tile * 128 + A.v * 32 + h * 16);
        sfor<0, 4>([&](auto I) { c[4 * A.v + I.v] = gelu((c[4 * A.v + I.v] - mean * s[I.v]) * rs); });
      });
      const unsigned so = OFF_H3 + (T32 * 112 + 8 * (7 * (xc & 1) + s7) + 2 * tile) * 1024;
      st16s(pack8(c, 0), sWS, lane * 16, so); st16s<1024>(pack8(c, 1), sWS, lane * 16, so);
    };
    half_gemm<28, 1024>(c2, c3, R0 + aperm, xf, [&](auto UU) {
      constexpr int U = UU.v;
      if constexpr (U < 24 && (U & 1) == 0) W4(IC<2 + U / 8>{}, IC<(U / 2) % 4>{});
    });
    loadq();
    bar();
    ST(11);
    epi3(c0, 0); epi3(c1, 1); epi3(c2, 2); epi3(c3, 3);
    wvm<0>();
    bar();
    publish(fl, FL_3 + g4 * 32 + 7 * (xc & 1) + s7, tag);
    ST(12);
    sfor<5, 9>([&](auto SS) { sfor<0, 4>([&](auto I) { W4(SS, I); }); });
  } else {
    const int sidx = xc * 4 + (r - 28), hd = sidx >> 1, sg = sidx & 1;
    loadq();
    poll(fl, FL_KV + hd * 32 + (lane & 15), tag);
    u32x4 part[16];
    sfor<0, 16>([&](auto G) { ld16s<0>(part[G.v], lane * 16, sWS, OFF_KVP + ((G.v * 16 + hd) * 2 + sg) * 4096 + w * 1024); });
    wvm<0>();
    f32x4 s = {0.f, 0.f, 0.f, 0.f};
    sfor<0, 16>([&](auto G) { tie(part[G.v]); s += __builtin_bit_cast(f32x4, part[G.v]); });
    st8s(u32x2{pk2(s[0], s[1]), pk2(s[2], s[3])}, sWS, lane * 16 + (w & 1) * 8, OFF_KV + ((hd * 2 + sg) * 2 + (w >> 1)) * 1024);
    wvm<0>();
    bar();
    publish(fl, FL_KR + hd * 32 + sg, tag);
    if (sidx == 0 && w == 0) {
      sfor<0, 4>([&](auto I) { poll(fl, FL_KV + (4 * I.v + (lane >> 4)) * 32 + (lane & 15), tag); });
      publish(fl, FL_E, tag);
    }
  }

  {
    sfor<0, 4>([&](auto I) { tie(qp[I.v]); });
    if (h) qp[3] = u32x2{0u, 0u};
    f32x16 pq = mfma(wp[0], u32x4{qp[0].x, qp[0].y, qp[1].x, qp[1].y}, zero16());
    pq = mfma(wp[1], u32x4{qp[2].x, qp[2].y, qp[3].x, qp[3].y}, pq);
    f32x16 ep, em;
    dual_softmax(pq, h != 0, ep, em);
    poll(fl, FL_KR + kh16 * 32 + (lane & 1), tag);
    u32x4 kvf[4];
    sfor<0, 4>([&](auto I) { ld16s<I.v * 1024>(kvf[I.v], lane * 16, sWS, OFF_KV + kh16 * 4096); });
    wvm<0>();
    sfor<0, 4>([&](auto I) { tie(kvf[I.v]); });
    f32x16 n = mfma(kvf[0], pack8(ep, 0), zero16());
    n = mfma(kvf[1], pack8(ep, 1), n);
    n = mfma(kvf[2], pack8(em, 0), n);
    n = mfma(kvf[3], pack8(em, 1), n);
    const float rd = __builtin_amdgcn_rcpf(swap_hi(n[12]) + 1e-6f);
#pragma unroll
    for (int i = 0; i < 16; ++i) n[i] *= rd;
    u32x4 o1 = pack8(n, 1);
    if (h) { o1[2] = 0u; o1[3] = 0u; }
    const unsigned so = OFF_O + ((4 * kg + w) * 32 + 2 * kh16) * 1024;
    st16s(pack8(n, 0), sWS, lane * 16, so); st16s<1024>(o1, sWS, lane * 16, so);
    wvm<0>();
    bar();
    publish(fl, FL_O + kg * 32 + kh16, tag);
  }
  ST(13);

  if (mainwg) {
    poll(fl, FL_3 + (tt >> 1) * 32 + (7 * w >> 1) + (lane & 3), tag);
    ST(14);
    u32x4 af[AW][4];
    auto ACT = [&](auto SS) {
      constexpr int s = SS.v;
      sfor<0, 2>([&](auto M) {
        const unsigned so = s < 14 ? OFF_H3 + ((2 * tt + M.v) * 112 + 28 * w + 2 * s) * 1024 : OFF_O + ((2 * tt + M.v) * 32 + 8 * w + 2 * (s - 14)) * 1024;
        ld16s<0>(af[s % AW][2 * M.v], lane * 16, sWS, so);
        ld16s<1024>(af[s % AW][2 * M.v + 1], lane * 16, sWS, so);
      });
    };
    sfor<0, AW>(ACT);
    const unsigned b2 = slot(w, 0) + aperm, b1 = slot(w, 2) + aperm, b0 = slot(w, 5) + aperm, b8 = slot(w, 8) + aperm;
    u32x4 wf[2][4];
    auto RW = [&](auto SS) {
      constexpr int j = SS.v % 9;
      const unsigned a = j < 2 ? b2 + j * 4096 : j < 5 ? b1 + (j - 2) * 4096 : j < 8 ? b0 + (j - 5) * 4096 : b8;
      sfor<0, 4>([&](auto I) {
        const unsigned aa = ((I.v & 1) ? a ^ 64u : a) + (I.v >> 1) * 2048;
        wf[SS.v & 1][I.v] = trfrag(aa, aa + 1024);
      });
    };
    auto RWDONE = [&](auto SS) {
      constexpr int k = SS.v & 1;
      wlg4(wf[k][0], wf[k][1], wf[k][2], wf[k][3]);
    };
    RW(IC<0>{}); RWDONE(IC<0>{});
    sfor<0, 4>([&](auto I) { W4(IC<9>{}, I); });
    f32x16 acc[4];
    sfor<0, 4>([&](auto I) { acc[I.v] = zero16(); });
    u32x2 ofl;
    const int own = kh * 2 + th;
    auto EX = [&](int j0) {
      sfor<0, 4>([&](auto T) {
        if (T.v != own) {
          const unsigned d = slot(w, j0 + (T.v > own ? T.v - 1 : T.v)) + lane * 16;
          sfor<0, 4>([&](auto A) { ldsw<f32x4>(d + A.v * 1024, f32x4{acc[T.v][4 * A.v], acc[T.v][4 * A.v + 1], acc[T.v][4 * A.v + 2], acc[T.v][4 * A.v + 3]}); });
        }
      });
    };
    f32x16 h2;
    sfor<0, 18>([&](auto SS) {
      constexpr int s = SS.v;
      wvm4<p4_younger(s)>(af[s % AW][0], af[s % AW][1], af[s % AW][2], af[s % AW][3]);
      if constexpr (s % 3 == 0) ST(16 + s / 3);
      if constexpr (s + 1 < 18) RW(IC<s + 1>{});
      SB();
      sfor<0, 8>([&](auto I) {
        constexpr int u = I.v >> 2, Nl = (I.v >> 1) & 1, mi = I.v & 1;
        acc[Nl * 2 + mi] = mfma(wf[s & 1][2 * u + Nl], af[s % AW][2 * mi + u], acc[Nl * 2 + mi]);
      });
      SB();
      if constexpr (s + 1 < 18) RWDONE(IC<s + 1>{});
      if constexpr (s + 10 <= 17) sfor<0, 4>([&](auto I) { W4(IC<s + 10>{}, I); });
      if constexpr (s + AW == 14) {
        wvm<28>(ofl);
        if (!__all((((u64)ofl.y << 32) | ofl.x) == tag)) poll(fl, FL_O + (tt >> 1) * 32 + 4 * w + (lane & 3), tag);
      }
      if constexpr (s + AW <= 17) ACT(IC<s + AW>{});
      if constexpr (s == 4) ld8s<0>(ofl, (FL_O + (tt >> 1) * 32 + 4 * w + (lane & 3)) * 8, sWS, OFF_FLG);
      if constexpr (s == 13) {
        EX(0);
        h2 = own == 0 ? acc[0] : own == 1 ? acc[1] : own == 2 ? acc[2] : acc[3];
        sfor<0, 4>([&](auto I) { acc[I.v] = zero16(); });
      }
    });
    ST(22);
    EX(3);
    f32x16 sv = own == 0 ? acc[0] : own == 1 ? acc[1] : own == 2 ? acc[2] : acc[3];
    bar();
    sfor<1, 4>([&](auto K) {
      const int src = (w + K.v) & 3, jj = own > src ? own - 1 : own;
      const unsigned d0 = slot(src, jj) + lane * 16, d1 = slot(src, 3 + jj) + lane * 16;
      sfor<0, 4>([&](auto A) {
        const f32x4 a = ldsr<f32x4>(d0 + A.v * 1024), c = ldsr<f32x4>(d1 + A.v * 1024);
        sfor<0, 4>([&](auto I) { h2[4 * A.v + I.v] += a[I.v]; sv[4 * A.v + I.v] += c[I.v]; });
      });
    });
    const srd_t sO = mk(p.out, 2048 * 896);
    sfor<0, 4>([&](auto A) {
      const unsigned a0 = x1p[A.v >> 1][(A.v & 1) * 2], a1 = x1p[A.v >> 1][(A.v & 1) * 2 + 1];
      const float bv[4] = {bflo(a0) + h2[4 * A.v], bfhi(a0) + h2[4 * A.v + 1], bflo(a1) + h2[4 * A.v + 2], bfhi(a1) + h2[4 * A.v + 3]};
      const float xx[4] = {bflo(xv[A.v].x), bfhi(xv[A.v].x), bflo(xv[A.v].y), bfhi(xv[A.v].y)};
      float o[4];
#pragma unroll
      for (int i = 0; i < 4; ++i) o[i] = fmaf(sv[4 * A.v + i], bv[i] * __builtin_amdgcn_rcpf(1.f + __builtin_amdgcn_exp2f(-LOG2E * bv[i])), xx[i]);
      __builtin_amdgcn_raw_buffer_store_b64(u32x2{pk2(o[0], o[1]), pk2(o[2], o[3])}, sO, ftok * 896 + h * 8, s7 * 128 + kh * 64 + A.v * 16, 0);
    });
  }
}

extern "C" void kernel_launch(void* const* d_in, const int* in_sizes, int n_in, void* d_out, int out_size, void* d_ws, size_t ws_size,
                              hipStream_t stream) {
  Params p;
  p.x = (const uint16_t*)d_in[0]; p.wqkv = (const uint16_t*)d_in[1]; p.wphi = (const uint16_t*)d_in[2]; p.wo = (const uint16_t*)d_in[3];
  p.w1 = (const uint16_t*)d_in[5]; p.w2 = (const uint16_t*)d_in[6]; p.w3 = (const uint16_t*)d_in[7]; p.w4 = (const uint16_t*)d_in[8];
  p.out = (uint16_t*)d_out; p.ws = (char*)d_ws;
  (void)hipFuncSetAttribute((const void*)fused, hipFuncAttributeMaxDynamicSharedMemorySize, LDS_BYTES);
  fused<<<256, 256, LDS_BYTES, stream>>>(p);
}
```

```cpp
#include <hip/hip_runtime.h>
#include <stdint.h>

#define LAS __attribute__((address_space(3)))
#define GAS __attribute__((address_space(1)))
#define DI __device__ __forceinline__
typedef short s16x4 __attribute__((ext_vector_type(4)));
typedef __bf16 bf16x8 __attribute__((ext_vector_type(8)));
typedef __bf16 bf16x2 __attribute__((ext_vector_type(2)));
typedef float f32x16 __attribute__((ext_vector_type(16)));
typedef float f32x4 __attribute__((ext_vector_type(4)));
typedef float f32x2 __attribute__((ext_vector_type(2)));
typedef unsigned u32x4 __attribute__((ext_vector_type(4)));
typedef unsigned u32x2 __attribute__((ext_vector_type(2)));
typedef unsigned long long u64;
typedef __amdgpu_buffer_rsrc_t srd_t;

constexpr int D = 448;
constexpr float LOG2E = 1.4426950408889634f;
constexpr unsigned OFF_FLG = 0;
constexpr unsigned OFF_H = 262144;
constexpr unsigned OFF_X1 = OFF_H + 64 * 28 * 1024;
constexpr unsigned OFF_QKV = OFF_X1 + 64 * 28 * 1024;
constexpr unsigned OFF_H3 = OFF_QKV + 64 * 84 * 1024;
constexpr unsigned OFF_O = OFF_H3 + 64 * 112 * 1024;
constexpr unsigned OFF_KVP = OFF_O + 64 * 32 * 1024;
constexpr unsigned OFF_ST0 = OFF_KVP + 32 * 8192;
constexpr unsigned OFF_ST1 = OFF_ST0 + 2048 * 16;
constexpr unsigned OFF_CS3 = OFF_ST1 + 14 * 2048 * 8;
constexpr unsigned WS_USED = OFF_CS3 + 1792 * 4;
constexpr size_t OFF_STAMP = 64u << 20;
constexpr int FL_E = 0, FL_A = 1, FL_Q = FL_A + 16 * 14, FL_X = FL_Q + 16 * 14, FL_3A = FL_X + 32 * 7, FL_3B = FL_3A + 16 * 14,
              FL_O = FL_3B + 16 * 14, FL_KV = FL_O + 256, FL_CS = FL_KV + 32;
static_assert((FL_CS + 28) * 128 <= 262144, "flag block");
constexpr unsigned RA = 0, R1 = 57344, R2 = 114688, LDS_BYTES = 163840;
constexpr unsigned OOB = 0x80000000u;
constexpr unsigned CODE_BYTES = 45056;
constexpr unsigned SPIN_CAP = 1u << 18;

struct Params {
  const uint16_t *x, *wqkv, *wphi, *wo, *w1, *w2, *w3, *w4;
  uint16_t* out;
  char* ws;
};

template <int I> struct IC { static constexpr int v = I; };
template <int B, int E, class F> DI void sfor(F&& f) {
  if constexpr (B < E) { f(IC<B>{}); sfor<B + 1, E>(f); }
}
DI f32x16 mfma(u32x4 a, u32x4 b, f32x16 c) {
  return __builtin_amdgcn_mfma_f32_32x32x16_bf16(__builtin_bit_cast(bf16x8, a), __builtin_bit_cast(bf16x8, b), c, 0, 0, 0);
}
DI unsigned pk2(float a, float b) {
  f32x2 v = {a, b};
  return __builtin_bit_cast(unsigned, __builtin_convertvector(v, bf16x2));
}
DI float u2f(unsigned u) { return __builtin_bit_cast(float, u); }
DI unsigned f2u(float f) { return __builtin_bit_cast(unsigned, f); }
DI float bflo(unsigned u) { return __builtin_bit_cast(float, u << 16); }
DI float bfhi(unsigned u) { return __builtin_bit_cast(float, u & 0xffff0000u); }
DI u32x4 trfrag(unsigned a0, unsigned a1) {
  u32x2 lo = __builtin_bit_cast(u32x2, __builtin_amdgcn_ds_read_tr16_b64_v4i16((LAS s16x4*)(uintptr_t)a0));
  u32x2 hi = __builtin_bit_cast(u32x2, __builtin_amdgcn_ds_read_tr16_b64_v4i16((LAS s16x4*)(uintptr_t)a1));
  return u32x4{lo.x, lo.y, hi.x, hi.y};
}
template <class T> DI T ldsr(unsigned a) { return *(LAS T*)(uintptr_t)a; }
template <class T> DI void ldsw(unsigned a, T v) { *(LAS T*)(uintptr_t)a = v; }
DI srd_t mk(const void* p, unsigned bytes) { return __builtin_amdgcn_make_buffer_rsrc((void*)p, 0, (int)bytes, 0x00020000); }
DI void dma16(unsigned voff, srd_t r, unsigned soff, unsigned lds) {
  asm volatile("s_mov_b32 m0, %3\n\ts_nop 0\n\tbuffer_load_dwordx4 %0, %1, %2 offen lds" ::"v"(voff), "s"(r), "s"(soff), "s"(lds) : "memory");
}
DI void dma16s(unsigned voff, srd_t r, unsigned soff, unsigned lds) {
  asm volatile("s_mov_b32 m0, %3\n\ts_nop 0\n\tbuffer_load_dwordx4 %0, %1, %2 offen sc1 lds" ::"v"(voff), "s"(r), "s"(soff), "s"(lds) : "memory");
}
DI void ld16p(u32x4& d, unsigned voff, srd_t r, unsigned soff) {
  asm volatile("buffer_load_dwordx4 %0, %1, %2, %3 offen" : "=v"(d) : "v"(voff), "s"(r), "s"(soff) : "memory");
}
template <int IMM> DI void ld16s(u32x4& d, unsigned voff, srd_t r, unsigned soff) {
  asm volatile("buffer_load_dwordx4 %0, %1, %2, %3 offen offset:%c4 sc1" : "=v"(d) : "v"(voff), "s"(r), "s"(soff), "i"(IMM) : "memory");
}
template <int IMM> DI void ld8s(u32x2& d, unsigned voff, srd_t r, unsigned soff) {
  asm volatile("buffer_load_dwordx2 %0, %1, %2, %3 offen offset:%c4 sc1" : "=v"(d) : "v"(voff), "s"(r), "s"(soff), "i"(IMM) : "memory");
}
template <int IMM = 0> DI void st16s(u32x4 v, srd_t r, unsigned voff, unsigned soff) {
  asm volatile("buffer_store_dwordx4 %0, %1, %2, %3 offen offset:%c4 sc1\n\ts_nop 1" ::"v"(v), "v"(voff), "s"(r), "s"(soff), "i"(IMM) : "memory");
}
DI void st8s(u32x2 v, srd_t r, unsigned voff, unsigned soff) {
  asm volatile("buffer_store_dwordx2 %0, %1, %2, %3 offen sc1" ::"v"(v), "v"(voff), "s"(r), "s"(soff) : "memory");
}
template <int N> DI void wvm() { asm volatile("s_waitcnt vmcnt(%c0)" ::"i"(N) : "memory"); }
template <class T> DI void tie(T& a) { asm volatile("" : "+v"(a)); }
template <int N, class T> DI void wvm(T& a) { asm volatile("s_waitcnt vmcnt(%c1)" : "+v"(a) : "i"(N) : "memory"); }
DI void wlg4(u32x4& a, u32x4& b, u32x4& c, u32x4& d) { asm volatile("s_waitcnt lgkmcnt(0)" : "+v"(a), "+v"(b), "+v"(c), "+v"(d)::"memory"); }
template <int N> DI void wvm4(u32x4& a, u32x4& b, u32x4& c, u32x4& d) {
  asm volatile("s_waitcnt vmcnt(%c4)" : "+v"(a), "+v"(b), "+v"(c), "+v"(d) : "i"(N) : "memory");
}
template <int N> DI void wvm2(u32x4& a, u32x4& b) { asm volatile("s_waitcnt vmcnt(%c2)" : "+v"(a), "+v"(b) : "i"(N) : "memory"); }
DI int fresh(int v) { asm volatile("" : "+s"(v)); return v; }
DI void bar() { asm volatile("s_waitcnt lgkmcnt(0)\n\ts_barrier" ::: "memory"); }
#define SB() __builtin_amdgcn_sched_barrier(0)
DI void halves(float v, float& lo, float& hi) {
  auto r = __builtin_amdgcn_permlane32_swap(f2u(v), f2u(v), false, false);
  lo = u2f(r[0]); hi = u2f(r[1]);
}
DI float swap_add(float v) { float a, b; halves(v, a, b); return a + b; }
DI float swap_max(float v) { float a, b; halves(v, a, b); return fmaxf(a, b); }
DI float swap_hi(float v) { float a, b; halves(v, a, b); return b; }
typedef f32x4 GeluK;
DI GeluK gelu_k(float r, float ir) {
  const float c0 = -2.f * 0.7978845608028654f * LOG2E, c1 = c0 * 0.044715f;
  return GeluK{c0 * r, c1 * r * r * r, ir, 0.f};
}
DI f32x2 gelu2(f32x2 z, GeluK k) {
  const f32x2 u = z * (z * z * k[1] + k[0]);
  const f32x2 e = {__builtin_amdgcn_exp2f(u[0]), __builtin_amdgcn_exp2f(u[1])};
  const f32x2 d = e * k[2] + k[2];
  return z * f32x2{__builtin_amdgcn_rcpf(d[0]), __builtin_amdgcn_rcpf(d[1])};
}
DI float dot2(unsigned a, unsigned b, float c) {
  return __builtin_amdgcn_fdot2_f32_bf16(__builtin_bit_cast(bf16x2, a), __builtin_bit_cast(bf16x2, b), c, false);
}
constexpr unsigned ONE2 = 0x3f803f80u;
DI f32x16 zero16() { f32x16 z; for (int i = 0; i < 16; ++i) z[i] = 0.f; return z; }
DI u32x4 pack8(const f32x16& a, int u) {
  return u32x4{pk2(a[8 * u], a[8 * u + 1]), pk2(a[8 * u + 2], a[8 * u + 3]), pk2(a[8 * u + 4], a[8 * u + 5]), pk2(a[8 * u + 6], a[8 * u + 7])};
}
DI void poll(GAS u64* fl, int idx, u64 tag) {
#pragma unroll 1
  for (unsigned spins = 0; spins < SPIN_CAP; ++spins) {
    const u64 v = __hip_atomic_load(fl + idx * 16, __ATOMIC_RELAXED, __HIP_MEMORY_SCOPE_AGENT);
    if (__all(v == tag)) break;
    __builtin_amdgcn_s_sleep(1);
  }
  asm volatile("" ::: "memory");
}
DI void publish(srd_t ws, u32x2 tg, unsigned idx, bool on) { st8s(tg, ws, on ? idx * 128 : OOB, OFF_FLG); }
DI bool all_set(u32x2 f, u32x2 tg) { return __all(f.x == tg.x && f.y == tg.y); }

#define ST(i) do {} while (0)

template <int NU> DI f32x16 colsum(unsigned a) {
  const u32x4 ones = {ONE2, ONE2, ONE2, ONE2};
  f32x16 c = zero16();
  sfor<0, NU>([&](auto UU) { c = mfma(trfrag(a + UU.v * 2048, a + UU.v * 2048 + 1024), ones, c); });
  return c;
}
DI void cs_write(unsigned dst, const f32x16& c, int lane) {
  if ((lane & 31) == 0) {
    const unsigned d = dst + (lane >> 5) * 16;
    sfor<0, 4>([&](auto A) { ldsw<f32x4>(d + A.v * 32, f32x4{c[4 * A.v], c[4 * A.v + 1], c[4 * A.v + 2], c[4 * A.v + 3]}); });
  }
}
DI float max3(float a, float b, float c) { float r; asm("v_max3_f32 %0, %1, %2, %3" : "=v"(r) : "v"(a), "v"(b), "v"(c)); return r; }
DI float min3(float a, float b, float c) { float r; asm("v_min3_f32 %0, %1, %2, %3" : "=v"(r) : "v"(a), "v"(b), "v"(c)); return r; }
DI void dual_softmax(f32x16 p, bool hi, u32x4 (&o)[4]) {
  p[0] += 0.f;
#pragma unroll
  for (int i = 12; i < 16; ++i) p[i] = hi ? p[0] : p[i];
  float mx = max3(p[0], p[1], p[2]), mn = min3(p[0], p[1], p[2]);
#pragma unroll
  for (int i = 3; i < 15; i += 2) { mx = max3(mx, p[i], p[i + 1]); mn = min3(mn, p[i], p[i + 1]); }
  float a, b;
  halves(max3(mx, p[15], p[15]), a, b);
  const float cp = -LOG2E * max3(a, b, b);
  halves(min3(mn, p[15], p[15]), a, b);
  const float cm = LOG2E * min3(a, b, b);
  f32x2 ep[8], em[8];
#pragma unroll
  for (int i = 0; i < 8; ++i) {
    const f32x2 v = {p[2 * i], p[2 * i + 1]}, x = v * LOG2E + cp, y = v * -LOG2E + cm;
    ep[i] = f32x2{__builtin_amdgcn_exp2f(x[0]), __builtin_amdgcn_exp2f(x[1])};
    em[i] = f32x2{__builtin_amdgcn_exp2f(y[0]), __builtin_amdgcn_exp2f(y[1])};
  }
  const float keep = hi ? 0.f : 1.f;
  const f32x2 sp = ((ep[0] + ep[1]) + (ep[2] + ep[3])) + ((ep[4] + ep[5]) + (ep[6] + ep[7]) * keep);
  const f32x2 sn = ((em[0] + em[1]) + (em[2] + em[3])) + ((em[4] + em[5]) + (em[6] + em[7]) * keep);
  const float rp = __builtin_amdgcn_rcpf(swap_add(sp[0] + sp[1])), rm = __builtin_amdgcn_rcpf(swap_add(sn[0] + sn[1]));
#pragma unroll
  for (int i = 0; i < 8; ++i) {
    const f32x2 x = ep[i] * (i < 6 ? rp : rp * keep), y = em[i] * (i < 6 ? rm : rm * keep);
    o[i >> 2][i & 3] = pk2(x[0], x[1]);
    o[2 + (i >> 2)][i & 3] = pk2(y[0], y[1]);
  }
}
DI void granule(u32x4& g, srd_t ws, unsigned voff, u32x2 tg) {
#pragma unroll 1
  for (unsigned spins = 0; spins < SPIN_CAP && !__all(g[1] == tg.x && g[3] == tg.y); ++spins) {
    ld16s<0>(g, voff, ws, OFF_ST0);
    wvm<0>(g);
  }
}
DI f32x16 cs_read(unsigned a, unsigned str) {
  f32x4 v[4][4];
  sfor<0, 4>([&](auto P) { sfor<0, 4>([&](auto A) { v[P.v][A.v] = ldsr<f32x4>(a + P.v * str + A.v * 32); }); });
  f32x16 r;
  sfor<0, 4>([&](auto A) {
    const f32x4 t = (v[0][A.v] + v[1][A.v]) + (v[2][A.v] + v[3][A.v]);
    sfor<0, 4>([&](auto I) { r[4 * A.v + I.v] = t[I.v]; });
  });
  return r;
}
DI f32x4 quad(const f32x16& c, int a) { return f32x4{c[4 * a], c[4 * a + 1], c[4 * a + 2], c[4 * a + 3]}; }
DI u32x4 cross(u32x4 v) {
  auto a = __builtin_amdgcn_permlane32_swap(v[0], v[2], false, false);
  auto b = __builtin_amdgcn_permlane32_swap(v[1], v[3], false, false);
  return u32x4{a[0], b[0], a[1], b[1]};
}

#define STARG
#define STPASS
DI void code_prefetch(u64 pc, int lane, int sel, unsigned lds) {
  const srd_t sC = mk((const void*)(pc & ~127ull), CODE_BYTES);
  asm volatile("s_mov_b32 m0, %3\n\ts_nop 0\n\tbuffer_load_dword %0, %1, %2 offen lds" ::"v"(lane * 128), "s"(sC), "s"(sel * 8192), "s"(lds) : "memory");
}
DI u64 make_tag(u32x2 ev) {
  return (((u64)(unsigned)__builtin_amdgcn_readfirstlane(ev.y) << 32) | (unsigned)__builtin_amdgcn_readfirstlane(ev.x)) + 0x9E3779B97F4A7C15ull;
}

struct SlotP1 { static constexpr unsigned b0 = R2 - 16384, b1 = b0; static constexpr unsigned off(int c) { return R2 - 16384 + ((c + 1) % 4) * 16384; } };
struct SlotP3 { static constexpr unsigned b0 = RA, b1 = R2; static constexpr unsigned off(int c) { return c < 3 ? RA + c * 16384 : c < 6 ? R2 + (c - 3) * 16384 : RA; } };
constexpr unsigned HREG = R2 + 4096;
constexpr unsigned XT = R2 - 8192;
constexpr int UH = 12;
constexpr int M3 = 20;
constexpr int AW = 3;
template <int TS, class Slot, bool CS, class Hook>
DI void pass_a(f32x16& c0, f32x16& c1, f32x16& s0, f32x16& s1, const unsigned (&ar)[8], const unsigned (&xa)[4], u32x4* xf, Hook&& hook) {
  unsigned xr[2][4];
  sfor<0, 4>([&](auto I) { xr[0][I.v] = xa[I.v] + Slot::b0; tie(xr[0][I.v]); xr[1][I.v] = xa[I.v] + Slot::b1; if constexpr (Slot::b0 != Slot::b1) tie(xr[1][I.v]); else xr[1][I.v] = xr[0][I.v]; });
  auto rx = [&](auto UU) { constexpr unsigned o = Slot::off(UU.v / 4); xf[UU.v] = ldsr<u32x4>(o >= Slot::b1 ? xr[1][UU.v & 3] + (o - Slot::b1) : xr[0][UU.v & 3] + (o - Slot::b0)); };
  const u32x4 ones = {ONE2, ONE2, ONE2, ONE2};
  u32x4 wf[3][2];
  auto rd = [&](auto UU, auto NN) {
    const unsigned ad = ar[4 * NN.v + (UU.v & 3)] + (UU.v >> 2) * 8192;
    wf[UU.v % 3][NN.v] = trfrag(ad, ad + TS);
  };
  sfor<0, 28>([&](auto UU) {
    constexpr int U = UU.v;
    if constexpr (U % 4 == 0) {
      bar();
      rx(UU);
      rd(UU, IC<0>{}); rd(UU, IC<1>{});
      rx(IC<U + 1>{});
      rd(IC<U + 1>{}, IC<0>{}); rd(IC<U + 1>{}, IC<1>{});
      SB();
    }
    if constexpr (U % 4 < 2) { rx(IC<U + 2>{}); rd(IC<U + 2>{}, IC<0>{}); }
    c0 = mfma(wf[U % 3][0], xf[U], c0);
    if constexpr (CS && U % 4 == 0) s0 = mfma(wf[U % 3][0], ones, s0);
    SB();
    if constexpr (U % 4 < 2) rd(IC<U + 2>{}, IC<1>{});
    c1 = mfma(wf[U % 3][1], xf[U], c1);
    if constexpr (CS && U % 4 == 0) s1 = mfma(wf[U % 3][1], ones, s1);
    hook(UU);
    SB();
  });
}
DI void cs_combine(unsigned base, unsigned priv, int lane, f32x16& s0, f32x16& s1) {
  const unsigned a = base + (lane >> 5) * 512 + (lane & 31) * 4;
  ldsw<float>(priv + lane * 4, (ldsr<float>(a) + ldsr<float>(a + 128)) + (ldsr<float>(a + 256) + ldsr<float>(a + 384)));
  sfor<0, 8>([&](auto A) {
    const f32x4 t = ldsr<f32x4>(priv + A.v * 32 + (lane >> 5) * 16);
    sfor<0, 4>([&](auto I) { (A.v < 4 ? s0 : s1)[4 * (A.v & 3) + I.v] = t[I.v]; });
  });
}
DI void arrive(unsigned& tk, unsigned ctr, int lane) {
  asm volatile("ds_add_rtn_u32 %0, %1, %2" : "=v"(tk) : "v"(lane == 0 ? ctr : (ctr & ~255u) + 256 + lane * 4), "v"(1u) : "memory");
}
template <int N> DI bool ticket(unsigned& tk, int lane) {
  asm volatile("s_waitcnt lgkmcnt(%c1)" : "+v"(tk) : "i"(N) : "memory");
  return lane == 0 && tk == 3;
}
DI void touch(unsigned voff, srd_t r, unsigned soff, unsigned lds) {
  asm volatile("s_mov_b32 m0, %3\n\ts_nop 0\n\tbuffer_load_dword %0, %1, %2 offen lds" ::"v"(voff), "s"(r), "s"(soff), "s"(lds) : "memory");
}
DI int rot7(int c, int rot) { const int v = c + rot; return v >= 7 ? v - 7 : v; }
struct Geo {
  int r7, s7, g4, tt, s14, rot;
  bool wtype;
  DI Geo(int xc, int r) {
    r7 = r / 7;
    s7 = r - 7 * r7;
    g4 = 4 * (xc >> 1) + r7;
    tt = 4 * xc + r7;
    s14 = 7 * (xc & 1) + s7;
    rot = (s7 + 2 * r7) % 7;
    wtype = (xc & 1) == 0;
  }
};
DI unsigned p4_slot(int kq, int j) {
  return j < 2 ? R2 + 16384 + (2 * kq + j) * 4096 : j < 5 ? R1 + (3 * kq + j - 2) * 4096 : j < 8 ? RA + (3 * kq + j - 5) * 4096 : (kq < 2 ? R1 : RA) + (12 + (kq & 1)) * 4096;
}
DI int p4_k32(int kq, int s, int rot) {
  const int a = 7 * kq + rot7(s < 7 ? s : s - 7, rot);
  return 4 * (a >> 1) + (a & 1) + (s < 7 ? 0 : 2);
}

DI void first_loads(const Params& p, const int w, const int xc, const int r) {
  const int lane = threadIdx.x & 63;
  const Geo g(xc, r);
  const int rr = lane >> 3, cp = lane & 7;
  const unsigned wsw = (unsigned)((cp ^ (((rr >> 1) & 1) << 2)) << 4);
  const srd_t sX = mk(p.x, 2048 * 896), sQ = mk(p.wqkv, 448 * 2688);
  const srd_t sA = g.wtype ? mk(p.w1, 448 * 896) : sQ;
  const unsigned pitchA = g.wtype ? 896 : 2688;
  const unsigned colA = g.wtype ? g.s7 : 7 + g.s7;
  const unsigned wvA = rr * pitchA + wsw;
  const unsigned xv0 = rr * 896 + ((cp ^ (rr >> 1)) << 4), xv1 = xv0 ^ 64u;
  const unsigned T32 = 4 * g.g4 + w, xs = T32 * 32 * 896;
  auto G1 = [&](int c) {
    const int cg = rot7(c, g.rot);
    sfor<0, 2>([&](auto J) { dma16(wvA, sA, (8 * cg + 2 * w + J.v) * 8 * pitchA + colA * 128, RA + (8 * c + 2 * w + J.v) * 1024); });
    sfor<0, 4>([&](auto I) { dma16((I.v & 1) ? xv1 : xv0, sX, xs + I.v * 8 * 896 + cg * 128, SlotP1::off(c) + w * 4096 + I.v * 1024); });
  };
  G1(0); G1(1);
}

DI void loader_path(const Params& p, const u64 pc, const int w, const int xc, const int r STARG) {
  const int lane = threadIdx.x & 63;
  const Geo g(xc, r);
  const int rr = lane >> 3, cp = lane & 7;
  const unsigned wsw = (unsigned)((cp ^ (((rr >> 1) & 1) << 2)) << 4);
  const srd_t sWS = mk(p.ws, WS_USED), sX = mk(p.x, 2048 * 896), sQ = mk(p.wqkv, 448 * 2688);
  const srd_t sA = g.wtype ? mk(p.w1, 448 * 896) : sQ;
  const unsigned pitchA = g.wtype ? 896 : 2688;
  const unsigned colA = g.wtype ? g.s7 : 7 + g.s7, colB = g.wtype ? g.s7 : 14 + g.s7;
  const unsigned wvA = rr * pitchA + wsw;
  const unsigned xv0 = rr * 896 + ((cp ^ (rr >> 1)) << 4), xv1 = xv0 ^ 64u;
  const unsigned T32 = 4 * g.g4 + w, xs = T32 * 32 * 896;
  auto G1 = [&](int c) {
    const int cg = rot7(c, g.rot);
    sfor<0, 2>([&](auto J) { dma16(wvA, sA, (8 * cg + 2 * w + J.v) * 8 * pitchA + colA * 128, RA + (8 * c + 2 * w + J.v) * 1024); });
    sfor<0, 4>([&](auto I) { dma16((I.v & 1) ? xv1 : xv0, sX, xs + I.v * 8 * 896 + cg * 128, SlotP1::off(c) + w * 4096 + I.v * 1024); });
  };
  u32x2 ev;
  ld8s<0>(ev, 0u, sWS, OFF_FLG);
  code_prefetch(pc, lane, (r & 1) * 4 + w, R1 + w * 1024);
  SB();
  ST(25);
  const unsigned wv896 = rr * 896 + wsw, wv3 = rr * 3584 + wsw, wvB = rr * 2688 + wsw;
  auto WB = [&](int k) { dma16(wvB, sQ, (8 * rot7(k >> 1, g.rot) + 4 * (k & 1) + w) * 8 * 2688 + colB * 128, R1 + (4 * k + w) * 1024); };
  wvm<8>(); bar(); G1(2);
  wvm<8>(); bar(); G1(3);
  wvm<6>(ev); bar(); G1(4);
  wvm<6>(); bar(); G1(5);
  wvm<6>(); bar(); G1(6);
  wvm<6>(); bar(); sfor<0, 2>([&](auto K) { WB(K.v); });
  wvm<2>(); bar(); sfor<2, 6>([&](auto K) { WB(K.v); });
  const u64 tag = make_tag(ev);
  GAS u64* fl = (GAS u64*)(p.ws + OFF_FLG);
  bar();
  ldsw<u32x2>(R2 + 3584, u32x2{0u, 0u});
  sfor<6, 14>([&](auto K) { WB(K.v); });
  const srd_t sW2 = mk(p.w2, 448 * 896);
  auto W2 = [&](int k) { const int pc = 8 * rot7(k >> 1, g.rot) + 4 * (k & 1) + w; dma16(wv896, sW2, pc * 8 * 896 + g.s7 * 128, RA + pc * 1024); };
  sfor<0, 2>([&](auto K) { W2(K.v); });
  wvm<2>(); bar();
  sfor<2, 14>([&](auto K) { W2(K.v); });
  const srd_t sW3 = mk(p.w3, 448 * 3584);
  touch((lane >> 1) < 28 ? (lane >> 1) * 3584 + (lane & 1) * 128 : OOB, sW3, (g.r7 * 112 + w * 28) * 3584 + g.s14 * 256, R2 + 3072);
  ST(4);
  {
    const int th = w & 1, kh = w >> 1, tc = 2 * g.tt + th;
    const int gsel = min(max(tc + ((lane & 4) ? 2 * th - 1 : 0), 0), 63) >> 2;
    poll(fl, FL_A + gsel * 14 + min(3 * kh + (lane & 3), 6), tag);
    ST(5);
    const srd_t sH = mk(p.ws + OFF_H, 64 * 28 * 1024);
    const int tok = tc * 32 + (lane & 31), ta = tok - 1 - kh, tb = tok + 1 + kh, hh = lane & 32;
    const unsigned oa = (unsigned)((ta >> 5) * 28 * 1024 + ((ta & 31) + hh) * 16), ob = (unsigned)((tb >> 5) * 28 * 1024 + ((tb & 31) + hh) * 16);
    sfor<0, 7>([&](auto U) { const int u = rot7(U.v, g.s7); dma16s(oa, sH, (kh * 14 + u) * 1024, HREG + w * 11264 + u * 1024); });
    sfor<0, 4>([&](auto U) { const int u = 7 + ((U.v + g.s7) & 3); dma16s(ob, sH, (kh * 14 + u) * 1024, HREG + w * 11264 + u * 1024); });
  }
  bar();
  sfor<0, 2>([&](auto J) { dma16(rr * 896 + ((cp ^ rr) << 4), sX, (g.tt * 64 + (2 * w + J.v) * 8) * 896 + g.s7 * 128, XT + (2 * w + J.v) * 1024); });
  wvm<0>();
  ST(6);
  bar();
  auto W3 = [&](int k, unsigned half, unsigned img) {
    dma16(wv3, sW3, (8 * rot7(k >> 1, g.rot) + 4 * (k & 1) + w) * 8 * 3584 + g.s14 * 256 + half * 128, img + (4 * k + w) * 1024);
  };
  sfor<0, 8>([&](auto K) { W3(K.v, 0, R1); });
  bar();
  sfor<8, 12>([&](auto K) { W3(K.v, 0, R1); });
  bar();
  sfor<12, 14>([&](auto K) { W3(K.v, 0, R1); });
  const srd_t sW4 = mk(p.w4, 1792 * 896), sWo = mk(p.wo, 448 * 896);
  poll(fl, (lane & 8) ? FL_CS + 2 * g.s14 + (lane & 1) : FL_X + (T32 >> 1) * 7 + min(lane & 7, 6), tag);
  ST(11);
  auto G3 = [&](int c) { sfor<0, 4>([&](auto I) { dma16s(lane * 16, sWS, OFF_X1 + (T32 * 28 + 4 * rot7(c, g.rot) + I.v) * 1024, SlotP3::off(c) + w * 4096 + I.v * 1024); }); };
  G3(0); G3(1);
  wvm<4>(); bar(); G3(2); G3(3);
  wvm<8>(); bar(); G3(4);
  wvm<8>(); bar(); G3(5);
  wvm<8>(); bar(); G3(6);
  wvm<8>(); bar();
  wvm<4>(); bar(); sfor<4, 6>([&](auto K) { W3(K.v, 1, RA); });
  wvm<2>(); bar(); sfor<6, 10>([&](auto K) { W3(K.v, 1, RA); });
  bar();
  ldsw<u32x2>(R2, u32x2{0u, 0u});
  sfor<10, 14>([&](auto K) { W3(K.v, 1, RA); });
  sfor<0, 4>([&](auto K) { W3(K.v, 1, RA); });
  auto W4 = [&](auto SS) {
    constexpr int s = SS.v;
    sfor<0, 4>([&](auto I) {
      if constexpr (s < 14) dma16(wv896, sW4, (32 * p4_k32(w, s, g.rot) + 8 * I.v) * 896 + g.s7 * 128, p4_slot(w, s % 9) + I.v * 1024);
      else dma16(wv896, sWo, (28 * (4 * w + ((s + g.rot) & 3)) + 8 * I.v) * 896 + g.s7 * 128, p4_slot(w, s % 9) + I.v * 1024);
    });
  };
  wvm<0>(); bar();
  sfor<0, 5>(W4);
  bar();
  W4(IC<5>{}); W4(IC<6>{});
  const unsigned f3 = (g.tt >> 1) * 14 + (7 * w >> 1) + (lane & 3);
  poll(fl, FL_3A + f3, tag);
  ST(16);
  bar();
  W4(IC<7>{});
  sfor<0, 18>([&](auto SS) {
    constexpr int s = SS.v;
    if constexpr (s + AW == 7) poll(fl, FL_3B + f3, tag);
    if constexpr (s + AW == 14) poll(fl, FL_O + (g.tt >> 1) * 16 + 4 * w + (lane & 3), tag);
    if constexpr (s >= 6 && s < 17) wvm<(s <= 9 ? 28 : 4 * (16 - s))>();
    bar();
    if constexpr (s == 0) W4(IC<8>{});
    if constexpr (s + 9 <= 17) W4(IC<s + 9>{});
  });
}

DI void compute_path(const Params& p, const int w, const int xc, const int r STARG) {
  const int tid = threadIdx.x, lane = tid & 63, t32 = lane & 31, h = lane >> 5, th = w & 1, kh = w >> 1;
  const Geo g(xc, r);
  const srd_t sWS = mk(p.ws, WS_USED);
  __builtin_amdgcn_s_setprio(3);
  u32x2 ev;
  ld8s<0>(ev, 0u, sWS, OFF_FLG);
  const unsigned T32 = 4 * g.g4 + w, tc = 2 * g.tt + th;
  const int ftok = tc * 32 + t32;
  u32x4 xv[2], x1p[2], gr;
  const int q = (lane & 15) >> 2, pp = lane & 3, blk = (lane >> 4) & 1;
  const unsigned acol = (unsigned)((((2 * blk + (pp >> 1)) ^ ((q >> 1) << 2)) << 4) + (pp & 1) * 8);
  const unsigned aperm = (4 * h + q) * 128 + acol, anat = (8 * h + q) * 128 + acol;
  const unsigned aown = aperm ^ (kh * 64);
  unsigned tk;
  u32x4 xf[28];
  unsigned ar[8], xr[4];
  sfor<0, 4>([&](auto I) {
    const unsigned u = (I.v + w) & 3;
    ar[I.v] = RA + anat + u * 2048; ar[4 + I.v] = ar[I.v] ^ 64u;
    xr[I.v] = w * 4096 + t32 * 128 + (((2 * u + h) ^ ((t32 >> 1) & 7)) << 4);
  });
  f32x16 c0 = zero16(), c1 = zero16(), c2 = zero16(), c3 = zero16();
  pass_a<512, SlotP1, true>(c0, c1, c2, c3, ar, xr, xf, [&](auto UU) {
    if constexpr (UU.v == 0) ST(1);
    if constexpr (UU.v == 22) ld16s<0>(gr, (T32 * 32 + t32) * 16, sWS, OFF_ST0);
  });
  cs_write(R2 + w * 128, c2, lane); cs_write(R2 + 512 + w * 128, c3, lane);
  bar();
  ST(2);
  cs_combine(R2, R2 + 2048 + w * 256, lane, c2, c3);
  wvm<0>(gr);
  tie(ev);
  const u64 tag = make_tag(ev);
  const u32x2 tg = {(unsigned)tag, (unsigned)(tag >> 32)};
  granule(gr, sWS, (T32 * 32 + t32) * 16, tg);
  const float mean1 = u2f(gr[0]), ve1 = u2f(gr[2]);
  const float r1 = __builtin_amdgcn_rsqf(ve1);
  const float v2 = (ve1 - 1e-5f) * r1 * r1 + 1e-5f;
  const GeluK gk1 = gelu_k(r1 * __builtin_amdgcn_rsqf(v2), __builtin_amdgcn_sqrtf(ve1 * v2));
  auto epi = [&](auto ACT, unsigned so) {
    unsigned pk[4];
    sfor<0, 16>([&](auto J) {
      constexpr int e = 2 * (J.v & 7);
      const f32x16 &c = J.v < 8 ? c0 : c1, &s = J.v < 8 ? c2 : c3;
      f32x2 z = f32x2{c[e], c[e + 1]} - f32x2{s[e], s[e + 1]} * mean1;
      if constexpr (ACT.v) z = gelu2(z, gk1); else z *= r1;
      pk[J.v & 3] = pk2(z[0], z[1]);
      if constexpr ((J.v & 3) == 3) st16s<(J.v >> 2) * 1024>(u32x4{pk[0], pk[1], pk[2], pk[3]}, sWS, lane * 16, so);
    });
  };
  if (g.wtype) epi(IC<1>{}, OFF_H + (T32 * 28 + 4 * g.s7) * 1024);
  else epi(IC<0>{}, OFF_QKV + (T32 * 84 + 28 + 4 * g.s7) * 1024);
  ld16s<0>(gr, ftok * 16, sWS, OFF_ST0);
  bar();
  ST(3);
  c0 = zero16(); c1 = zero16(); c2 = zero16(); c3 = zero16();
  {
    const u32x4 ones = {ONE2, ONE2, ONE2, ONE2};
    u32x4 wf[3][2];
    auto rd = [&](auto UU, auto NN) {
      const unsigned ad = ar[4 * NN.v + (UU.v & 3)] + (UU.v >> 2) * 8192;
      wf[UU.v % 3][NN.v] = trfrag(ad, ad + 512);
    };
    sfor<0, 8>([&](auto I) { ar[I.v] += R1; tie(ar[I.v]); });
    rd(IC<0>{}, IC<0>{}); rd(IC<0>{}, IC<1>{}); rd(IC<1>{}, IC<0>{}); rd(IC<1>{}, IC<1>{});
    sfor<0, 28>([&](auto UU) {
      constexpr int U = UU.v;
      c0 = mfma(wf[U % 3][0], xf[U], c0);
      if constexpr (U + 2 < 28) rd(IC<U + 2>{}, IC<0>{});
      if constexpr (U % 4 == 0) c2 = mfma(wf[U % 3][0], ones, c2);
      SB();
      c1 = mfma(wf[U % 3][1], xf[U], c1);
      if constexpr (U + 2 < 28) rd(IC<U + 2>{}, IC<1>{});
      if constexpr (U % 4 == 0) c3 = mfma(wf[U % 3][1], ones, c3);
      if constexpr (U == UH) { wvm<0>(gr); arrive(tk, R2 + 3584, lane); }
      if constexpr (U == UH + 1) publish(sWS, tg, FL_A + g.g4 * 14 + g.s14, ticket<4>(tk, lane));
      SB();
    });
  }
  cs_write(R2 + 1024 + w * 128, c2, lane); cs_write(R2 + 1536 + w * 128, c3, lane);
  ST(4);
  bar();
  {
    const srd_t sH = mk(p.ws + OFF_H, 64 * 28 * 1024);
    const int tb = ftok + 1 + kh;
    const unsigned ob = (unsigned)((tb >> 5) * 28 * 1024 + ((tb & 31) + 32 * h) * 16);
    sfor<11, 14>([&](auto U) { ld16s<(U.v - 11) * 1024>(xf[U.v], ob, sH, (kh * 14 + 11) * 1024); });
  }
  cs_combine(R2 + 1024, R2 + 2048 + w * 256, lane, c2, c3);
  epi(IC<0>{}, OFF_QKV + (T32 * 84 + (g.wtype ? 0 : 56) + 4 * g.s7) * 1024);
  ST(5);
  bar();
  ST(6);
  c0 = zero16(); c1 = zero16();
  {
    const unsigned a0 = RA + aown + kh * 14 * 2048, a1 = a0 ^ 64u, hb = HREG + w * 11264 + lane * 16;
    u32x4 wf[3][2];
    auto rd = [&](auto UU) {
      if constexpr (UU.v < 11) xf[UU.v] = ldsr<u32x4>(hb + UU.v * 1024);
      wf[UU.v % 3][0] = trfrag(a0 + UU.v * 2048, a0 + UU.v * 2048 + 1024); wf[UU.v % 3][1] = trfrag(a1 + UU.v * 2048, a1 + UU.v * 2048 + 1024);
    };
    rd(IC<0>{}); rd(IC<1>{});
    sfor<0, 14>([&](auto UU) {
      constexpr int U = UU.v;
      if constexpr (U >= 11) wvm<17 - U>(xf[U]);
      c0 = mfma(wf[U % 3][0], xf[U], c0);
      if constexpr (U + 2 < 14) rd(IC<U + 2>{});
      c1 = mfma(wf[U % 3][1], xf[U], c1);
      SB();
    });
    ST(26);
    sfor<0, 4>([&](auto A) { ldsw<f32x4>(hb + A.v * 1024, quad(c1, A.v)); });
  }
  bar();
  ST(27);
  {
    f32x4 o[4];
    sfor<0, 4>([&](auto A) { o[A.v] = ldsr<f32x4>(HREG + (w ^ 2) * 11264 + A.v * 1024 + lane * 16); });
    sfor<0, 4>([&](auto A) {
      const u32x2 t = ldsr<u32x2>(XT + (th * 32 + t32) * 128 + (((4 * kh + A.v) ^ (t32 & 7)) << 4) + h * 8);
      xv[A.v >> 1][2 * (A.v & 1)] = t.x; xv[A.v >> 1][2 * (A.v & 1) + 1] = t.y;
    });
    granule(gr, sWS, ftok * 16, tg);
    const float m0 = u2f(gr[0]), r0 = __builtin_amdgcn_rsqf(u2f(gr[2]));
    sfor<0, 4>([&](auto A) {
      const f32x4 t = quad(c0, A.v) + o[A.v];
      const unsigned y0 = xv[A.v >> 1][2 * (A.v & 1)], y1 = xv[A.v >> 1][2 * (A.v & 1) + 1];
      x1p[A.v >> 1][2 * (A.v & 1)] = pk2(t[0] + (bflo(y0) - m0) * r0, t[1] + (bfhi(y0) - m0) * r0);
      x1p[A.v >> 1][2 * (A.v & 1) + 1] = pk2(t[2] + (bflo(y1) - m0) * r0, t[3] + (bfhi(y1) - m0) * r0);
    });
    const unsigned so = OFF_X1 + (tc * 28 + 4 * g.s7 + 2 * kh) * 1024;
    st16s(x1p[0], sWS, lane * 16, so); st16s<1024>(x1p[1], sWS, lane * 16, so);
    float a1 = 0.f, a2 = 0.f;
    sfor<0, 8>([&](auto I) { a1 = dot2(x1p[I.v >> 2][I.v & 3], ONE2, a1); a2 = dot2(x1p[I.v >> 2][I.v & 3], x1p[I.v >> 2][I.v & 3], a2); });
    st8s(u32x2{f2u(swap_add(a1)), f2u(swap_add(a2))}, sWS, h == 0 ? ftok * 8 : OOB, OFF_ST1 + (2 * g.s7 + kh) * 2048 * 8);
  }
  ST(7);
  wvm<3>();
  arrive(tk, R2 + 3588, lane);
  publish(sWS, tg, FL_Q + g.g4 * 14 + g.s14, ticket<0>(tk, lane));
  wvm<1>();
  bar();
  publish(sWS, tg, FL_X + g.tt * 7 + g.s7, tid == 0);
  ST(8);
  sfor<0, 4>([&](auto I) {
    ar[I.v] = R1 + aperm + I.v * 2048; ar[4 + I.v] = R1 + (aperm ^ 64u) + I.v * 2048;
    xr[I.v] = w * 4096 + I.v * 1024 + lane * 16;
  });
  c0 = zero16(); c1 = zero16(); c2 = zero16(); c3 = zero16();
  u32x4 cs3[2][4];
  u32x2 sp[7];
  auto CS3 = [&](auto T) { sfor<0, 4>([&](auto A) { ld16s<A.v * 32>(cs3[T.v & 1][A.v], h * 16, sWS, OFF_CS3 + (g.s14 * 128 + T.v * 32) * 4); }); };
  pass_a<1024, SlotP3, false>(c0, c1, c2, c3, ar, xr, xf, [&](auto UU) {
    constexpr int U = UU.v;
    if constexpr (U == 0) ST(12);
    if constexpr (U >= 8 && U < 15) ld8s<0>(sp[U - 8], (T32 * 32 + t32 + h * 7 * 2048) * 8, sWS, OFF_ST1 + (U - 8) * 2048 * 8);
    if constexpr (U == 16) CS3(IC<0>{});
    if constexpr (U == 18) CS3(IC<1>{});
  });
  bar();
  ST(13);
  wvm4<4>(cs3[0][0], cs3[0][1], cs3[0][2], cs3[0][3]);
  float sm = 0.f, sq = 0.f;
  sfor<0, 7>([&](auto I) { tie(sp[I.v]); sm += u2f(sp[I.v].x); sq += u2f(sp[I.v].y); });
  const float mean3 = swap_add(sm) * (1.f / D);
  const float ve3 = fmaxf(swap_add(sq) * (1.f / D) - mean3 * mean3, 0.f) + 1e-5f;
  const GeluK gk3 = gelu_k(__builtin_amdgcn_rsqf(ve3), __builtin_amdgcn_sqrtf(ve3));
  const unsigned so3 = OFF_H3 + (T32 * 112 + 8 * g.s14) * 1024;
  unsigned pk[4];
  auto G = [&](auto JJ) {
    constexpr int j = JJ.v, t = j >> 3, e = 2 * (j & 7);
    const f32x16& c = t == 0 ? c0 : t == 1 ? c1 : t == 2 ? c2 : c3;
    const f32x2 z = gelu2(f32x2{c[e], c[e + 1]} - f32x2{u2f(cs3[t & 1][e >> 2][e & 3]), u2f(cs3[t & 1][e >> 2][(e & 3) + 1])} * mean3, gk3);
    pk[j & 3] = pk2(z[0], z[1]);
    if constexpr ((j & 3) == 3) st16s<(j >> 2 & 3) * 1024>(u32x4{pk[0], pk[1], pk[2], pk[3]}, sWS, lane * 16, so3 + (j >> 4) * 4096);
  };
  sfor<0, 8>([&](auto J) { G(J); });
  wvm4<2>(cs3[1][0], cs3[1][1], cs3[1][2], cs3[1][3]);
  CS3(IC<2>{});
  sfor<8, 16>([&](auto J) { G(J); });
  CS3(IC<3>{});
  bar();
  ST(24);
  {
    u32x4 wf[4];
    const unsigned a0 = RA + aperm, a1 = a0 ^ 64u;
    auto rd = [&](auto MM) { const unsigned ad = (MM.v < 28 ? a0 : a1) + (MM.v % 28) * 2048; wf[MM.v & 3] = trfrag(ad, ad + 1024); };
    rd(IC<0>{}); rd(IC<1>{}); rd(IC<2>{});
    sfor<0, 56>([&](auto MM) {
      constexpr int M = MM.v;
      if constexpr (M < 28) c2 = mfma(wf[M & 3], xf[M], c2); else c3 = mfma(wf[M & 3], xf[M - 28], c3);
      if constexpr (M + 3 < 56) rd(IC<M + 3>{});
      if constexpr (M == M3) {
        wvm4<0>(cs3[0][0], cs3[0][1], cs3[0][2], cs3[0][3]);
        sfor<0, 4>([&](auto A) { tie(cs3[1][A.v]); });
        arrive(tk, R2, lane);
      }
      if constexpr (M == M3 + 2) publish(sWS, tg, FL_3A + g.g4 * 14 + g.s14, ticket<4>(tk, lane));
      SB();
    });
  }
  bar();
  ST(14);
  sfor<16, 32>([&](auto J) { G(J); });
  ST(15);
  u32x4 af[AW][4];
  auto ACT = [&](auto SS) {
    constexpr int s = SS.v;
    sfor<0, 2>([&](auto M) {
      const unsigned tm = tc ^ M.v;
      const unsigned so = s < 14 ? OFF_H3 + (tm * 112 + 2 * p4_k32(w, s, g.rot)) * 1024 : OFF_O + (tm * 32 + 8 * w + 2 * ((s + g.rot) & 3)) * 1024;
      ld16s<0>(af[s % AW][2 * M.v], lane * 16, sWS, so);
      ld16s<1024>(af[s % AW][2 * M.v + 1], lane * 16, sWS, so);
    });
  };
  const unsigned b2 = p4_slot(w, 0) + aown, b1 = p4_slot(w, 2) + aown, b0 = p4_slot(w, 5) + aown, b8 = p4_slot(w, 8) + aown;
  u32x4 wf[2][4];
  auto RW = [&](auto SS, auto I) {
    constexpr int j = SS.v % 9;
    const unsigned a = j < 2 ? b2 + j * 4096 : j < 5 ? b1 + (j - 2) * 4096 : j < 8 ? b0 + (j - 5) * 4096 : b8;
    const unsigned aa = ((I.v & 1) ? a ^ 64u : a) + (I.v >> 1) * 2048;
    wf[SS.v & 1][I.v] = trfrag(aa, aa + 1024);
  };
  bar();
  sfor<0, AW>(ACT);
  wvm<4 * AW>();
  arrive(tk, R2 + 4, lane);
  publish(sWS, tg, FL_3B + g.g4 * 14 + g.s14, ticket<0>(tk, lane));
  ST(16);
  sfor<0, 4>([&](auto I) { RW(IC<0>{}, I); });
  c0 = zero16(); c1 = zero16(); c2 = zero16(); c3 = zero16();
  f32x16 d0 = zero16(), d1 = zero16(), d2 = zero16(), d3 = zero16();
  const unsigned exw = lane * 16 + w * 12288;
  sfor<0, 18>([&](auto SS) {
    constexpr int s = SS.v;
    bar();
    if constexpr (s % 3 == 0) ST(17 + s / 3);
    wvm4<(s + AW <= 18 ? 4 * (AW - 1) + (s < AW) : 4 * (17 - s))>(af[s % AW][0], af[s % AW][1], af[s % AW][2], af[s % AW][3]);
    f32x16 &a0 = s < 14 ? c0 : d0, &a1 = s < 14 ? c1 : d1, &a2 = s < 14 ? c2 : d2, &a3 = s < 14 ? c3 : d3;
    auto side = [&](auto KK) {
      constexpr int k = 4 * s + KK.v;
      if constexpr (k >= 57 && k < 63) sfor<0, 2>([&](auto J) { constexpr int A = 2 * (k - 57) + J.v; ldsw<f32x4>(R1 + exw + A * 1024, quad(A < 4 ? c1 : A < 8 ? c2 : c3, A & 3)); });
      if constexpr (k >= 64 && k < 67) {
        constexpr int P = k - 63;
        f32x16& t = P == 1 ? c1 : P == 2 ? c2 : c3;
        sfor<0, 4>([&](auto A) { const f32x4 v = ldsr<f32x4>(R1 + (w ^ P) * 12288 + (4 * (P - 1) + A.v) * 1024 + lane * 16); sfor<0, 4>([&](auto I) { t[4 * A.v + I.v] = v[I.v]; }); });
      }
      if constexpr (k >= 68) {
        sfor<0, 4>([&](auto I) {
          constexpr int e = 4 * (k - 68) + I.v;
          const unsigned xp = x1p[e >> 3][(e >> 1) & 3];
          float b = ((e & 1) ? bfhi(xp) : bflo(xp)) + c0[e];
          tie(b);
          b += c1[e]; tie(b); b += c2[e]; tie(b); b += c3[e];
          c0[e] = b * __builtin_amdgcn_rcpf(1.f + __builtin_amdgcn_exp2f(-LOG2E * b));
        });
      }
    };
    sfor<0, 2>([&](auto U) {
      a0 = mfma(wf[s & 1][2 * U.v], af[s % AW][U.v], a0);
      if constexpr (s + 1 < 18) RW(IC<s + 1>{}, IC<2 * U.v>{});
      a1 = mfma(wf[s & 1][2 * U.v], af[s % AW][2 + U.v], a1);
      side(IC<2 * U.v>{});
      SB();
      a2 = mfma(wf[s & 1][2 * U.v + 1], af[s % AW][U.v], a2);
      if constexpr (s + 1 < 18) RW(IC<s + 1>{}, IC<2 * U.v + 1>{});
      a3 = mfma(wf[s & 1][2 * U.v + 1], af[s % AW][2 + U.v], a3);
      side(IC<2 * U.v + 1>{});
      SB();
    });
    if constexpr (s + AW <= 17) ACT(IC<s + AW>{});
  });
  ST(23);
  sfor<0, 12>([&](auto A) { ldsw<f32x4>(RA + exw + A.v * 1024, quad(A.v < 4 ? d1 : A.v < 8 ? d2 : d3, A.v & 3)); });
  bar();
  sfor<0, 12>([&](auto A) {
    const f32x4 v = ldsr<f32x4>(RA + (w ^ (A.v / 4 + 1)) * 12288 + A.v * 1024 + lane * 16);
    sfor<0, 4>([&](auto I) { d0[4 * (A.v & 3) + I.v] += v[I.v]; });
  });
  const srd_t sO = mk(p.out, 2048 * 896);
  sfor<0, 2>([&](auto U) {
    u32x4 ov;
    sfor<0, 4>([&](auto I) {
      constexpr int e = 8 * U.v + 2 * I.v;
      ov[I.v] = pk2(fmaf(d0[e], c0[e], bflo(xv[U.v][I.v])), fmaf(d0[e + 1], c0[e + 1], bfhi(xv[U.v][I.v])));
    });
    __builtin_amdgcn_raw_buffer_store_b128(cross(ov), sO, ftok * 896 + h * 16 + g.s7 * 128 + kh * 64 + U.v * 32, 0, 0);
  });
}

DI void spare_path(const Params& p, const int w, const int xc, const int r STARG) {
  const int tid = threadIdx.x, lane = tid & 63, t32 = lane & 31, h = lane >> 5, tb = w & 3, ch = w >> 2;
  constexpr unsigned WPHI = 160768, ZERO = 159744, DUMMY = 158720;
  const int rr = lane >> 3, cp = lane & 7, rs = r - 28;
  const srd_t sWS = mk(p.ws, WS_USED), sX = mk(p.x, 2048 * 896);
  GAS u64* fl = (GAS u64*)(p.ws + OFF_FLG);
  const unsigned srow = 512 * (xc >> 1) + 64 * ((xc & 1) * 4 + rs) + 8 * w + rr;
  u32x2 ev;
  ld8s<0>(ev, 0u, sWS, OFF_FLG);
  u32x4 xr7[7];
  sfor<0, 7>([&](auto K) { ld16p(xr7[K.v], srow * 896 + cp * 16, sX, K.v * 128); });
  const int hd = 2 * xc + (rs >> 1), hf = rs & 1;
  const int bi = (xc >> 1) * 4 + rs, cb = 14 * (xc & 1) + bi;
  {
    const srd_t sW3 = mk(p.w3, 448 * 3584), sP = mk(p.wphi, 16 * 1568);
    const unsigned wv3 = rr * 3584 + ((cp ^ (((rr >> 1) & 1) << 2)) << 4);
    sfor<0, 7>([&](auto K) { dma16(wv3, sW3, (8 * K.v + w) * 8 * 3584 + min(cb, 27) * 128, RA + (8 * K.v + w) * 1024); });
    dma16(lane * 16, sP, hd * 1568 + (w & 1) * 1024, WPHI + (w & 1) * 1024);
  }
  wvm<8>(ev);
  const u64 tag = make_tag(ev);
  const u32x2 tg = {(unsigned)tag, (unsigned)(tag >> 32)};
  {
    float a1 = 0.f, a2 = 0.f;
    sfor<0, 7>([&](auto K) { tie(xr7[K.v]); sfor<0, 4>([&](auto I) { a1 = dot2(xr7[K.v][I.v], ONE2, a1); a2 = dot2(xr7[K.v][I.v], xr7[K.v][I.v], a2); }); });
    sfor<0, 3>([&](auto K) { a1 += __shfl_xor(a1, 1 << K.v); a2 += __shfl_xor(a2, 1 << K.v); });
    const float mean = a1 * (1.f / D), ve = fmaxf(a2 * (1.f / D) - mean * mean, 0.f) + 1e-5f;
    st16s(u32x4{f2u(mean), tg.x, f2u(ve), tg.y}, sWS, cp == 0 ? srow * 16 : OOB, OFF_ST0);
  }
  ST(1);
  const int q = (lane & 15) >> 2, pp = lane & 3, blk = (lane >> 4) & 1;
  const unsigned acol = (unsigned)((((2 * blk + (pp >> 1)) ^ ((q >> 1) << 2)) << 4) + (pp & 1) * 8);
  const unsigned aperm = (4 * h + q) * 128 + acol;
  wvm<1>();
  bar();
  cs_write(R1 + w * 128, colsum<7>(RA + (aperm ^ (ch * 64)) + tb * 7 * 2048), lane);
  if (tid < 16) ldsw<unsigned>(ZERO + tid * 4, 0u);
  bar();
  {
    const f32x16 cs = cs_read(R1 + ch * 512 + h * 16, 128);
    sfor<0, 4>([&](auto A) { st16s<A.v * 32>(__builtin_bit_cast(u32x4, quad(cs, A.v)), sWS, bi < 14 && tb == 0 && t32 == 0 ? h * 16 : OOB, OFF_CS3 + (cb * 64 + ch * 32) * 4); });
  }
  wvm<0>();
  bar();
  publish(sWS, tg, FL_CS + cb, tid == 0 && bi < 14);
  ST(2);
  const int gq = 8 * hf + w;
  const unsigned qkvs = OFF_QKV + gq * 4 * 84 * 1024;
  const int b01 = (28 * hd + (lane & 1) * 27) >> 6;
  const int U0 = (28 * hd) >> 4, o = (28 * hd) & 15;
  const unsigned f2 = (o >= 8 ? 2 : 0) * 1024;
  const unsigned img = w * 18432;
  u32x4 wp[3];
  unsigned vw[6];
  sfor<0, 3>([&](auto I) {
    unsigned a[2];
    sfor<0, 2>([&](auto T) {
      const int d0 = 16 * I.v + 8 * T.v + 4 * h - o;
      const bool ok = d0 >= 0 && d0 < 28;
      a[T.v] = (ok ? WPHI + (d0 + q) * 56 : ZERO) + (16 * blk + 4 * pp) * 2;
      vw[2 * I.v + T.v] = ok ? img + 16384 + t32 * 64 + (((d0 >> 2) ^ ((t32 >> 1) & 7)) << 3) : DUMMY + lane * 8;
    });
    wp[I.v] = trfrag(a[0], a[1]);
  });
  u32x4 kp[4][3], vp[4][3], qp[4][3];
  auto gather = [&](u32x4 (&d)[4][3], unsigned base) {
    sfor<0, 4>([&](auto T) {
      const unsigned so = qkvs + (T.v * 84 + base + U0) * 1024;
      ld16s<0>(d[T.v][0], lane * 16, sWS, so); ld16s<1024>(d[T.v][1], lane * 16, sWS, so); ld16s<0>(d[T.v][2], lane * 16, sWS, so + f2);
    });
  };
  auto feat = [&](u32x4 (&k)[3], u32x4 (&out)[4]) {
    sfor<0, 3>([&](auto I) { tie(k[I.v]); });
    f32x16 f = mfma(wp[0], k[0], zero16());
    f = mfma(wp[1], k[1], f);
    f = mfma(wp[2], k[2], f);
    dual_softmax(f, h != 0, out);
  };
  const unsigned fq = gq * 14 + b01;
  poll(fl, FL_A + fq + 7, tag);
  ST(3);
  gather(kp, 28);
  if (h) ldsw<u32x2>(img + 16384 + t32 * 64 + ((7 ^ ((t32 >> 1) & 7)) << 3), u32x2{0x3f80u, 0u});
  sfor<0, 4>([&](auto T) {
    if constexpr (T.v < 3) wvm<9 - 3 * T.v>();
    else {
      poll(fl, FL_Q + fq + ((lane & 2) ? 7 : 0), tag);
      ST(5);
      gather(vp, 56);
      gather(qp, 0);
    }
    u32x4 ph[4];
    feat(kp[T.v], ph);
    const unsigned pr = img + T.v * 4096 + t32 * 128, sw = h ^ (((t32 >> 1) & 1) << 3) ^ (t32 & 1) ^ (((t32 >> 2) & 3) << 1);
    sfor<0, 8>([&](auto A) { ldsw<u32x2>(pr + (((2 * A.v) ^ sw) << 3), u32x2{ph[A.v >> 1][2 * (A.v & 1)], ph[A.v >> 1][2 * (A.v & 1) + 1]}); });
  });
  ST(4);
  f32x16 kvp = zero16(), kvm = zero16();
  unsigned aa[2], ba[2];
  sfor<0, 2>([&](auto T) {
    const int row = 8 * h + 4 * T.v + q, pc = 4 * blk + pp;
    aa[T.v] = img + row * 128 + ((pc ^ ((q >> 1) << 3) ^ (q & 1) ^ ((2 * h + T.v) << 1)) << 3);
    ba[T.v] = img + 16384 + row * 64 + ((pc ^ (row >> 1)) << 3);
  });
  sfor<0, 4>([&](auto T) {
    wvm<21 - 3 * T.v>();
    sfor<0, 3>([&](auto I) { tie(vp[T.v][I.v]); });
    sfor<0, 6>([&](auto A) { ldsw<u32x2>(vw[A.v], u32x2{vp[T.v][A.v >> 1][2 * (A.v & 1)], vp[T.v][A.v >> 1][2 * (A.v & 1) + 1]}); });
    sfor<0, 2>([&](auto U) {
      constexpr unsigned oa = T.v * 4096 + U.v * 2048;
      const u32x4 bf = trfrag(ba[0] + U.v * 1024, ba[1] + U.v * 1024);
      kvp = mfma(trfrag(aa[0] + oa, aa[1] + oa), bf, kvp);
      kvm = mfma(trfrag((aa[0] ^ 64u) + oa, (aa[1] ^ 64u) + oa), bf, kvm);
    });
  });
  sfor<0, 8>([&](auto A) { ldsw<f32x4>(img + A.v * 1024 + lane * 16, quad(A.v < 4 ? kvp : kvm, A.v & 3)); });
  bar();
  f32x4 own = {0.f, 0.f, 0.f, 0.f};
  sfor<0, 8>([&](auto S) { own += ldsr<f32x4>(S.v * 18432 + w * 1024 + lane * 16); });
  st16s(__builtin_bit_cast(u32x4, own), sWS, lane * 16, OFF_KVP + ((hd * 2 + hf) * 8 + w) * 1024);
  u32x4 phq[4][4];
  wvm<1>();
  feat(qp[0], phq[0]);
  wvm<0>();
  bar();
  publish(sWS, tg, FL_KV + hd * 2 + hf, tid == 0);
  ST(6);
  feat(qp[1], phq[1]); feat(qp[2], phq[2]);
  poll(fl, FL_KV + hd * 2 + (hf ^ 1), tag);
  u32x4 oth;
  ld16s<0>(oth, lane * 16, sWS, OFF_KVP + ((hd * 2 + (hf ^ 1)) * 8 + w) * 1024);
  feat(qp[3], phq[3]);
  wvm<0>(oth);
  own += __builtin_bit_cast(f32x4, oth);
  ldsw<u32x2>(147456 + (w >> 1) * 1024 + lane * 16 + (w & 1) * 8, u32x2{pk2(own[0], own[1]), pk2(own[2], own[3])});
  bar();
  ST(7);
  u32x4 kvf[4];
  sfor<0, 4>([&](auto I) { kvf[I.v] = ldsr<u32x4>(147456 + I.v * 1024 + lane * 16); });
  sfor<0, 4>([&](auto T) {
    f32x16 n = mfma(kvf[0], phq[T.v][0], zero16());
    n = mfma(kvf[1], phq[T.v][1], n);
    n = mfma(kvf[2], phq[T.v][2], n);
    n = mfma(kvf[3], phq[T.v][3], n);
    const float rd = __builtin_amdgcn_rcpf(swap_hi(n[12]) + 1e-6f);
#pragma unroll
    for (int i = 0; i < 16; ++i) n[i] *= rd;
    u32x4 o1 = pack8(n, 1);
    if (h) { o1[2] = 0u; o1[3] = 0u; }
    const unsigned so = OFF_O + ((4 * gq + T.v) * 32 + 2 * hd) * 1024;
    st16s(pack8(n, 0), sWS, lane * 16, so); st16s<1024>(o1, sWS, lane * 16, so);
  });
  wvm<0>();
  publish(sWS, tg, FL_O + gq * 16 + hd, lane == 0);
  ST(8);
  if (xc == 0 && rs == 0 && w == 0) {
    sfor<0, 4>([&](auto I) { poll(fl, FL_Q + min(64 * I.v + lane, 223), tag); });
    publish(sWS, tg, FL_E, lane == 0);
  }
}

__global__ __launch_bounds__(512, 2) void fused(Params p) {
  u64 pc;
  asm volatile("s_getpc_b64 %0" : "=s"(pc));
  ST(0);
  int w = __builtin_amdgcn_readfirstlane(threadIdx.x >> 6);
  asm volatile("s_nop 4" : "+s"(w));
  const int xc = blockIdx.x & 7, r = blockIdx.x >> 3;
  if (r < 28 && w >= 4) first_loads(p, w - 4, xc, r);
  SB();
  if (r < 28) {
    if (w < 4) compute_path(p, w, xc, r STPASS);
    else loader_path(p, pc, w - 4, xc, r STPASS);
  }
  else spare_path(p, w, xc, r STPASS);
}

extern "C" void kernel_launch(void* const* d_in, const int* in_sizes, int n_in, void* d_out, int out_size, void* d_ws, size_t ws_size,
                              hipStream_t stream) {
  Params p;
  p.x = (const uint16_t*)d_in[0]; p.wqkv = (const uint16_t*)d_in[1]; p.wphi = (const uint16_t*)d_in[2]; p.wo = (const uint16_t*)d_in[3];
  p.w1 = (const uint16_t*)d_in[5]; p.w2 = (const uint16_t*)d_in[6]; p.w3 = (const uint16_t*)d_in[7]; p.w4 = (const uint16_t*)d_in[8];
  p.out = (uint16_t*)d_out; p.ws = (char*)d_ws;
  (void)hipFuncSetAttribute((const void*)fused, hipFuncAttributeMaxDynamicSharedMemorySize, LDS_BYTES);
  fused<<<256, 512, LDS_BYTES, stream>>>(p);
}
```

```cpp
#include <hip/hip_runtime.h>
#include <stdint.h>

#define LAS __attribute__((address_space(3)))
#define GAS __attribute__((address_space(1)))
#define DI __device__ __forceinline__
typedef short s16x4 __attribute__((ext_vector_type(4)));
typedef __bf16 bf16x8 __attribute__((ext_vector_type(8)));
typedef __bf16 bf16x2 __attribute__((ext_vector_type(2)));
typedef float f32x16 __attribute__((ext_vector_type(16)));
typedef float f32x4 __attribute__((ext_vector_type(4)));
typedef float f32x2 __attribute__((ext_vector_type(2)));
typedef unsigned u32x4 __attribute__((ext_vector_type(4)));
typedef unsigned u32x2 __attribute__((ext_vector_type(2)));
typedef unsigned long long u64;
typedef __amdgpu_buffer_rsrc_t srd_t;

constexpr int D = 448;
constexpr float LOG2E = 1.4426950408889634f;
constexpr unsigned OFF_FLG = 0;
constexpr unsigned OFF_H = 262144;
constexpr unsigned OFF_X1 = OFF_H + 64 * 28 * 1024;
constexpr unsigned OFF_QKV = OFF_X1 + 64 * 28 * 1024;
constexpr unsigned OFF_H3 = OFF_QKV + 64 * 84 * 1024;
constexpr unsigned OFF_O = OFF_H3 + 64 * 112 * 1024;
constexpr unsigned OFF_KVP = OFF_O + 64 * 32 * 1024;
constexpr unsigned OFF_ST0 = OFF_KVP + 32 * 8192;
constexpr unsigned OFF_ST1 = OFF_ST0 + 2048 * 16;
constexpr unsigned OFF_CS3 = OFF_ST1 + 14 * 2048 * 8;
constexpr unsigned WS_USED = OFF_CS3 + 42 * 256;
constexpr size_t OFF_STAMP = 64u << 20;
constexpr int FL_E = 0, FL_A = 1, FL_Q = FL_A + 16 * 14, FL_X = FL_Q + 16 * 14, FL_3A = FL_X + 32 * 7, FL_3B = FL_3A + 16 * 14,
              FL_O = FL_3B + 16 * 14, FL_KV = FL_O + 256, FL_CS = FL_KV + 32;
static_assert((FL_CS + 42) * 128 <= 262144, "flag block");
constexpr unsigned RA = 0, R1 = 57344, R2 = 114688, LDS_BYTES = 163840;
constexpr unsigned OOB = 0x80000000u;
constexpr unsigned CODE_BYTES = 46080;
constexpr unsigned SPIN_CAP = 1u << 18;

struct Params {
  const uint16_t *x, *wqkv, *wphi, *wo, *w1, *w2, *w3, *w4;
  uint16_t* out;
  char* ws;
};

template <int I> struct IC { static constexpr int v = I; };
template <int B, int E, class F> DI void sfor(F&& f) {
  if constexpr (B < E) { f(IC<B>{}); sfor<B + 1, E>(f); }
}
DI f32x16 mfma(u32x4 a, u32x4 b, f32x16 c) {
  return __builtin_amdgcn_mfma_f32_32x32x16_bf16(__builtin_bit_cast(bf16x8, a), __builtin_bit_cast(bf16x8, b), c, 0, 0, 0);
}
DI void mma(f32x16& c, u32x4 a, u32x4 b) { asm volatile("v_mfma_f32_32x32x16_bf16 %0, %1, %2, %0" : "+v"(c) : "v"(a), "v"(b) : "memory"); }
DI void mma0(f32x16& c, u32x4 a, u32x4 b) { asm volatile("v_mfma_f32_32x32x16_bf16 %0, %1, %2, 0" : "=&v"(c) : "v"(a), "v"(b) : "memory"); }
template <bool FIRST> DI void mmx(f32x16& c, u32x4 a, u32x4 b) { if constexpr (FIRST) mma0(c, a, b); else mma(c, a, b); }
DI void settle(f32x16& a, f32x16& b) { asm volatile("s_nop 11" : "+v"(a), "+v"(b)); }
DI unsigned pk2(float a, float b) {
  f32x2 v = {a, b};
  return __builtin_bit_cast(unsigned, __builtin_convertvector(v, bf16x2));
}
DI float u2f(unsigned u) { return __builtin_bit_cast(float, u); }
DI unsigned f2u(float f) { return __builtin_bit_cast(unsigned, f); }
DI float bflo(unsigned u) { return __builtin_bit_cast(float, u << 16); }
DI float bfhi(unsigned u) { return __builtin_bit_cast(float, u & 0xffff0000u); }
DI u32x4 trfrag(unsigned a0, unsigned a1) {
  u32x2 lo = __builtin_bit_cast(u32x2, __builtin_amdgcn_ds_read_tr16_b64_v4i16((LAS s16x4*)(uintptr_t)a0));
  u32x2 hi = __builtin_bit_cast(u32x2, __builtin_amdgcn_ds_read_tr16_b64_v4i16((LAS s16x4*)(uintptr_t)a1));
  return u32x4{lo.x, lo.y, hi.x, hi.y};
}
template <class T> DI T ldsr(unsigned a) { return *(LAS T*)(uintptr_t)a; }
template <class T> DI void ldsw(unsigned a, T v) { *(LAS T*)(uintptr_t)a = v; }
DI srd_t mk(const void* p, unsigned bytes) { return __builtin_amdgcn_make_buffer_rsrc((void*)p, 0, (int)bytes, 0x00020000); }
DI void dma16(unsigned voff, srd_t r, unsigned soff, unsigned lds) {
  asm volatile("s_mov_b32 m0, %3\n\ts_nop 0\n\tbuffer_load_dwordx4 %0, %1, %2 offen lds" ::"v"(voff), "s"(r), "s"(soff), "s"(lds) : "memory");
}
DI void dma16s(unsigned voff, srd_t r, unsigned soff, unsigned lds) {
  asm volatile("s_mov_b32 m0, %3\n\ts_nop 0\n\tbuffer_load_dwordx4 %0, %1, %2 offen sc1 lds" ::"v"(voff), "s"(r), "s"(soff), "s"(lds) : "memory");
}
DI void ld16p(u32x4& d, unsigned voff, srd_t r, unsigned soff) {
  asm volatile("buffer_load_dwordx4 %0, %1, %2, %3 offen" : "=v"(d) : "v"(voff), "s"(r), "s"(soff) : "memory");
}
template <int IMM> DI void ld16s(u32x4& d, unsigned voff, srd_t r, unsigned soff) {
  asm volatile("buffer_load_dwordx4 %0, %1, %2, %3 offen offset:%c4 sc1" : "=v"(d) : "v"(voff), "s"(r), "s"(soff), "i"(IMM) : "memory");
}
template <int IMM> DI void ld8s(u32x2& d, unsigned voff, srd_t r, unsigned soff) {
  asm volatile("buffer_load_dwordx2 %0, %1, %2, %3 offen offset:%c4 sc1" : "=v"(d) : "v"(voff), "s"(r), "s"(soff), "i"(IMM) : "memory");
}
template <int IMM = 0> DI void st16s(u32x4 v, srd_t r, unsigned voff, unsigned soff) {
  asm volatile("buffer_store_dwordx4 %0, %1, %2, %3 offen offset:%c4 sc1\n\ts_nop 1" ::"v"(v), "v"(voff), "s"(r), "s"(soff), "i"(IMM) : "memory");
}
DI void st8s(u32x2 v, srd_t r, unsigned voff, unsigned soff) {
  asm volatile("buffer_store_dwordx2 %0, %1, %2, %3 offen sc1" ::"v"(v), "v"(voff), "s"(r), "s"(soff) : "memory");
}
template <int N> DI void wvm() { asm volatile("s_waitcnt vmcnt(%c0)" ::"i"(N) : "memory"); }
template <class T> DI void tie(T& a) { asm volatile("" : "+v"(a)); }
template <int N, class T> DI void wvm(T& a) { asm volatile("s_waitcnt vmcnt(%c1)" : "+v"(a) : "i"(N) : "memory"); }
DI void wlg4(u32x4& a, u32x4& b, u32x4& c, u32x4& d) { asm volatile("s_waitcnt lgkmcnt(0)" : "+v"(a), "+v"(b), "+v"(c), "+v"(d)::"memory"); }
template <int N> DI void wvm4(u32x4& a, u32x4& b, u32x4& c, u32x4& d) {
  asm volatile("s_waitcnt vmcnt(%c4)" : "+v"(a), "+v"(b), "+v"(c), "+v"(d) : "i"(N) : "memory");
}
template <int N> DI void wvm2(u32x4& a, u32x4& b) { asm volatile("s_waitcnt vmcnt(%c2)" : "+v"(a), "+v"(b) : "i"(N) : "memory"); }
DI int fresh(int v) { asm volatile("" : "+s"(v)); return v; }
DI void bar() { asm volatile("s_waitcnt lgkmcnt(0)\n\ts_barrier" ::: "memory"); }
#define SB() __builtin_amdgcn_sched_barrier(0)
DI void halves(float v, float& lo, float& hi) {
  auto r = __builtin_amdgcn_permlane32_swap(f2u(v), f2u(v), false, false);
  lo = u2f(r[0]); hi = u2f(r[1]);
}
DI float swap_add(float v) { float a, b; halves(v, a, b); return a + b; }
DI float swap_max(float v) { float a, b; halves(v, a, b); return fmaxf(a, b); }
DI float swap_hi(float v) { float a, b; halves(v, a, b); return b; }
typedef f32x4 GeluK;
DI GeluK gelu_k(float r, float ir) {
  const float c0 = -2.f * 0.7978845608028654f * LOG2E, c1 = c0 * 0.044715f;
  return GeluK{c0 * r, c1 * r * r * r, ir, 0.f};
}
DI f32x2 gelu2(f32x2 z, GeluK k) {
  const f32x2 u = z * (z * z * k[1] + k[0]);
  const f32x2 e = {__builtin_amdgcn_exp2f(u[0]), __builtin_amdgcn_exp2f(u[1])};
  const f32x2 d = e * k[2] + k[2];
  return z * f32x2{__builtin_amdgcn_rcpf(d[0]), __builtin_amdgcn_rcpf(d[1])};
}
DI float dot2(unsigned a, unsigned b, float c) {
  return __builtin_amdgcn_fdot2_f32_bf16(__builtin_bit_cast(bf16x2, a), __builtin_bit_cast(bf16x2, b), c, false);
}
constexpr unsigned ONE2 = 0x3f803f80u;
DI f32x16 zero16() { f32x16 z; for (int i = 0; i < 16; ++i) z[i] = 0.f; return z; }
DI u32x4 pack8(const f32x16& a, int u) {
  return u32x4{pk2(a[8 * u], a[8 * u + 1]), pk2(a[8 * u + 2], a[8 * u + 3]), pk2(a[8 * u + 4], a[8 * u + 5]), pk2(a[8 * u + 6], a[8 * u + 7])};
}
DI void poll(GAS u64* fl, int idx, u64 tag) {
#pragma unroll 1
  for (unsigned spins = 0; spins < SPIN_CAP; ++spins) {
    const u64 v = __hip_atomic_load(fl + idx * 16, __ATOMIC_RELAXED, __HIP_MEMORY_SCOPE_AGENT);
    if (__all(v == tag)) break;
    __builtin_amdgcn_s_sleep(1);
  }
  asm volatile("" ::: "memory");
}
DI void publish(srd_t ws, u32x2 tg, unsigned idx, bool on) { st8s(tg, ws, on ? idx * 128 : OOB, OFF_FLG); }
DI bool all_set(u32x2 f, u32x2 tg) { return __all(f.x == tg.x && f.y == tg.y); }

#define ST(i) do {} while (0)

template <int NU> DI f32x16 colsum(unsigned a) {
  const u32x4 ones = {ONE2, ONE2, ONE2, ONE2};
  f32x16 c = zero16();
  sfor<0, NU>([&](auto UU) { c = mfma(trfrag(a + UU.v * 2048, a + UU.v * 2048 + 1024), ones, c); });
  return c;
}
DI void cs_write(unsigned dst, const f32x16& c, int lane) {
  if ((lane & 31) == 0) {
    const unsigned d = dst + (lane >> 5) * 16;
    sfor<0, 4>([&](auto A) { ldsw<f32x4>(d + A.v * 32, f32x4{c[4 * A.v], c[4 * A.v + 1], c[4 * A.v + 2], c[4 * A.v + 3]}); });
  }
}
DI float max3(float a, float b, float c) { float r; asm("v_max3_f32 %0, %1, %2, %3" : "=v"(r) : "v"(a), "v"(b), "v"(c)); return r; }
DI float min3(float a, float b, float c) { float r; asm("v_min3_f32 %0, %1, %2, %3" : "=v"(r) : "v"(a), "v"(b), "v"(c)); return r; }
DI void dual_softmax(f32x16 p, bool hi, u32x4 (&o)[4]) {
  p[0] += 0.f;
#pragma unroll
  for (int i = 12; i < 16; ++i) p[i] = hi ? p[0] : p[i];
  float mx = max3(p[0], p[1], p[2]), mn = min3(p[0], p[1], p[2]);
#pragma unroll
  for (int i = 3; i < 15; i += 2) { mx = max3(mx, p[i], p[i + 1]); mn = min3(mn, p[i], p[i + 1]); }
  float a, b;
  halves(max3(mx, p[15], p[15]), a, b);
  const float cp = -LOG2E * max3(a, b, b);
  halves(min3(mn, p[15], p[15]), a, b);
  const float cm = LOG2E * min3(a, b, b);
  f32x2 ep[8], em[8];
#pragma unroll
  for (int i = 0; i < 8; ++i) {
    const f32x2 v = {p[2 * i], p[2 * i + 1]}, x = v * LOG2E + cp, y = v * -LOG2E + cm;
    ep[i] = f32x2{__builtin_amdgcn_exp2f(x[0]), __builtin_amdgcn_exp2f(x[1])};
    em[i] = f32x2{__builtin_amdgcn_exp2f(y[0]), __builtin_amdgcn_exp2f(y[1])};
  }
  const float keep = hi ? 0.f : 1.f;
  const f32x2 sp = ((ep[0] + ep[1]) + (ep[2] + ep[3])) + ((ep[4] + ep[5]) + (ep[6] + ep[7]) * keep);
  const f32x2 sn = ((em[0] + em[1]) + (em[2] + em[3])) + ((em[4] + em[5]) + (em[6] + em[7]) * keep);
  const float rp = __builtin_amdgcn_rcpf(swap_add(sp[0] + sp[1])), rm = __builtin_amdgcn_rcpf(swap_add(sn[0] + sn[1]));
#pragma unroll
  for (int i = 0; i < 8; ++i) {
    const f32x2 x = ep[i] * (i < 6 ? rp : rp * keep), y = em[i] * (i < 6 ? rm : rm * keep);
    o[i >> 2][i & 3] = pk2(x[0], x[1]);
    o[2 + (i >> 2)][i & 3] = pk2(y[0], y[1]);
  }
}
DI void granule(u32x4& g, srd_t ws, unsigned voff, u32x2 tg) {
#pragma unroll 1
  for (unsigned spins = 0; spins < SPIN_CAP && !__all(g[1] == tg.x && g[3] == tg.y); ++spins) {
    ld16s<0>(g, voff, ws, OFF_ST0);
    wvm<0>(g);
  }
}
DI f32x16 cs_read(unsigned a, unsigned str) {
  f32x4 v[4][4];
  sfor<0, 4>([&](auto P) { sfor<0, 4>([&](auto A) { v[P.v][A.v] = ldsr<f32x4>(a + P.v * str + A.v * 32); }); });
  f32x16 r;
  sfor<0, 4>([&](auto A) {
    const f32x4 t = (v[0][A.v] + v[1][A.v]) + (v[2][A.v] + v[3][A.v]);
    sfor<0, 4>([&](auto I) { r[4 * A.v + I.v] = t[I.v]; });
  });
  return r;
}
DI f32x4 quad(const f32x16& c, int a) { return f32x4{c[4 * a], c[4 * a + 1], c[4 * a + 2], c[4 * a + 3]}; }
DI u32x4 cross(u32x4 v) {
  auto a = __builtin_amdgcn_permlane32_swap(v[0], v[2], false, false);
  auto b = __builtin_amdgcn_permlane32_swap(v[1], v[3], false, false);
  return u32x4{a[0], b[0], a[1], b[1]};
}

#define STARG
#define STPASS
DI void code_prefetch(unsigned& d, u64 pc, unsigned voff) {
  asm volatile("buffer_load_dword %0, %1, %2, 0 offen" : "=v"(d) : "v"(voff), "s"(mk((const void*)(pc & ~127ull), CODE_BYTES)) : "memory");
}
DI u64 make_tag(u32x2 ev) {
  return (((u64)(unsigned)__builtin_amdgcn_readfirstlane(ev.y) << 32) | (unsigned)__builtin_amdgcn_readfirstlane(ev.x)) + 0x9E3779B97F4A7C15ull;
}

struct SlotP1 {
  static constexpr unsigned off(int c) { return R2 - 16384 + ((c + 1) % 4) * 16384; }
  static DI unsigned addr(const unsigned (&xr)[4], int U) { return xr[U & 3] + ((U / 4 + 1) % 4) * 16384; }
};
struct SlotP3 {
  static constexpr unsigned off(int c) { return c < 3 ? RA + c * 16384 : c < 6 ? R2 + (c - 3) * 16384 : RA; }
  static DI unsigned addr(const unsigned (&xr)[4], int U) { return xr[U >= 12 && U < 24] + (U / 4 % 3) * 16384 + (U & 3) * 1024; }
};
constexpr unsigned HREG = R2;
constexpr unsigned XT = R2 + 40960;
constexpr int UH = 12;
constexpr int M3 = 20;
template <int TS, class Slot>
DI void pass_a(f32x16& c0, f32x16& c1, const unsigned a0, const unsigned a1, const unsigned (&xr)[4], u32x4* xf) {
  auto rx = [&](auto UU) { xf[UU.v] = ldsr<u32x4>(Slot::addr(xr, UU.v)); };
  u32x4 wf[3][2];
  auto rd = [&](auto UU, auto NN) {
    const unsigned ad = (NN.v ? a1 : a0) + UU.v * 2048;
    wf[UU.v % 3][NN.v] = trfrag(ad, ad + TS);
  };
  bar();
  rx(IC<0>{}); rd(IC<0>{}, IC<0>{}); rd(IC<0>{}, IC<1>{}); rx(IC<1>{}); rd(IC<1>{}, IC<0>{}); rd(IC<1>{}, IC<1>{});
  sfor<0, 28>([&](auto UU) {
    constexpr int U = UU.v;
    if constexpr (U % 4 == 2 && U < 26) asm volatile("s_barrier" ::: "memory");
    mmx<U == 0>(c0, wf[U % 3][0], xf[U]);
    if constexpr (U + 2 < 28) { rx(IC<U + 2>{}); rd(IC<U + 2>{}, IC<0>{}); }
    SB();
    mmx<U == 0>(c1, wf[U % 3][1], xf[U]);
    if constexpr (U + 2 < 28) rd(IC<U + 2>{}, IC<1>{});
    SB();
  });
}
DI int rot7(int c, int rot) { const int v = c + rot; return v >= 7 ? v - 7 : v; }
struct Geo {
  int r7, s7, g4, tt, s14, rot;
  bool wtype;
  DI Geo(int xc, int r) {
    r7 = r / 7;
    s7 = r - 7 * r7;
    g4 = 4 * (xc >> 1) + r7;
    tt = 4 * xc + r7;
    s14 = 7 * (xc & 1) + s7;
    rot = (s7 + 2 * r7) % 7;
    wtype = (xc & 1) == 0;
  }
};
DI unsigned p4_slot(int kq, int j) {
  return j < 2 ? R2 + 16384 + (2 * kq + j) * 4096 : j < 5 ? R1 + (3 * kq + j - 2) * 4096 : j < 8 ? RA + (3 * kq + j - 5) * 4096 : (kq < 2 ? R1 + 49152 : R2 + 8192) + (kq & 1) * 4096;
}
DI int p4_k32(int kq, int s, int rot) {
  const int a = 7 * kq + rot7(s < 7 ? s : s - 7, rot);
  return 4 * (a >> 1) + (a & 1) + (s < 7 ? 0 : 2);
}

DI void first_loads(const Params& p, const int w, const int xc, const int r) {
  const int lane = threadIdx.x & 63;
  const Geo g(xc, r);
  const int rr = lane >> 3, cp = lane & 7;
  const unsigned wsw = (unsigned)((cp ^ (((rr >> 1) & 1) << 2)) << 4);
  const srd_t sX = mk(p.x, 2048 * 896), sQ = mk(p.wqkv, 448 * 2688);
  const srd_t sA = g.wtype ? mk(p.w1, 448 * 896) : sQ;
  const unsigned pitchA = g.wtype ? 896 : 2688;
  const unsigned colA = g.wtype ? g.s7 : 7 + g.s7;
  const unsigned wvA = rr * pitchA + wsw;
  const unsigned xv0 = rr * 896 + ((cp ^ (rr >> 1)) << 4), xv1 = xv0 ^ 64u;
  const unsigned T32 = 4 * g.g4 + w, xs = T32 * 32 * 896;
  auto G1 = [&](int c) {
    const int cg = rot7(c, g.rot);
    sfor<0, 2>([&](auto J) { dma16(wvA, sA, (8 * cg + 2 * w + J.v) * 8 * pitchA + colA * 128, RA + (8 * c + 2 * w + J.v) * 1024); });
    sfor<0, 4>([&](auto I) { dma16((I.v & 1) ? xv1 : xv0, sX, xs + I.v * 8 * 896 + cg * 128, SlotP1::off(c) + w * 4096 + I.v * 1024); });
  };
  G1(0); G1(1);
}

DI void loader_path(const Params& p, const int w, const int xc, const int r STARG) {
  const int lane = threadIdx.x & 63, t32 = lane & 31, h = lane >> 5;
  const Geo g(xc, r);
  const int rr = lane >> 3, cp = lane & 7;
  const unsigned wsw = (unsigned)((cp ^ (((rr >> 1) & 1) << 2)) << 4);
  const srd_t sWS = mk(p.ws, WS_USED), sX = mk(p.x, 2048 * 896), sQ = mk(p.wqkv, 448 * 2688);
  const srd_t sA = g.wtype ? mk(p.w1, 448 * 896) : sQ;
  const unsigned pitchA = g.wtype ? 896 : 2688;
  const unsigned colA = g.wtype ? g.s7 : 7 + g.s7, colB = g.wtype ? g.s7 : 14 + g.s7;
  const unsigned wvA = rr * pitchA + wsw;
  const unsigned xv0 = rr * 896 + ((cp ^ (rr >> 1)) << 4), xv1 = xv0 ^ 64u;
  const unsigned T32 = 4 * g.g4 + w, xs = T32 * 32 * 896;
  auto G1 = [&](int c) {
    const int cg = rot7(c, g.rot);
    sfor<0, 2>([&](auto J) { dma16(wvA, sA, (8 * cg + 2 * w + J.v) * 8 * pitchA + colA * 128, RA + (8 * c + 2 * w + J.v) * 1024); });
    sfor<0, 4>([&](auto I) { dma16((I.v & 1) ? xv1 : xv0, sX, xs + I.v * 8 * 896 + cg * 128, SlotP1::off(c) + w * 4096 + I.v * 1024); });
  };
  u32x2 ev;
  ld8s<0>(ev, 0u, sWS, OFF_FLG);
  SB();
  const unsigned wv896 = rr * 896 + wsw, wv3 = rr * 3584 + wsw, wvB = rr * 2688 + wsw;
  auto WB = [&](int k) { dma16(wvB, sQ, (8 * rot7(k >> 1, g.rot) + 4 * (k & 1) + w) * 8 * 2688 + colB * 128, R1 + (4 * k + w) * 1024); };
  const int q = (lane & 15) >> 2, pp = lane & 3, blk = (lane >> 4) & 1;
  const unsigned ca = (4 * h + q) * 128 + (unsigned)((((2 * blk + (pp >> 1)) ^ ((q >> 1) << 2)) << 4) + (pp & 1) * 8);
  const u32x4 ones = {ONE2, ONE2, ONE2, ONE2};
  f32x16 s0 = zero16(), s1 = zero16();
  auto CS = [&](unsigned a) { s0 = mfma(trfrag(a, a + 1024), ones, s0); s1 = mfma(trfrag(a ^ 64u, (a ^ 64u) + 1024), ones, s1); };
  const unsigned cw = RA + ca + w * 2048;
  u32x4 gr;
  wvm<7>(); bar(); G1(2); CS(cw);
  wvm<7>(); bar(); G1(3); CS(cw + 8192);
  wvm<6>(ev); bar(); G1(4); CS(cw + 2 * 8192);
  wvm<6>(); bar(); G1(5); CS(cw + 3 * 8192);
  ld16s<0>(gr, (T32 * 32 + t32) * 16, sWS, OFF_ST0);
  wvm<7>(); bar(); G1(6); CS(cw + 4 * 8192);
  const u64 tag = make_tag(ev);
  const u32x2 tg = {(unsigned)tag, (unsigned)(tag >> 32)};
  wvm<6>(gr); bar(); sfor<0, 3>([&](auto K) { WB(K.v); });
  CS(cw + 5 * 8192);
  granule(gr, sWS, (T32 * 32 + t32) * 16, tg);
  const float ve1 = u2f(gr[2]);
  float r1 = __builtin_amdgcn_rsqf(ve1);
  const float v2 = (ve1 - 1e-5f) * r1 * r1 + 1e-5f;
  GeluK k1 = gelu_k(r1 * __builtin_amdgcn_rsqf(v2), __builtin_amdgcn_sqrtf(ve1 * v2));
  k1[3] = u2f(gr[0]);
  tie(k1); tie(r1);
  cs_write(R2 - 16384 + w * 128, s0, lane); cs_write(R2 - 16384 + 512 + w * 128, s1, lane);
  wvm<3>(); bar();
  {
    const unsigned a = R2 - 16384 + h * 512 + t32 * 4, c6 = (RA + ca + 24 * 2048) ^ ((w & 1) * 64);
    u32x4 f[4];
    sfor<0, 4>([&](auto U) { f[U.v] = trfrag(c6 + U.v * 2048, c6 + U.v * 2048 + 1024); });
    const float t = (ldsr<float>(a) + ldsr<float>(a + 128)) + (ldsr<float>(a + 256) + ldsr<float>(a + 384));
    s0 = zero16();
    sfor<0, 4>([&](auto U) { s0 = mfma(f[U.v], ones, s0); });
    sfor<3, 6>([&](auto K) { WB(K.v); });
    ldsw<f32x4>(R2 + 2048 + w * 1024 + lane * 16, k1);
    ldsw<float>(R2 + 6144 + w * 256 + lane * 4, r1);
    ldsw<float>(R2 + 1024 + lane * 4, t);
    cs_write(R2 + 1280 + (w & 1) * 128, s0, lane);
  }
  GAS u64* fl = (GAS u64*)(p.ws + OFF_FLG);
  const bool l0 = threadIdx.x == 256;
  bar();
  sfor<6, 14>([&](auto K) { WB(K.v); });
  sfor<0, 2>([&](auto J) { dma16(rr * 896 + ((cp ^ rr) << 4), sX, (g.tt * 64 + (2 * w + J.v) * 8) * 896 + g.s7 * 128, XT + (2 * w + J.v) * 1024); });
  const srd_t sW2 = mk(p.w2, 448 * 896);
  auto W2 = [&](int k) { const int pc = 8 * rot7(k >> 1, g.rot) + 4 * (k & 1) + w; dma16(wv896, sW2, pc * 8 * 896 + g.s7 * 128, RA + pc * 1024); };
  sfor<0, 4>([&](auto K) { W2(K.v); });
  wvm<4>(); bar();
  sfor<4, 14>([&](auto K) { W2(K.v); });
  const srd_t sW4 = mk(p.w4, 1792 * 896), sWo = mk(p.wo, 448 * 896), sW3 = mk(p.w3, 448 * 3584);
  u32x4 w4r[9][4], w3r[14];
  auto L4 = [&](auto SS) {
    constexpr int s = SS.v;
    sfor<0, 4>([&](auto I) {
      u32x4& d = s < 9 ? w3r[4 * (s - 5) + I.v] : w4r[s < 9 ? 0 : s - 9][I.v];
      if constexpr (s < 14) ld16p(d, wv896, sW4, (32 * p4_k32(w, s, g.rot) + 8 * I.v) * 896 + g.s7 * 128);
      else ld16p(d, wv896, sWo, (28 * (4 * w + ((s + g.rot) & 3)) + 8 * I.v) * 896 + g.s7 * 128);
    });
  };
  bar();
  publish(sWS, tg, FL_A + g.g4 * 14 + g.s14, l0);
  poll(fl, FL_CS + 28 + g.s14, tag);
  asm volatile("s_mov_b32 m0, %3\n\ts_nop 0\n\tbuffer_load_dword %0, %1, %2 offen sc1 lds" ::"v"(lane * 4), "s"(sWS), "s"(OFF_CS3 + (28 + g.s14) * 256), "s"(XT) : "memory");
  {
    const int th = w & 1, kh = w >> 1, tc = 2 * g.tt + th;
    const int gsel = min(max(tc + ((lane & 4) ? 2 * th - 1 : 0), 0), 63) >> 2;
    poll(fl, FL_A + gsel * 14 + min(3 * kh + (lane & 3), 6), tag);
    const srd_t sH = mk(p.ws + OFF_H, 64 * 28 * 1024);
    const int tok = tc * 32 + t32, ta = tok - 1 - kh, tb = tok + 1 + kh;
    const unsigned oa = (unsigned)((ta >> 5) * 28 * 1024 + ((ta & 31) + 32 * h) * 16), ob = (unsigned)((tb >> 5) * 28 * 1024 + ((tb & 31) + 32 * h) * 16);
    sfor<0, 6>([&](auto U) { dma16s(oa, sH, (kh * 14 + U.v) * 1024, HREG + w * 10240 + U.v * 1024); });
    bar();
    dma16s(oa, sH, (kh * 14 + 6) * 1024, HREG + w * 10240 + 6144);
    sfor<7, 10>([&](auto U) { dma16s(ob, sH, (kh * 14 + U.v) * 1024, HREG + w * 10240 + U.v * 1024); });
  }
  wvm<0>();
  bar();
  publish(sWS, tg, FL_Q + g.g4 * 14 + g.s14, l0);
  auto o3 = [&](int k) { return (unsigned)((8 * rot7(k >> 1, g.rot) + 4 * (k & 1) + w) * 8 * 3584 + g.s14 * 256); };
  sfor<0, 12>([&](auto K) { dma16(wv3, sW3, o3(K.v), R1 + (4 * K.v + w) * 1024); });
  bar();
  sfor<12, 14>([&](auto K) { dma16(wv3, sW3, o3(K.v), R1 + (4 * K.v + w) * 1024); });
  sfor<0, 8>([&](auto K) { ld16p(w3r[K.v], wv3, sW3, o3(K.v) + 128); });
  bar();
  publish(sWS, tg, FL_X + g.tt * 7 + g.s7, l0);
  sfor<8, 14>([&](auto K) { ld16p(w3r[K.v], wv3, sW3, o3(K.v) + 128); });
  L4(IC<9>{}); L4(IC<10>{});
  poll(fl, (lane & 8) ? FL_CS + 2 * g.s14 + (lane & 1) : FL_X + (T32 >> 1) * 7 + min(lane & 7, 6), tag);
  auto G3 = [&](int c) { sfor<0, 4>([&](auto I) { dma16s(lane * 16, sWS, OFF_X1 + (T32 * 28 + 4 * rot7(c, g.rot) + I.v) * 1024, SlotP3::off(c) + w * 4096 + I.v * 1024); }); };
  u32x2 sp[7], cs3;
  G3(0);
  sfor<0, 7>([&](auto I) { ld8s<0>(sp[I.v], (T32 * 32 + t32 + h * 7 * 2048) * 8, sWS, OFF_ST1 + I.v * 2048 * 8); });
  ld8s<0>(cs3, lane * 8, sWS, OFF_CS3 + g.s14 * 512);
  G3(1);
  wvm<12>(); bar(); G3(2); G3(3);
  wvm<8>(cs3); bar(); G3(4);
  wvm<8>(); bar(); G3(5);
  wvm<8>(); bar(); G3(6);
  auto S3 = [&](auto K) { tie(w3r[K.v]); ldsw<u32x4>(RA + (4 * K.v + w) * 1024 + lane * 16, w3r[K.v]); };
  GeluK k3;
  {
    float sm = 0.f, sq = 0.f;
    sfor<0, 7>([&](auto I) { tie(sp[I.v]); sm += u2f(sp[I.v].x); sq += u2f(sp[I.v].y); });
    const float mean = swap_add(sm) * (1.f / D);
    const float ve = fmaxf(swap_add(sq) * (1.f / D) - mean * mean, 0.f) + 1e-5f;
    k3 = gelu_k(__builtin_amdgcn_rsqf(ve), __builtin_amdgcn_sqrtf(ve));
    k3[3] = mean;
  }
  sfor<4, 7>(S3);
  wvm<8>(); bar(); sfor<7, 10>(S3);
  wvm<4>(); bar(); L4(IC<11>{}); sfor<10, 12>(S3);
  ldsw<f32x4>(R2 + 2048 + w * 1024 + lane * 16, k3);
  ldsw<u32x2>(R2 + 1024 + lane * 8, cs3);
  wvm<4>(); bar(); L4(IC<12>{}); sfor<12, 14>(S3);
  bar();
  sfor<0, 4>(S3);
  auto W4 = [&](auto SS) {
    sfor<0, 4>([&](auto I) { dma16(wv896, sW4, (32 * p4_k32(w, SS.v, g.rot) + 8 * I.v) * 896 + g.s7 * 128, p4_slot(w, SS.v) + I.v * 1024); });
  };
  sfor<0, 3>(W4);
  bar();
  W4(IC<3>{}); W4(IC<4>{});
  bar();
  publish(sWS, tg, FL_3A + g.g4 * 14 + g.s14, l0);
  W4(IC<8>{});
  sfor<5, 8>(L4);
  bar();
  const unsigned f3 = (g.tt >> 1) * 14 + (7 * w >> 1) + (lane & 3);
  poll(fl, FL_3A + f3, tag);
  bar();
  sfor<0, 12>([&](auto K) { tie(w3r[K.v]); ldsw<u32x4>(p4_slot(w, 5 + K.v / 4) + (K.v & 3) * 1024 + lane * 16, w3r[K.v]); });
  bar();
  publish(sWS, tg, FL_3B + g.g4 * 14 + g.s14, l0);
  sfor<13, 18>(L4);
  auto S4 = [&](auto J) { sfor<0, 4>([&](auto I) { tie(w4r[J.v][I.v]); ldsw<u32x4>(p4_slot(w, J.v) + I.v * 1024 + lane * 16, w4r[J.v][I.v]); }); };
  poll(fl, FL_3B + f3, tag);
  bar(); sfor<0, 4>(S4);
  bar(); sfor<4, 8>(S4);
  poll(fl, FL_O + (g.tt >> 1) * 16 + 4 * w + (lane & 3), tag);
  bar(); S4(IC<8>{});
  bar();
}

DI void compute_path(const Params& p, unsigned& dummy, const int w, const int xc, const int r STARG) {
  const int tid = threadIdx.x, lane = tid & 63, t32 = lane & 31, h = lane >> 5, th = w & 1, kh = w >> 1;
  const Geo g(xc, r);
  const srd_t sWS = mk(p.ws, WS_USED);
  __builtin_amdgcn_s_setprio(3);
  u32x2 ev;
  ld8s<0>(ev, 0u, sWS, OFF_FLG);
  const unsigned T32 = 4 * g.g4 + w, tc = 2 * g.tt + th;
  const int ftok = tc * 32 + t32;
  u32x4 xv[2], x1p[2], gr;
  f32x16 xn;
  const int q = (lane & 15) >> 2, pp = lane & 3, blk = (lane >> 4) & 1;
  unsigned aperm = (4 * h + q) * 128 + (((2 * blk + (pp >> 1)) ^ ((q >> 1) << 2)) << 4) + (pp & 1) * 8, l16 = lane * 16;
  auto tra = [&](int hs, unsigned base, unsigned x) {
    tie(aperm);
    unsigned a = base + (aperm ^ x);
    if (hs == 8) a += l16 & 512;
    tie(a);
    return a;
  };
  auto lnv = [&](unsigned base) { tie(l16); unsigned a = base + l16; tie(a); return a; };
  u32x4 xf[28];
  unsigned xr[4], a0 = tra(8, RA, 0), a1 = tra(8, RA, 64);
  sfor<0, 4>([&](auto I) { xr[I.v] = R2 - 16384 + w * 4096 + t32 * 128 + (((2 * I.v + h) ^ ((t32 >> 1) & 7)) << 4); tie(xr[I.v]); });
  f32x16 c0, c1, c2, c3;
  pass_a<512, SlotP1>(c0, c1, a0, a1, xr, xf);
  bar();
  settle(c0, c1);
  f32x4 cst[8];
  const GeluK gk1 = ldsr<f32x4>(R2 + 2048 + w * 1024 + lane * 16);
  const float mean1 = gk1[3], r1 = ldsr<float>(R2 + 6144 + w * 256 + lane * 4);
  auto cs_load = [&](unsigned a) {
    sfor<0, 8>([&](auto A) { cst[A.v] = ldsr<f32x4>(a + A.v * 32 + h * 16); });
    sfor<0, 8>([&](auto A) { sfor<0, 4>([&](auto I) { (A.v < 4 ? c2 : c3)[4 * (A.v & 3) + I.v] = cst[A.v][I.v]; }); });
  };
  cs_load(R2 + 1024);
  sfor<0, 8>([&](auto A) { const f32x4 t = ldsr<f32x4>(R2 + 1280 + A.v * 32 + h * 16); sfor<0, 4>([&](auto I) { (A.v < 4 ? c2 : c3)[4 * (A.v & 3) + I.v] += t[I.v]; }); });
  wvm<0>(ev);
  tie(dummy);
  const u64 tag = make_tag(ev);
  const u32x2 tg = {(unsigned)tag, (unsigned)(tag >> 32)};
  auto epi = [&](auto ACT, unsigned so) {
    unsigned pk[4];
    sfor<0, 16>([&](auto J) {
      constexpr int e = 2 * (J.v & 7);
      const f32x16 &c = J.v < 8 ? c0 : c1, &s = J.v < 8 ? c2 : c3;
      f32x2 z = f32x2{c[e], c[e + 1]} - f32x2{s[e], s[e + 1]} * mean1;
      if constexpr (ACT.v) z = gelu2(z, gk1); else z *= r1;
      pk[J.v & 3] = pk2(z[0], z[1]);
      if constexpr ((J.v & 3) == 3) st16s<(J.v >> 2) * 1024>(u32x4{pk[0], pk[1], pk[2], pk[3]}, sWS, l16, so);
    });
  };
  if (g.wtype) epi(IC<1>{}, OFF_H + (T32 * 28 + 4 * g.s7) * 1024);
  else epi(IC<0>{}, OFF_QKV + (T32 * 84 + 28 + 4 * g.s7) * 1024);
  ld16s<0>(gr, ftok * 16, sWS, OFF_ST0);
  bar();
  ST(3);
  {
    u32x4 wf[3][2];
    float m0, r0;
    auto rd = [&](auto UU, auto NN) {
      const unsigned ad = (NN.v ? a1 : a0) + UU.v * 2048;
      wf[UU.v % 3][NN.v] = trfrag(ad, ad + 512);
    };
    a0 = tra(8, R1, 0); a1 = tra(8, R1, 64);
    rd(IC<0>{}, IC<0>{}); rd(IC<0>{}, IC<1>{}); rd(IC<1>{}, IC<0>{}); rd(IC<1>{}, IC<1>{});
    sfor<0, 28>([&](auto UU) {
      constexpr int U = UU.v;
      mmx<U == 0>(c0, wf[U % 3][0], xf[U]);
      if constexpr (U == UH) {
        wvm<0>(gr);
        bar();
        granule(gr, sWS, ftok * 16, tg);
      }
      if constexpr (U + 2 < 28) rd(IC<U + 2>{}, IC<0>{});
      if constexpr (U >= 2 && U < 6) {
        constexpr int A = U - 2;
        const u32x2 t = ldsr<u32x2>(XT + (th * 32 + t32) * 128 + (((4 * kh + A) ^ (t32 & 7)) << 4) + h * 8);
        xv[A >> 1][2 * (A & 1)] = t.x; xv[A >> 1][2 * (A & 1) + 1] = t.y;
      }
      SB();
      mmx<U == 0>(c1, wf[U % 3][1], xf[U]);
      if constexpr (U + 2 < 28) rd(IC<U + 2>{}, IC<1>{});
      if constexpr (U == UH + 1) { r0 = __builtin_amdgcn_rsqf(u2f(gr[2])); m0 = -u2f(gr[0]) * r0; tie(r0); tie(m0); }
      if constexpr (U >= 20) {
        constexpr int I = U - 20;
        float a = fmaf(bflo(xv[I >> 2][I & 3]), r0, m0), b = fmaf(bfhi(xv[I >> 2][I & 3]), r0, m0);
        tie(a); tie(b);
        xn[2 * I] = a; xn[2 * I + 1] = b;
      }
      SB();
    });
  }
  ST(4);
  bar();
  {
    const srd_t sH = mk(p.ws + fresh(OFF_H), 64 * 28 * 1024);
    const int tb = ftok + 1 + fresh(kh);
    const unsigned ob = (unsigned)((tb >> 5) * 28 * 1024 + ((tb & 31) + 32 * h) * 16);
    sfor<10, 14>([&](auto U) { ld16s<(U.v - 10) * 1024>(xf[U.v], ob, sH, (fresh(kh) * 14 + 10) * 1024); });
  }
  cs_load(XT);
  settle(c0, c1);
  epi(IC<0>{}, OFF_QKV + (fresh(T32) * 84 + (g.wtype ? 0 : 56) + 4 * g.s7) * 1024);
  ST(5);
  wvm4<0>(xf[10], xf[11], xf[12], xf[13]);
  bar();
  ST(6);
  c0 = xn;
  {
    a0 = tra(4, RA + fresh(kh) * 14 * 2048, fresh(kh) * 64); a1 = a0 ^ 64u; tie(a1);
    const unsigned hb = lnv(HREG + fresh(w) * 10240);
    u32x4 wf[3][2];
    auto rd = [&](auto UU) {
      if constexpr (UU.v < 10) xf[UU.v] = ldsr<u32x4>(hb + UU.v * 1024);
      wf[UU.v % 3][0] = trfrag(a0 + UU.v * 2048, a0 + UU.v * 2048 + 1024); wf[UU.v % 3][1] = trfrag(a1 + UU.v * 2048, a1 + UU.v * 2048 + 1024);
    };
    rd(IC<0>{}); rd(IC<1>{});
    sfor<0, 14>([&](auto UU) {
      constexpr int U = UU.v;
      mma(c0, wf[U % 3][0], xf[U]);
      if constexpr (U + 2 < 14) rd(IC<U + 2>{});
      mmx<U == 0>(c1, wf[U % 3][1], xf[U]);
      SB();
    });
    settle(c0, c1);
    sfor<0, 4>([&](auto A) { ldsw<f32x4>(hb + A.v * 1024, quad(c1, A.v)); });
  }
  bar();
  ST(27);
  {
    f32x4 o[4];
    const unsigned pb = lnv(HREG + (fresh(w) ^ 2) * 10240);
    sfor<0, 4>([&](auto A) { o[A.v] = ldsr<f32x4>(pb + A.v * 1024); });
    sfor<0, 4>([&](auto A) {
      const f32x4 t = quad(c0, A.v) + o[A.v];
      x1p[A.v >> 1][2 * (A.v & 1)] = pk2(t[0], t[1]);
      x1p[A.v >> 1][2 * (A.v & 1) + 1] = pk2(t[2], t[3]);
    });
    const unsigned so = OFF_X1 + (fresh(tc) * 28 + 4 * g.s7 + 2 * kh) * 1024;
    st16s(x1p[0], sWS, l16, so); st16s<1024>(x1p[1], sWS, l16, so);
    float a1 = 0.f, a2 = 0.f;
    sfor<0, 8>([&](auto I) { a1 = dot2(x1p[I.v >> 2][I.v & 3], ONE2, a1); a2 = dot2(x1p[I.v >> 2][I.v & 3], x1p[I.v >> 2][I.v & 3], a2); });
    st8s(u32x2{f2u(swap_add(a1)), f2u(swap_add(a2))}, sWS, h == 0 ? ftok * 8 : OOB, OFF_ST1 + (2 * fresh(g.s7) + kh) * 2048 * 8);
  }
  ST(7);
  wvm<0>();
  bar();
  ST(8);
  a0 = tra(4, R1, 0); a1 = tra(4, R1, 64);
  xr[0] = lnv(RA + fresh(w) * 4096); xr[1] = lnv(R2 + fresh(w) * 4096);
  pass_a<1024, SlotP3>(c0, c1, a0, a1, xr, xf);
  bar();
  ST(13);
  const GeluK gk3 = ldsr<f32x4>(lnv(R2 + 2048 + fresh(w) * 1024));
  const float mean3 = gk3[3];
  unsigned csb = R2 + 1024 + h * 16;
  tie(csb);
  auto CS3 = [&](int t) { sfor<0, 8>([&](auto A) { cst[A.v] = ldsr<f32x4>(csb + t * 128 + A.v * 32); }); };
  const unsigned so3 = OFF_H3 + (fresh(T32) * 112 + 8 * g.s14) * 1024;
  unsigned pk[4];
  auto G = [&](auto JJ) {
    constexpr int j = JJ.v, t = j >> 3, e = 2 * (j & 7);
    const f32x16& c = t == 0 ? c0 : t == 1 ? c1 : t == 2 ? c2 : c3;
    const f32x2 z = gelu2(f32x2{c[e], c[e + 1]} - f32x2{cst[4 * (t & 1) + (e >> 2)][e & 3], cst[4 * (t & 1) + (e >> 2)][(e & 3) + 1]} * mean3, gk3);
    pk[j & 3] = pk2(z[0], z[1]);
    if constexpr ((j & 3) == 3) st16s<(j >> 2 & 3) * 1024>(u32x4{pk[0], pk[1], pk[2], pk[3]}, sWS, l16, so3 + (j >> 4) * 4096);
  };
  CS3(0);
  settle(c0, c1);
  sfor<0, 16>([&](auto J) { G(J); });
  bar();
  ST(24);
  {
    u32x4 wf[4];
    a0 = tra(4, RA, 0); a1 = tra(4, RA, 64);
    auto rd = [&](auto MM) { const unsigned ad = (MM.v < 28 ? a0 : a1) + (MM.v % 28) * 2048; wf[MM.v & 3] = trfrag(ad, ad + 1024); };
    rd(IC<0>{}); rd(IC<1>{}); rd(IC<2>{});
    sfor<0, 56>([&](auto MM) {
      constexpr int M = MM.v;
      mmx<M % 28 == 0>(M < 28 ? c2 : c3, wf[M & 3], xf[M % 28]);
      if constexpr (M + 3 < 56) rd(IC<M + 3>{});
      if constexpr (M == M3) { wvm<0>(); asm volatile("s_barrier" ::: "memory"); }
      if constexpr (M == 50) CS3(2);
      SB();
    });
  }
  bar();
  ST(14);
  settle(c2, c3);
  sfor<16, 24>([&](auto J) { G(J); });
  ST(15);
  u32x4 af[4][4];
  unsigned av[2] = {lnv(OFF_H3 + fresh(tc) * 112 * 1024), lnv(OFF_H3 + (fresh(tc) ^ 1) * 112 * 1024)}, as;
  auto ACT1 = [&](auto SS, auto II) {
    constexpr int s = SS.v;
    if constexpr (s == 14 && II.v == 0) { av[0] = lnv(OFF_O + fresh(tc) * 32 * 1024); av[1] = lnv(OFF_O + (fresh(tc) ^ 1) * 32 * 1024); }
    if constexpr (II.v == 0) as = s < 14 ? p4_k32(w, s, fresh(g.rot)) * 2048 : (4 * w + ((s + fresh(g.rot)) & 3)) * 2048;
    ld16s<(II.v & 1) * 1024>(af[s & 3][II.v], av[II.v >> 1], sWS, as);
  };
  unsigned bs[4][2];
  sfor<0, 4>([&](auto I) { bs[I.v][0] = tra(4, p4_slot(fresh(w), I.v ? 3 * I.v - 1 : 0), fresh(kh) * 64); bs[I.v][1] = bs[I.v][0] ^ 64u; tie(bs[I.v][1]); });
  u32x4 wf[2][4];
  auto RW = [&](auto SS, auto I) {
    constexpr int j = SS.v % 9;
    const unsigned aa = bs[(j + 1) / 3][I.v & 1] + (j < 2 ? j : (j + 1) % 3) * 4096 + (I.v >> 1) * 2048;
    wf[SS.v & 1][I.v] = trfrag(aa, aa + 1024);
  };
  bar();
  sfor<0, 12>([&](auto K) { ACT1(IC<K.v / 4>{}, IC<K.v % 4>{}); });
  tie(c3);
  sfor<24, 32>([&](auto J) { G(J); });
  ST(16);
  sfor<0, 4>([&](auto I) { RW(IC<0>{}, I); });
  f32x16 d0, d1, d2, d3;
  const unsigned exw = lnv(R1 + fresh(w) * 12288);
  sfor<0, 18>([&](auto SS) {
    constexpr int s = SS.v;
    if constexpr (s == 4 || s == 8 || s == 11 || s == 15) bar();
    if constexpr (s % 3 == 0) ST(17 + s / 3);
    wvm4<(s == 0 ? 10 : s == 1 ? 4 : s <= 15 ? 8 : 4 * (17 - s))>(af[s & 3][0], af[s & 3][1], af[s & 3][2], af[s & 3][3]);
    if constexpr (s == 1) asm volatile("s_barrier" ::: "memory");
    f32x16 &a0 = s < 14 ? c0 : d0, &a1 = s < 14 ? c1 : d1, &a2 = s < 14 ? c2 : d2, &a3 = s < 14 ? c3 : d3;
    auto side = [&](auto KK) {
      constexpr int k = 4 * s + KK.v;
      if constexpr (s + 3 <= 17) ACT1(IC<s + 3>{}, KK);
      if constexpr (k == 56) { settle(c0, c1); settle(c2, c3); }
      if constexpr (k >= 56 && k < 60) sfor<0, 3>([&](auto J) { constexpr int A = 3 * (k - 56) + J.v; ldsw<f32x4>(exw + A * 1024, quad(A < 4 ? c1 : A < 8 ? c2 : c3, A & 3)); });
      if constexpr (k >= 60 && k < 63) {
        constexpr int P = k - 59;
        f32x16& t = P == 1 ? c1 : P == 2 ? c2 : c3;
        const unsigned ep = lnv(R1 + (fresh(w) ^ P) * 12288 + (P - 1) * 4096);
        sfor<0, 4>([&](auto A) { const f32x4 v = ldsr<f32x4>(ep + A.v * 1024); sfor<0, 4>([&](auto I) { t[4 * A.v + I.v] = v[I.v]; }); });
      }
      if constexpr (k >= 64) {
        sfor<0, 2>([&](auto I) {
          constexpr int e = 2 * (k - 64) + I.v;
          const unsigned xp = x1p[e >> 3][(e >> 1) & 3];
          float b = ((e & 1) ? bfhi(xp) : bflo(xp)) + c0[e];
          tie(b);
          b += c1[e]; tie(b); b += c2[e]; tie(b); b += c3[e];
          c0[e] = b * __builtin_amdgcn_rcpf(1.f + __builtin_amdgcn_exp2f(-LOG2E * b));
        });
      }
    };
    sfor<0, 2>([&](auto U) {
      mmx<s % 14 == 0 && U.v == 0>(a0, wf[s & 1][2 * U.v], af[s & 3][U.v]);
      if constexpr (s + 1 < 18) RW(IC<s + 1>{}, IC<2 * U.v>{});
      mmx<s % 14 == 0 && U.v == 0>(a1, wf[s & 1][2 * U.v], af[s & 3][2 + U.v]);
      side(IC<2 * U.v>{});
      SB();
      mmx<s % 14 == 0 && U.v == 0>(a2, wf[s & 1][2 * U.v + 1], af[s & 3][U.v]);
      if constexpr (s + 1 < 18) RW(IC<s + 1>{}, IC<2 * U.v + 1>{});
      mmx<s % 14 == 0 && U.v == 0>(a3, wf[s & 1][2 * U.v + 1], af[s & 3][2 + U.v]);
      side(IC<2 * U.v + 1>{});
      SB();
    });
  });
  ST(23);
  settle(d0, d1); tie(d2); tie(d3);
  const unsigned e3[3] = {lnv(RA + (fresh(w) ^ 1) * 12288), lnv(RA + (fresh(w) ^ 2) * 12288), lnv(RA + (fresh(w) ^ 3) * 12288)};
  unsigned oo = ftok * 896 + h * 16 + fresh(g.s7) * 128 + kh * 64;
  tie(oo);
  sfor<0, 12>([&](auto A) { ldsw<f32x4>(exw - R1 + A.v * 1024, quad(A.v < 4 ? d1 : A.v < 8 ? d2 : d3, A.v & 3)); });
  bar();
  sfor<0, 12>([&](auto A) {
    const f32x4 v = ldsr<f32x4>(e3[A.v / 4] + A.v * 1024);
    sfor<0, 4>([&](auto I) { d0[4 * (A.v & 3) + I.v] += v[I.v]; });
  });
  const srd_t sO = mk(p.out, 2048 * 896);
  sfor<0, 2>([&](auto U) {
    u32x4 ov;
    sfor<0, 4>([&](auto I) {
      constexpr int e = 8 * U.v + 2 * I.v;
      ov[I.v] = pk2(fmaf(d0[e], c0[e], bflo(xv[U.v][I.v])), fmaf(d0[e + 1], c0[e + 1], bfhi(xv[U.v][I.v])));
    });
    __builtin_amdgcn_raw_buffer_store_b128(cross(ov), sO, oo + U.v * 32, 0, 0);
  });
}

DI void spare_path(const Params& p, const int w, const int xc, const int r STARG) {
  const int tid = threadIdx.x, lane = tid & 63, t32 = lane & 31, h = lane >> 5, tb = w & 3, ch = w >> 2;
  constexpr unsigned WPHI = 160768, ZERO = 159744, DUMMY = 158720;
  const int rr = lane >> 3, cp = lane & 7, rs = r - 28;
  const srd_t sWS = mk(p.ws, WS_USED), sX = mk(p.x, 2048 * 896);
  GAS u64* fl = (GAS u64*)(p.ws + OFF_FLG);
  const unsigned srow = 512 * (xc >> 1) + 64 * ((xc & 1) * 4 + rs) + 8 * w + rr;
  u32x2 ev;
  ld8s<0>(ev, 0u, sWS, OFF_FLG);
  u32x4 xr7[7];
  sfor<0, 7>([&](auto K) { ld16p(xr7[K.v], srow * 896 + cp * 16, sX, K.v * 128); });
  const int hd = 2 * xc + (rs >> 1), hf = rs & 1;
  wvm<0>(ev);
  const u64 tag = make_tag(ev);
  const u32x2 tg = {(unsigned)tag, (unsigned)(tag >> 32)};
  {
    float a1 = 0.f, a2 = 0.f;
    sfor<0, 7>([&](auto K) { tie(xr7[K.v]); sfor<0, 4>([&](auto I) { a1 = dot2(xr7[K.v][I.v], ONE2, a1); a2 = dot2(xr7[K.v][I.v], xr7[K.v][I.v], a2); }); });
    sfor<0, 3>([&](auto K) { a1 += __shfl_xor(a1, 1 << K.v); a2 += __shfl_xor(a2, 1 << K.v); });
    const float mean = a1 * (1.f / D), ve = fmaxf(a2 * (1.f / D) - mean * mean, 0.f) + 1e-5f;
    st16s(u32x4{f2u(mean), tg.x, f2u(ve), tg.y}, sWS, cp == 0 ? srow * 16 : OOB, OFF_ST0);
  }
  ST(1);
  const int bi = (xc >> 1) * 4 + rs, par = xc & 1;
  const int q = (lane & 15) >> 2, pp = lane & 3, blk = (lane >> 4) & 1;
  const unsigned acol = (unsigned)((((2 * blk + (pp >> 1)) ^ ((q >> 1) << 2)) << 4) + (pp & 1) * 8);
  const unsigned aperm = (4 * h + q) * 128 + acol;
  const srd_t sW3 = mk(p.w3, 448 * 3584);
  const unsigned wsw = (cp ^ (((rr >> 1) & 1) << 2)) << 4;
  {
    const srd_t s0 = bi < 7 ? mk(p.wqkv, 448 * 2688) : sW3, sP = mk(p.wphi, 16 * 1568);
    const unsigned pitch = bi < 7 ? 2688 : 3584, col = (14 * par + (bi < 7 ? bi : bi - 7)) * 128;
    sfor<0, 7>([&](auto K) { dma16(rr * pitch + wsw, s0, (8 * K.v + w) * 8 * pitch + col, RA + (8 * K.v + w) * 1024); });
    dma16(lane * 16, sP, hd * 1568 + (w & 1) * 1024, WPHI + (w & 1) * 1024);
  }
  if (tid < 16) ldsw<unsigned>(ZERO + tid * 4, 0u);
  auto sums = [&](int cb) {
    wvm<0>();
    bar();
    cs_write(R2 + w * 128, colsum<7>(RA + (aperm ^ (ch * 64)) + tb * 7 * 2048), lane);
    bar();
    if (tb == 0) {
      const f32x16 cs = cs_read(R2 + ch * 512 + h * 16, 128);
      sfor<0, 4>([&](auto A) { st16s<A.v * 32>(__builtin_bit_cast(u32x4, quad(cs, A.v)), sWS, t32 == 0 ? h * 16 : OOB, OFF_CS3 + cb * 256 + ch * 128); });
      wvm<0>();
    }
    bar();
    publish(sWS, tg, FL_CS + cb, tid == 0);
  };
  sums(bi < 7 ? 28 + 7 * par + bi : 14 * par + bi - 7);
  ST(9);
  if (bi < 5) {
    sfor<0, 7>([&](auto K) { dma16(rr * 3584 + wsw, sW3, (8 * K.v + w) * 8 * 3584 + (14 * par + 9 + bi) * 128, RA + (8 * K.v + w) * 1024); });
    sums(14 * par + 9 + bi);
  }
  ST(2);
  const int gq = 8 * hf + w;
  const unsigned qkvs = OFF_QKV + gq * 4 * 84 * 1024;
  const int b01 = (28 * hd + (lane & 1) * 27) >> 6;
  const int U0 = (28 * hd) >> 4, o = (28 * hd) & 15;
  const unsigned f2 = (o >= 8 ? 2 : 0) * 1024;
  const unsigned img = w * 18432;
  u32x4 wp[3];
  unsigned vw[6];
  sfor<0, 3>([&](auto I) {
    unsigned a[2];
    sfor<0, 2>([&](auto T) {
      const int d0 = 16 * I.v + 8 * T.v + 4 * h - o;
      const bool ok = d0 >= 0 && d0 < 28;
      a[T.v] = (ok ? WPHI + (d0 + q) * 56 : ZERO) + (16 * blk + 4 * pp) * 2;
      vw[2 * I.v + T.v] = ok ? img + 16384 + t32 * 64 + (((d0 >> 2) ^ ((t32 >> 1) & 7)) << 3) : DUMMY + lane * 8;
    });
    wp[I.v] = trfrag(a[0], a[1]);
  });
  u32x4 kp[4][3], vp[4][3], qp[4][3];
  auto gather = [&](u32x4 (&d)[4][3], unsigned base) {
    sfor<0, 4>([&](auto T) {
      const unsigned so = qkvs + (T.v * 84 + base + U0) * 1024;
      ld16s<0>(d[T.v][0], lane * 16, sWS, so); ld16s<1024>(d[T.v][1], lane * 16, sWS, so); ld16s<0>(d[T.v][2], lane * 16, sWS, so + f2);
    });
  };
  auto feat = [&](u32x4 (&k)[3], u32x4 (&out)[4]) {
    sfor<0, 3>([&](auto I) { tie(k[I.v]); });
    f32x16 f = mfma(wp[0], k[0], zero16());
    f = mfma(wp[1], k[1], f);
    f = mfma(wp[2], k[2], f);
    dual_softmax(f, h != 0, out);
  };
  const unsigned fq = gq * 14 + b01;
  poll(fl, FL_A + fq + 7, tag);
  ST(3);
  gather(kp, 28);
  if (h) ldsw<u32x2>(img + 16384 + t32 * 64 + ((7 ^ ((t32 >> 1) & 7)) << 3), u32x2{0x3f80u, 0u});
  sfor<0, 4>([&](auto T) {
    wvm<9 - 3 * T.v>();
    u32x4 ph[4];
    feat(kp[T.v], ph);
    const unsigned pr = img + T.v * 4096 + t32 * 128, sw = h ^ (((t32 >> 1) & 1) << 3) ^ (t32 & 1) ^ (((t32 >> 2) & 3) << 1);
    sfor<0, 8>([&](auto A) { ldsw<u32x2>(pr + (((2 * A.v) ^ sw) << 3), u32x2{ph[A.v >> 1][2 * (A.v & 1)], ph[A.v >> 1][2 * (A.v & 1) + 1]}); });
  });
  ST(4);
  poll(fl, FL_Q + fq + ((lane & 2) ? 7 : 0), tag);
  ST(5);
  gather(qp, 0);
  gather(vp, 56);
  u32x4 phq[4][4];
  wvm<21>();
  feat(qp[0], phq[0]);
  f32x16 kvp = zero16(), kvm = zero16();
  unsigned aa[2], ba[2];
  sfor<0, 2>([&](auto T) {
    const int row = 8 * h + 4 * T.v + q, pc = 4 * blk + pp;
    aa[T.v] = img + row * 128 + ((pc ^ ((q >> 1) << 3) ^ (q & 1) ^ ((2 * h + T.v) << 1)) << 3);
    ba[T.v] = img + 16384 + row * 64 + ((pc ^ (row >> 1)) << 3);
  });
  sfor<0, 4>([&](auto T) {
    wvm<9 - 3 * T.v>();
    sfor<0, 3>([&](auto I) { tie(vp[T.v][I.v]); });
    sfor<0, 6>([&](auto A) { ldsw<u32x2>(vw[A.v], u32x2{vp[T.v][A.v >> 1][2 * (A.v & 1)], vp[T.v][A.v >> 1][2 * (A.v & 1) + 1]}); });
    sfor<0, 2>([&](auto U) {
      constexpr unsigned oa = T.v * 4096 + U.v * 2048;
      const u32x4 bf = trfrag(ba[0] + U.v * 1024, ba[1] + U.v * 1024);
      kvp = mfma(trfrag(aa[0] + oa, aa[1] + oa), bf, kvp);
      kvm = mfma(trfrag((aa[0] ^ 64u) + oa, (aa[1] ^ 64u) + oa), bf, kvm);
    });
  });
  sfor<0, 8>([&](auto A) { ldsw<f32x4>(img + A.v * 1024 + lane * 16, quad(A.v < 4 ? kvp : kvm, A.v & 3)); });
  bar();
  f32x4 own = {0.f, 0.f, 0.f, 0.f};
  sfor<0, 8>([&](auto S) { own += ldsr<f32x4>(S.v * 18432 + w * 1024 + lane * 16); });
  st16s(__builtin_bit_cast(u32x4, own), sWS, lane * 16, OFF_KVP + ((hd * 2 + hf) * 8 + w) * 1024);
  feat(qp[1], phq[1]);
  wvm<0>();
  bar();
  publish(sWS, tg, FL_KV + hd * 2 + hf, tid == 0);
  ST(6);
  feat(qp[2], phq[2]);
  poll(fl, FL_KV + hd * 2 + (hf ^ 1), tag);
  u32x4 oth;
  ld16s<0>(oth, lane * 16, sWS, OFF_KVP + ((hd * 2 + (hf ^ 1)) * 8 + w) * 1024);
  feat(qp[3], phq[3]);
  wvm<0>(oth);
  own += __builtin_bit_cast(f32x4, oth);
  ldsw<u32x2>(147456 + (w >> 1) * 1024 + lane * 16 + (w & 1) * 8, u32x2{pk2(own[0], own[1]), pk2(own[2], own[3])});
  bar();
  ST(7);
  u32x4 kvf[4];
  sfor<0, 4>([&](auto I) { kvf[I.v] = ldsr<u32x4>(147456 + I.v * 1024 + lane * 16); });
  sfor<0, 4>([&](auto T) {
    f32x16 n = mfma(kvf[0], phq[T.v][0], zero16());
    n = mfma(kvf[1], phq[T.v][1], n);
    n = mfma(kvf[2], phq[T.v][2], n);
    n = mfma(kvf[3], phq[T.v][3], n);
    const float rd = __builtin_amdgcn_rcpf(swap_hi(n[12]) + 1e-6f);
#pragma unroll
    for (int i = 0; i < 16; ++i) n[i] *= rd;
    u32x4 o1 = pack8(n, 1);
    if (h) { o1[2] = 0u; o1[3] = 0u; }
    const unsigned so = OFF_O + ((4 * gq + T.v) * 32 + 2 * hd) * 1024;
    st16s(pack8(n, 0), sWS, lane * 16, so); st16s<1024>(o1, sWS, lane * 16, so);
  });
  wvm<0>();
  publish(sWS, tg, FL_O + gq * 16 + hd, lane == 0);
  ST(8);
  if (xc == 0 && rs == 0 && w == 0) {
    sfor<0, 4>([&](auto I) { poll(fl, FL_Q + min(64 * I.v + lane, 223), tag); });
    publish(sWS, tg, FL_E, lane == 0);
  }
}

__global__ __launch_bounds__(512, 2) void fused(Params p) {
  u64 pc;
  asm volatile("s_getpc_b64 %0" : "=s"(pc));
  ST(0);
  int w = __builtin_amdgcn_readfirstlane(threadIdx.x >> 6);
  asm volatile("s_nop 4" : "+s"(w));
  const int xc = blockIdx.x & 7, r = blockIdx.x >> 3;
  unsigned dummy = 0;
  if (r < 28 && w >= 4) first_loads(p, w - 4, xc, r);
  if (r < 28 && w < 4) code_prefetch(dummy, pc, (((r & 1) * 4 + w) * 64 + (threadIdx.x & 63)) * 128);
  SB();
  if (r < 28) {
    if (w < 4) compute_path(p, dummy, w, xc, r STPASS);
    else loader_path(p, w - 4, xc, r STPASS);
  }
  else spare_path(p, w, xc, r STPASS);
}

extern "C" void kernel_launch(void* const* d_in, const int* in_sizes, int n_in, void* d_out, int out_size, void* d_ws, size_t ws_size,
                              hipStream_t stream) {
  Params p;
  p.x = (const uint16_t*)d_in[0]; p.wqkv = (const uint16_t*)d_in[1]; p.wphi = (const uint16_t*)d_in[2]; p.wo = (const uint16_t*)d_in[3];
  p.w1 = (const uint16_t*)d_in[5]; p.w2 = (const uint16_t*)d_in[6]; p.w3 = (const uint16_t*)d_in[7]; p.w4 = (const uint16_t*)d_in[8];
  p.out = (uint16_t*)d_out; p.ws = (char*)d_ws;
  (void)hipFuncSetAttribute((const void*)fused, hipFuncAttributeMaxDynamicSharedMemorySize, LDS_BYTES);
  fused<<<256, 512, LDS_BYTES, stream>>>(p);
}
```

```cpp
#include <hip/hip_runtime.h>
#include <stdint.h>

#define LAS __attribute__((address_space(3)))
#define GAS __attribute__((address_space(1)))
#define DI __device__ __forceinline__
typedef short s16x4 __attribute__((ext_vector_type(4)));
typedef __bf16 bf16x8 __attribute__((ext_vector_type(8)));
typedef __bf16 bf16x2 __attribute__((ext_vector_type(2)));
typedef float f32x16 __attribute__((ext_vector_type(16)));
typedef float f32x4 __attribute__((ext_vector_type(4)));
typedef float f32x2 __attribute__((ext_vector_type(2)));
typedef unsigned u32x4 __attribute__((ext_vector_type(4)));
typedef unsigned u32x2 __attribute__((ext_vector_type(2)));
typedef unsigned long long u64;
typedef __amdgpu_buffer_rsrc_t srd_t;

constexpr int D = 448;
constexpr float LOG2E = 1.4426950408889634f;
constexpr unsigned OFF_FLG = 0;
constexpr unsigned OFF_H = 262144;
constexpr unsigned OFF_X1 = OFF_H + 64 * 28 * 1024;
constexpr unsigned OFF_QKV = OFF_X1 + 64 * 28 * 1024;
constexpr unsigned OFF_H3 = OFF_QKV + 64 * 84 * 1024;
constexpr unsigned OFF_O = OFF_H3 + 64 * 112 * 1024;
constexpr unsigned OFF_KVP = OFF_O + 64 * 32 * 1024;
constexpr unsigned OFF_ST0 = OFF_KVP + 32 * 8192;
constexpr unsigned OFF_ST1 = OFF_ST0 + 2048 * 16;
constexpr unsigned OFF_CS3 = OFF_ST1 + 14 * 2048 * 8;
constexpr unsigned WS_USED = OFF_CS3 + 42 * 256;
constexpr size_t OFF_STAMP = 64u << 20;
constexpr int FL_E = 0, FL_A = 1, FL_Q = FL_A + 16 * 14, FL_X = FL_Q + 16 * 14, FL_3A = FL_X + 32 * 7, FL_3B = FL_3A + 16 * 14,
              FL_O = FL_3B + 16 * 14, FL_KV = FL_O + 256, FL_CS = FL_KV + 32;
static_assert((FL_CS + 42) * 128 <= 262144, "flag block");
constexpr unsigned RA = 0, R1 = 57344, R2 = 114688, LDS_BYTES = 163840;
constexpr unsigned OOB = 0x80000000u;
constexpr unsigned CODE_BYTES = 48128;
constexpr unsigned SPIN_CAP = 1u << 18;

struct Params {
  const uint16_t *x, *wqkv, *wphi, *wo, *w1, *w2, *w3, *w4;
  uint16_t* out;
  char* ws;
};

template <int I> struct IC { static constexpr int v = I; };
template <int B, int E, class F> DI void sfor(F&& f) {
  if constexpr (B < E) { f(IC<B>{}); sfor<B + 1, E>(f); }
}
DI f32x16 mfma(u32x4 a, u32x4 b, f32x16 c) {
  return __builtin_amdgcn_mfma_f32_32x32x16_bf16(__builtin_bit_cast(bf16x8, a), __builtin_bit_cast(bf16x8, b), c, 0, 0, 0);
}
DI void mma(f32x16& c, u32x4 a, u32x4 b) { asm volatile("v_mfma_f32_32x32x16_bf16 %0, %1, %2, %0" : "+v"(c) : "v"(a), "v"(b) : "memory"); }
DI void mma0(f32x16& c, u32x4 a, u32x4 b) { asm volatile("v_mfma_f32_32x32x16_bf16 %0, %1, %2, 0" : "=&v"(c) : "v"(a), "v"(b) : "memory"); }
template <bool FIRST> DI void mmx(f32x16& c, u32x4 a, u32x4 b) { if constexpr (FIRST) mma0(c, a, b); else mma(c, a, b); }
DI void settle(f32x16& a, f32x16& b) { asm volatile("s_nop 11" : "+v"(a), "+v"(b)); }
DI unsigned pk2(float a, float b) {
  f32x2 v = {a, b};
  return __builtin_bit_cast(unsigned, __builtin_convertvector(v, bf16x2));
}
DI float u2f(unsigned u) { return __builtin_bit_cast(float, u); }
DI unsigned f2u(float f) { return __builtin_bit_cast(unsigned, f); }
DI float bflo(unsigned u) { return __builtin_bit_cast(float, u << 16); }
DI float bfhi(unsigned u) { return __builtin_bit_cast(float, u & 0xffff0000u); }
DI u32x4 trfrag(unsigned a0, unsigned a1) {
  u32x2 lo = __builtin_bit_cast(u32x2, __builtin_amdgcn_ds_read_tr16_b64_v4i16((LAS s16x4*)(uintptr_t)a0));
  u32x2 hi = __builtin_bit_cast(u32x2, __builtin_amdgcn_ds_read_tr16_b64_v4i16((LAS s16x4*)(uintptr_t)a1));
  return u32x4{lo.x, lo.y, hi.x, hi.y};
}
template <class T> DI T ldsr(unsigned a) { return *(LAS T*)(uintptr_t)a; }
template <class T> DI void ldsw(unsigned a, T v) { *(LAS T*)(uintptr_t)a = v; }
DI srd_t mk(const void* p, unsigned bytes) { return __builtin_amdgcn_make_buffer_rsrc((void*)p, 0, (int)bytes, 0x00020000); }
DI void dma16(unsigned voff, srd_t r, unsigned soff, unsigned lds) {
  asm volatile("s_mov_b32 m0, %3\n\ts_nop 0\n\tbuffer_load_dwordx4 %0, %1, %2 offen lds" ::"v"(voff), "s"(r), "s"(soff), "s"(lds) : "memory");
}
DI void dma16s(unsigned voff, srd_t r, unsigned soff, unsigned lds) {
  asm volatile("s_mov_b32 m0, %3\n\ts_nop 0\n\tbuffer_load_dwordx4 %0, %1, %2 offen sc1 lds" ::"v"(voff), "s"(r), "s"(soff), "s"(lds) : "memory");
}
DI void ld16p(u32x4& d, unsigned voff, srd_t r, unsigned soff) {
  asm volatile("buffer_load_dwordx4 %0, %1, %2, %3 offen" : "=v"(d) : "v"(voff), "s"(r), "s"(soff) : "memory");
}
template <int IMM> DI void ld16s(u32x4& d, unsigned voff, srd_t r, unsigned soff) {
  asm volatile("buffer_load_dwordx4 %0, %1, %2, %3 offen offset:%c4 sc1" : "=v"(d) : "v"(voff), "s"(r), "s"(soff), "i"(IMM) : "memory");
}
template <int IMM> DI void ld8s(u32x2& d, unsigned voff, srd_t r, unsigned soff) {
  asm volatile("buffer_load_dwordx2 %0, %1, %2, %3 offen offset:%c4 sc1" : "=v"(d) : "v"(voff), "s"(r), "s"(soff), "i"(IMM) : "memory");
}
template <int IMM = 0> DI void st16s(u32x4 v, srd_t r, unsigned voff, unsigned soff) {
  asm volatile("buffer_store_dwordx4 %0, %1, %2, %3 offen offset:%c4 sc1\n\ts_nop 1" ::"v"(v), "v"(voff), "s"(r), "s"(soff), "i"(IMM) : "memory");
}
DI void st8s(u32x2 v, srd_t r, unsigned voff, unsigned soff) {
  asm volatile("buffer_store_dwordx2 %0, %1, %2, %3 offen sc1" ::"v"(v), "v"(voff), "s"(r), "s"(soff) : "memory");
}
template <int N> DI void wvm() { asm volatile("s_waitcnt vmcnt(%c0)" ::"i"(N) : "memory"); }
template <class T> DI void tie(T& a) { asm volatile("" : "+v"(a)); }
template <int N, class T> DI void wvm(T& a) { asm volatile("s_waitcnt vmcnt(%c1)" : "+v"(a) : "i"(N) : "memory"); }
DI void wlg4(u32x4& a, u32x4& b, u32x4& c, u32x4& d) { asm volatile("s_waitcnt lgkmcnt(0)" : "+v"(a), "+v"(b), "+v"(c), "+v"(d)::"memory"); }
template <int N> DI void wvm4(u32x4& a, u32x4& b, u32x4& c, u32x4& d) {
  asm volatile("s_waitcnt vmcnt(%c4)" : "+v"(a), "+v"(b), "+v"(c), "+v"(d) : "i"(N) : "memory");
}
template <int N> DI void wvm2(u32x4& a, u32x4& b) { asm volatile("s_waitcnt vmcnt(%c2)" : "+v"(a), "+v"(b) : "i"(N) : "memory"); }
DI int fresh(int v) { asm volatile("" : "+s"(v)); return v; }
DI unsigned rtn() { u64 t; asm volatile("s_memrealtime %0\n\ts_waitcnt lgkmcnt(0)" : "=s"(t) :: "memory"); return (unsigned)t; }
DI void bar() { asm volatile("s_waitcnt lgkmcnt(0)\n\ts_barrier" ::: "memory"); }
#define SB() __builtin_amdgcn_sched_barrier(0)
DI void halves(float v, float& lo, float& hi) {
  auto r = __builtin_amdgcn_permlane32_swap(f2u(v), f2u(v), false, false);
  lo = u2f(r[0]); hi = u2f(r[1]);
}
DI float swap_add(float v) { float a, b; halves(v, a, b); return a + b; }
DI float swap_max(float v) { float a, b; halves(v, a, b); return fmaxf(a, b); }
DI float swap_hi(float v) { float a, b; halves(v, a, b); return b; }
typedef f32x4 GeluK;
DI GeluK gelu_k(float r, float ir) {
  const float c0 = -2.f * 0.7978845608028654f * LOG2E, c1 = c0 * 0.044715f;
  return GeluK{c0 * r, c1 * r * r * r, ir, 0.f};
}
DI f32x2 gelu2(f32x2 z, GeluK k) {
  const f32x2 u = z * (z * z * k[1] + k[0]);
  const f32x2 e = {__builtin_amdgcn_exp2f(u[0]), __builtin_amdgcn_exp2f(u[1])};
  const f32x2 d = e * k[2] + k[2];
  return z * f32x2{__builtin_amdgcn_rcpf(d[0]), __builtin_amdgcn_rcpf(d[1])};
}
DI float dot2(unsigned a, unsigned b, float c) {
  return __builtin_amdgcn_fdot2_f32_bf16(__builtin_bit_cast(bf16x2, a), __builtin_bit_cast(bf16x2, b), c, false);
}
constexpr unsigned ONE2 = 0x3f803f80u;
DI f32x16 zero16() { f32x16 z; for (int i = 0; i < 16; ++i) z[i] = 0.f; return z; }
DI u32x4 pack8(const f32x16& a, int u) {
  return u32x4{pk2(a[8 * u], a[8 * u + 1]), pk2(a[8 * u + 2], a[8 * u + 3]), pk2(a[8 * u + 4], a[8 * u + 5]), pk2(a[8 * u + 6], a[8 * u + 7])};
}
DI void poll(GAS u64* fl, int n, int idx, u64 tag) {
  const bool on = (int)(threadIdx.x & 63) < n;
#pragma unroll 1
  for (unsigned spins = 0; spins < SPIN_CAP; ++spins) {
    u64 v = tag;
    if (on) v = __hip_atomic_load(fl + idx * 16, __ATOMIC_RELAXED, __HIP_MEMORY_SCOPE_AGENT);
    if (__all(v == tag)) break;
    __builtin_amdgcn_s_sleep(1);
  }
  wvm<0>();
}
DI void publish(srd_t ws, u32x2 tg, unsigned idx, bool on) { st8s(tg, ws, on ? idx * 128 : OOB, OFF_FLG); }
DI bool all_set(u32x2 f, u32x2 tg) { return __all(f.x == tg.x && f.y == tg.y); }

#define ST(i) do {} while (0)

template <int NU> DI f32x16 colsum(unsigned a) {
  const u32x4 ones = {ONE2, ONE2, ONE2, ONE2};
  f32x16 c = zero16();
  sfor<0, NU>([&](auto UU) { c = mfma(trfrag(a + UU.v * 2048, a + UU.v * 2048 + 1024), ones, c); });
  return c;
}
DI void cs_write(unsigned dst, const f32x16& c, int lane) {
  if ((lane & 31) == 0) {
    const unsigned d = dst + (lane >> 5) * 16;
    sfor<0, 4>([&](auto A) { ldsw<f32x4>(d + A.v * 32, f32x4{c[4 * A.v], c[4 * A.v + 1], c[4 * A.v + 2], c[4 * A.v + 3]}); });
  }
}
DI float max3(float a, float b, float c) { float r; asm("v_max3_f32 %0, %1, %2, %3" : "=v"(r) : "v"(a), "v"(b), "v"(c)); return r; }
DI float min3(float a, float b, float c) { float r; asm("v_min3_f32 %0, %1, %2, %3" : "=v"(r) : "v"(a), "v"(b), "v"(c)); return r; }
DI void dual_softmax(f32x16 p, bool hi, u32x4 (&o)[4]) {
  p[0] += 0.f;
#pragma unroll
  for (int i = 12; i < 16; ++i) p[i] = hi ? p[0] : p[i];
  float mx = max3(p[0], p[1], p[2]), mn = min3(p[0], p[1], p[2]);
#pragma unroll
  for (int i = 3; i < 15; i += 2) { mx = max3(mx, p[i], p[i + 1]); mn = min3(mn, p[i], p[i + 1]); }
  float a, b;
  halves(max3(mx, p[15], p[15]), a, b);
  const float cp = -LOG2E * max3(a, b, b);
  halves(min3(mn, p[15], p[15]), a, b);
  const float cm = LOG2E * min3(a, b, b);
  f32x2 ep[8], em[8];
#pragma unroll
  for (int i = 0; i < 8; ++i) {
    const f32x2 v = {p[2 * i], p[2 * i + 1]}, x = v * LOG2E + cp, y = v * -LOG2E + cm;
    ep[i] = f32x2{__builtin_amdgcn_exp2f(x[0]), __builtin_amdgcn_exp2f(x[1])};
    em[i] = f32x2{__builtin_amdgcn_exp2f(y[0]), __builtin_amdgcn_exp2f(y[1])};
  }
  const float keep = hi ? 0.f : 1.f;
  const f32x2 sp = ((ep[0] + ep[1]) + (ep[2] + ep[3])) + ((ep[4] + ep[5]) + (ep[6] + ep[7]) * keep);
  const f32x2 sn = ((em[0] + em[1]) + (em[2] + em[3])) + ((em[4] + em[5]) + (em[6] + em[7]) * keep);
  const float rp = __builtin_amdgcn_rcpf(swap_add(sp[0] + sp[1])), rm = __builtin_amdgcn_rcpf(swap_add(sn[0] + sn[1]));
#pragma unroll
  for (int i = 0; i < 8; ++i) {
    const f32x2 x = ep[i] * (i < 6 ? rp : rp * keep), y = em[i] * (i < 6 ? rm : rm * keep);
    o[i >> 2][i & 3] = pk2(x[0], x[1]);
    o[2 + (i >> 2)][i & 3] = pk2(y[0], y[1]);
  }
}
DI void granule(u32x4& g, srd_t ws, unsigned voff, u32x2 tg) {
#pragma unroll 1
  for (unsigned spins = 0; spins < SPIN_CAP && !__all(g[1] == tg.x && g[3] == tg.y); ++spins) {
    ld16s<0>(g, voff, ws, OFF_ST0);
    wvm<0>(g);
  }
}
DI f32x16 cs_read(unsigned a, unsigned str) {
  f32x4 v[4][4];
  sfor<0, 4>([&](auto P) { sfor<0, 4>([&](auto A) { v[P.v][A.v] = ldsr<f32x4>(a + P.v * str + A.v * 32); }); });
  f32x16 r;
  sfor<0, 4>([&](auto A) {
    const f32x4 t = (v[0][A.v] + v[1][A.v]) + (v[2][A.v] + v[3][A.v]);
    sfor<0, 4>([&](auto I) { r[4 * A.v + I.v] = t[I.v]; });
  });
  return r;
}
DI f32x4 quad(const f32x16& c, int a) { return f32x4{c[4 * a], c[4 * a + 1], c[4 * a + 2], c[4 * a + 3]}; }
DI u32x4 cross(u32x4 v) {
  auto a = __builtin_amdgcn_permlane32_swap(v[0], v[2], false, false);
  auto b = __builtin_amdgcn_permlane32_swap(v[1], v[3], false, false);
  return u32x4{a[0], b[0], a[1], b[1]};
}

#define STARG , unsigned& tp, const unsigned t0
#define STPASS , tp, t0
DI void code_prefetch(unsigned& d, u64 pc, unsigned voff) {
  asm volatile("buffer_load_dword %0, %1, %2, 0 offen" : "=v"(d) : "v"(voff), "s"(mk((const void*)(pc & ~127ull), CODE_BYTES)) : "memory");
}
DI u64 make_tag(u32x2 ev) {
  return (((u64)(unsigned)__builtin_amdgcn_readfirstlane(ev.y) << 32) | (unsigned)__builtin_amdgcn_readfirstlane(ev.x)) + 0x9E3779B97F4A7C15ull;
}

struct SlotP1 {
  static constexpr unsigned off(int c) { return R2 - 16384 + ((c + 1) % 4) * 16384; }
  static DI unsigned addr(const unsigned (&xr)[4], int U) { return xr[U & 3] + ((U / 4 + 1) % 4) * 16384; }
};
struct SlotP3 {
  static constexpr unsigned off(int c) { return c < 3 ? RA + c * 16384 : c < 6 ? R2 + (c - 3) * 16384 : RA; }
  static DI unsigned addr(const unsigned (&xr)[4], int U) { return xr[U >= 12 && U < 24] + (U / 4 % 3) * 16384 + (U & 3) * 1024; }
};
constexpr unsigned HREG = R2;
constexpr unsigned XT = R2 + 40960;
constexpr int UH = 12;
constexpr int M3 = 20;
template <int TS, class Slot>
DI void pass_a(unsigned& tp, const unsigned t0, f32x16& c0, f32x16& c1, const unsigned a0, const unsigned a1, const unsigned (&xr)[4], u32x4* xf) {
  auto rx = [&](auto UU) { xf[UU.v] = ldsr<u32x4>(Slot::addr(xr, UU.v)); };
  u32x4 wf[3][2];
  auto rd = [&](auto UU, auto NN) {
    const unsigned ad = (NN.v ? a1 : a0) + UU.v * 2048;
    wf[UU.v % 3][NN.v] = trfrag(ad, ad + TS);
  };
  bar();
  tp = rtn() - t0;
  rx(IC<0>{}); rd(IC<0>{}, IC<0>{}); rd(IC<0>{}, IC<1>{}); rx(IC<1>{}); rd(IC<1>{}, IC<0>{}); rd(IC<1>{}, IC<1>{});
  sfor<0, 28>([&](auto UU) {
    constexpr int U = UU.v;
    if constexpr (U % 4 == 2 && U < 26) asm volatile("s_barrier" ::: "memory");
    mmx<U == 0>(c0, wf[U % 3][0], xf[U]);
    if constexpr (U + 2 < 28) { rx(IC<U + 2>{}); rd(IC<U + 2>{}, IC<0>{}); }
    SB();
    mmx<U == 0>(c1, wf[U % 3][1], xf[U]);
    if constexpr (U + 2 < 28) rd(IC<U + 2>{}, IC<1>{});
    SB();
  });
}
DI int rot7(int c, int rot) { const int v = c + rot; return v >= 7 ? v - 7 : v; }
struct Geo {
  int r7, s7, g4, tt, s14, rot;
  bool wtype;
  DI Geo(int xc, int r) {
    r7 = r / 7;
    s7 = r - 7 * r7;
    g4 = 4 * (xc >> 1) + r7;
    tt = 4 * xc + r7;
    s14 = 7 * (xc & 1) + s7;
    rot = (s7 + 2 * r7) % 7;
    wtype = (xc & 1) == 0;
  }
};
DI unsigned p4_slot(int kq, int j) {
  return j < 2 ? R2 + 16384 + (2 * kq + j) * 4096 : j < 5 ? R1 + (3 * kq + j - 2) * 4096 : j < 8 ? RA + (3 * kq + j - 5) * 4096 : (kq < 2 ? R1 : RA) + 49152 + (kq & 1) * 4096;
}
DI int p4_k32(int kq, int s, int rot) {
  const int a = 7 * kq + rot7(s < 7 ? s : s - 7, rot);
  return 4 * (a >> 1) + (a & 1) + (s < 7 ? 0 : 2);
}

DI void first_loads(const Params& p, const int w, const int xc, const int r) {
  const int lane = threadIdx.x & 63;
  const Geo g(xc, r);
  const int rr = lane >> 3, cp = lane & 7;
  const unsigned wsw = (unsigned)((cp ^ (((rr >> 1) & 1) << 2)) << 4);
  const srd_t sX = mk(p.x, 2048 * 896), sQ = mk(p.wqkv, 448 * 2688);
  const srd_t sA = g.wtype ? mk(p.w1, 448 * 896) : sQ;
  const unsigned pitchA = g.wtype ? 896 : 2688;
  const unsigned colA = g.wtype ? g.s7 : 7 + g.s7;
  const unsigned wvA = rr * pitchA + wsw;
  const unsigned xv0 = rr * 896 + ((cp ^ (rr >> 1)) << 4), xv1 = xv0 ^ 64u;
  const unsigned T32 = 4 * g.g4 + w, xs = T32 * 32 * 896;
  auto G1 = [&](int c) {
    const int cg = rot7(c, g.rot);
    sfor<0, 2>([&](auto J) { dma16(wvA, sA, (8 * cg + 2 * w + J.v) * 8 * pitchA + colA * 128, RA + (8 * c + 2 * w + J.v) * 1024); });
    sfor<0, 4>([&](auto I) { dma16((I.v & 1) ? xv1 : xv0, sX, xs + I.v * 8 * 896 + cg * 128, SlotP1::off(c) + w * 4096 + I.v * 1024); });
  };
  __builtin_amdgcn_s_setprio(3);
  G1(0); G1(1);
}

DI void loader_path(const Params& p, const int w, const int xc, const int r STARG) {
  const int lane = threadIdx.x & 63, t32 = lane & 31, h = lane >> 5;
  const Geo g(xc, r);
  const int rr = lane >> 3, cp = lane & 7;
  const unsigned wsw = (unsigned)((cp ^ (((rr >> 1) & 1) << 2)) << 4);
  const srd_t sWS = mk(p.ws, WS_USED), sX = mk(p.x, 2048 * 896), sQ = mk(p.wqkv, 448 * 2688);
  const srd_t sA = g.wtype ? mk(p.w1, 448 * 896) : sQ;
  const unsigned pitchA = g.wtype ? 896 : 2688;
  const unsigned colA = g.wtype ? g.s7 : 7 + g.s7, colB = g.wtype ? g.s7 : 14 + g.s7;
  const unsigned wvA = rr * pitchA + wsw;
  const unsigned xv0 = rr * 896 + ((cp ^ (rr >> 1)) << 4), xv1 = xv0 ^ 64u;
  const unsigned T32 = 4 * g.g4 + w, xs = T32 * 32 * 896;
  auto G1 = [&](int c) {
    const int cg = rot7(c, g.rot);
    sfor<0, 2>([&](auto J) { dma16(wvA, sA, (8 * cg + 2 * w + J.v) * 8 * pitchA + colA * 128, RA + (8 * c + 2 * w + J.v) * 1024); });
    sfor<0, 4>([&](auto I) { dma16((I.v & 1) ? xv1 : xv0, sX, xs + I.v * 8 * 896 + cg * 128, SlotP1::off(c) + w * 4096 + I.v * 1024); });
  };
  u32x2 ev;
  ld8s<0>(ev, 0u, sWS, OFF_FLG);
  SB();
  const unsigned wv896 = rr * 896 + wsw, wv3 = rr * 3584 + wsw, wvB = rr * 2688 + wsw;
  auto WB = [&](int k) { dma16(wvB, sQ, (8 * rot7(k >> 1, g.rot) + 4 * (k & 1) + w) * 8 * 2688 + colB * 128, R1 + (4 * k + w) * 1024); };
  const int q = (lane & 15) >> 2, pp = lane & 3, blk = (lane >> 4) & 1;
  const unsigned ca = (4 * h + q) * 128 + (unsigned)((((2 * blk + (pp >> 1)) ^ ((q >> 1) << 2)) << 4) + (pp & 1) * 8);
  const u32x4 ones = {ONE2, ONE2, ONE2, ONE2};
  f32x16 s0 = zero16(), s1 = zero16();
  auto CS = [&](unsigned a) { s0 = mfma(trfrag(a, a + 1024), ones, s0); s1 = mfma(trfrag(a ^ 64u, (a ^ 64u) + 1024), ones, s1); };
  const unsigned cw = RA + ca + w * 2048;
  u32x4 gr;
  wvm<7>(); bar(); G1(2); CS(cw);
  wvm<7>(); bar(); G1(3); CS(cw + 8192);
  wvm<6>(ev); bar(); G1(4); CS(cw + 2 * 8192);
  wvm<6>(); bar(); G1(5); CS(cw + 3 * 8192);
  ld16s<0>(gr, (T32 * 32 + t32) * 16, sWS, OFF_ST0);
  wvm<7>(); bar(); G1(6); CS(cw + 4 * 8192);
  const u64 tag = make_tag(ev);
  const u32x2 tg = {(unsigned)tag, (unsigned)(tag >> 32)};
  wvm<6>(gr); bar(); sfor<0, 3>([&](auto K) { WB(K.v); });
  CS(cw + 5 * 8192);
  granule(gr, sWS, (T32 * 32 + t32) * 16, tg);
  const float ve1 = u2f(gr[2]);
  float r1 = __builtin_amdgcn_rsqf(ve1);
  const float v2 = (ve1 - 1e-5f) * r1 * r1 + 1e-5f;
  GeluK k1 = gelu_k(r1 * __builtin_amdgcn_rsqf(v2), __builtin_amdgcn_sqrtf(ve1 * v2));
  k1[3] = u2f(gr[0]);
  tie(k1); tie(r1);
  cs_write(R2 - 16384 + w * 128, s0, lane); cs_write(R2 - 16384 + 512 + w * 128, s1, lane);
  wvm<3>(); bar();
  {
    const unsigned a = R2 - 16384 + h * 512 + t32 * 4, c6 = (RA + ca + 24 * 2048) ^ ((w & 1) * 64);
    u32x4 f[4];
    sfor<0, 4>([&](auto U) { f[U.v] = trfrag(c6 + U.v * 2048, c6 + U.v * 2048 + 1024); });
    const float t = (ldsr<float>(a) + ldsr<float>(a + 128)) + (ldsr<float>(a + 256) + ldsr<float>(a + 384));
    s0 = zero16();
    sfor<0, 4>([&](auto U) { s0 = mfma(f[U.v], ones, s0); });
    sfor<3, 6>([&](auto K) { WB(K.v); });
    ldsw<f32x4>(R2 + 2048 + w * 1024 + lane * 16, k1);
    ldsw<float>(R2 + 6144 + w * 256 + lane * 4, r1);
    ldsw<float>(R2 + 1024 + lane * 4, t);
    cs_write(R2 + 1280 + (w & 1) * 128, s0, lane);
  }
  GAS u64* fl = (GAS u64*)(p.ws + OFF_FLG);
  const bool l0 = threadIdx.x == 256;
  bar();
  __builtin_amdgcn_s_setprio(0);
  sfor<6, 14>([&](auto K) { WB(K.v); });
  sfor<0, 2>([&](auto J) { dma16(rr * 896 + ((cp ^ rr) << 4), sX, (g.tt * 64 + (2 * w + J.v) * 8) * 896 + g.s7 * 128, XT + (2 * w + J.v) * 1024); });
  const srd_t sW2 = mk(p.w2, 448 * 896);
  auto W2 = [&](int k) { const int pc = 8 * rot7(k >> 1, g.rot) + 4 * (k & 1) + w; dma16(wv896, sW2, pc * 8 * 896 + g.s7 * 128, RA + pc * 1024); };
  sfor<0, 4>([&](auto K) { W2(K.v); });
  wvm<4>(); bar();
  sfor<4, 14>([&](auto K) { W2(K.v); });
  const srd_t sW4 = mk(p.w4, 1792 * 896), sWo = mk(p.wo, 448 * 896), sW3 = mk(p.w3, 448 * 3584);
  u32x4 w4r[9][4], w3r[14];
  auto L4 = [&](auto SS) {
    constexpr int s = SS.v;
    sfor<0, 4>([&](auto I) {
      u32x4& d = s < 9 ? w3r[4 * (s - 5) + I.v] : w4r[s < 9 ? 0 : s - 9][I.v];
      if constexpr (s < 14) ld16p(d, wv896, sW4, (32 * p4_k32(w, s, g.rot) + 8 * I.v) * 896 + g.s7 * 128);
      else ld16p(d, wv896, sWo, (28 * (4 * w + ((s + g.rot) & 3)) + 8 * I.v) * 896 + g.s7 * 128);
    });
  };
  bar();
  publish(sWS, tg, FL_A + g.g4 * 14 + g.s14, l0);
  poll(fl, 1, FL_CS + 28 + g.s14, tag);
  asm volatile("s_mov_b32 m0, %3\n\ts_nop 0\n\tbuffer_load_dword %0, %1, %2 offen sc1 lds" ::"v"(lane * 4), "s"(sWS), "s"(OFF_CS3 + (28 + g.s14) * 256), "s"(XT) : "memory");
  {
    const int th = w & 1, kh = w >> 1, tc = 2 * g.tt + th;
    const int gsel = min(max(tc + ((lane & 4) ? 2 * th - 1 : 0), 0), 63) >> 2;
    poll(fl, 8, FL_A + gsel * 14 + min(3 * kh + (lane & 3), 6), tag);
    const srd_t sH = mk(p.ws + OFF_H, 64 * 28 * 1024);
    const int tok = tc * 32 + t32, ta = tok - 1 - kh, tb = tok + 1 + kh;
    const unsigned oa = (unsigned)((ta >> 5) * 28 * 1024 + ((ta & 31) + 32 * h) * 16), ob = (unsigned)((tb >> 5) * 28 * 1024 + ((tb & 31) + 32 * h) * 16);
    sfor<0, 6>([&](auto U) { dma16s(oa, sH, (kh * 14 + U.v) * 1024, HREG + w * 10240 + U.v * 1024); });
    bar();
    dma16s(oa, sH, (kh * 14 + 6) * 1024, HREG + w * 10240 + 6144);
    sfor<7, 10>([&](auto U) { dma16s(ob, sH, (kh * 14 + U.v) * 1024, HREG + w * 10240 + U.v * 1024); });
  }
  wvm<0>();
  bar();
  auto o3 = [&](int k) { return (unsigned)((8 * rot7(k >> 1, g.rot) + 4 * (k & 1) + w) * 8 * 3584 + g.s14 * 256); };
  sfor<0, 12>([&](auto K) { dma16(wv3, sW3, o3(K.v), R1 + (4 * K.v + w) * 1024); });
  bar();
  publish(sWS, tg, FL_Q + g.g4 * 14 + g.s14, l0);
  sfor<12, 14>([&](auto K) { dma16(wv3, sW3, o3(K.v), R1 + (4 * K.v + w) * 1024); });
  sfor<0, 8>([&](auto K) { ld16p(w3r[K.v], wv3, sW3, o3(K.v) + 128); });
  bar();
  publish(sWS, tg, FL_X + g.tt * 7 + g.s7, l0);
  wvm<9>(); bar();
  sfor<8, 14>([&](auto K) { ld16p(w3r[K.v], wv3, sW3, o3(K.v) + 128); });
  sfor<9, 18>(L4);
  wvm<36>();
  bar();
  sfor<0, 14>([&](auto K) { tie(w3r[K.v]); ldsw<u32x4>(RA + (4 * K.v + w) * 1024 + lane * 16, w3r[K.v]); });
  auto W4 = [&](auto SS) {
    sfor<0, 4>([&](auto I) { dma16(wv896, sW4, (32 * p4_k32(w, SS.v, g.rot) + 8 * I.v) * 896 + g.s7 * 128, p4_slot(w, SS.v) + I.v * 1024); });
  };
  sfor<0, 2>(W4);
  wvm<8>();
  bar();
  sfor<2, 5>(W4);
  bar();
  publish(sWS, tg, FL_3A + g.g4 * 14 + g.s14, l0);
  sfor<5, 8>(L4);
  bar();
  W4(IC<8>{});
  const unsigned f3 = (g.tt >> 1) * 14 + (7 * w >> 1) + (lane & 3);
  poll(fl, 4, FL_3A + f3, tag);
  bar();
  sfor<0, 12>([&](auto K) { tie(w3r[K.v]); ldsw<u32x4>(p4_slot(w, 5 + K.v / 4) + (K.v & 3) * 1024 + lane * 16, w3r[K.v]); });
  bar();
  publish(sWS, tg, FL_3B + g.g4 * 14 + g.s14, l0);
  auto S4 = [&](auto J) { sfor<0, 4>([&](auto I) { tie(w4r[J.v][I.v]); ldsw<u32x4>(p4_slot(w, J.v) + I.v * 1024 + lane * 16, w4r[J.v][I.v]); }); };
  poll(fl, 4, FL_3B + f3, tag);
  bar();
  u32x4 x1[2];
  sfor<0, 2>([&](auto U) { ld16s<U.v * 1024>(x1[U.v], lane * 16, sWS, OFF_X1 + ((2 * g.tt + (w & 1)) * 28 + 4 * g.s7 + (w & 2)) * 1024); });
  sfor<0, 4>(S4);
  wvm2<0>(x1[0], x1[1]);
  bar(); sfor<4, 8>(S4);
  poll(fl, 4, FL_O + (g.tt >> 1) * 16 + 4 * w + (lane & 3), tag);
  bar(); S4(IC<8>{});
  bar();
  __builtin_amdgcn_s_setprio(3);
  sfor<0, 4>([&](auto A) {
    f32x4 b = ldsr<f32x4>(p4_slot(w, 0) + A.v * 1024 + lane * 16);
    f32x4 o[3];
    sfor<0, 3>([&](auto P) { o[P.v] = ldsr<f32x4>(R1 + (w ^ (P.v + 1)) * 12288 + (4 * P.v + A.v) * 1024 + lane * 16); });
    const unsigned y0 = x1[A.v >> 1][2 * (A.v & 1)], y1 = x1[A.v >> 1][2 * (A.v & 1) + 1];
    const float xx[4] = {bflo(y0), bfhi(y0), bflo(y1), bfhi(y1)};
    sfor<0, 4>([&](auto I) {
      float t = b[I.v] + o[0][I.v], u = o[1][I.v] + o[2][I.v];
      tie(t); tie(u);
      t = (t + xx[I.v]) + u;
      b[I.v] = t * __builtin_amdgcn_rcpf(1.f + __builtin_amdgcn_exp2f(-LOG2E * t));
    });
    ldsw<f32x4>(p4_slot(w, 1) + A.v * 1024 + lane * 16, b);
  });
  bar();
}

DI void compute_path(const Params& p, unsigned& dummy, const int w, const int xc, const int r STARG) {
  const int tid = threadIdx.x, lane = tid & 63, t32 = lane & 31, h = lane >> 5, th = w & 1, kh = w >> 1;
  const Geo g(xc, r);
  const srd_t sWS = mk(p.ws, WS_USED);
  __builtin_amdgcn_s_setprio(2);
  u32x2 ev;
  ld8s<0>(ev, 0u, sWS, OFF_FLG);
  const unsigned T32 = 4 * g.g4 + w, tc = 2 * g.tt + th;
  const int ftok = tc * 32 + t32;
  u32x4 xv[2], x1p[2], gr;
  f32x16 xn;
  const int q = (lane & 15) >> 2, pp = lane & 3, blk = (lane >> 4) & 1;
  unsigned aperm = (4 * h + q) * 128 + (((2 * blk + (pp >> 1)) ^ ((q >> 1) << 2)) << 4) + (pp & 1) * 8, l16 = lane * 16;
  auto tra = [&](int hs, unsigned base, unsigned x) {
    tie(aperm);
    unsigned a = base + (aperm ^ x);
    if (hs == 8) a += l16 & 512;
    tie(a);
    return a;
  };
  auto lnv = [&](unsigned base) { tie(l16); unsigned a = base + l16; tie(a); return a; };
  u32x4 xf[28];
  unsigned xr[4], a0 = tra(8, RA, 0), a1 = tra(8, RA, 64);
  sfor<0, 4>([&](auto I) { xr[I.v] = R2 - 16384 + w * 4096 + t32 * 128 + (((2 * I.v + h) ^ ((t32 >> 1) & 7)) << 4); tie(xr[I.v]); });
  f32x16 c0, c1, c2, c3;
  pass_a<512, SlotP1>(tp, t0, c0, c1, a0, a1, xr, xf);
  bar();
  settle(c0, c1);
  f32x4 cst[8];
  const GeluK gk1 = ldsr<f32x4>(R2 + 2048 + w * 1024 + lane * 16);
  const float mean1 = gk1[3], r1 = ldsr<float>(R2 + 6144 + w * 256 + lane * 4);
  auto cs_load = [&](unsigned a) {
    sfor<0, 8>([&](auto A) { cst[A.v] = ldsr<f32x4>(a + A.v * 32 + h * 16); });
    sfor<0, 8>([&](auto A) { sfor<0, 4>([&](auto I) { (A.v < 4 ? c2 : c3)[4 * (A.v & 3) + I.v] = cst[A.v][I.v]; }); });
  };
  cs_load(R2 + 1024);
  sfor<0, 8>([&](auto A) { const f32x4 t = ldsr<f32x4>(R2 + 1280 + A.v * 32 + h * 16); sfor<0, 4>([&](auto I) { (A.v < 4 ? c2 : c3)[4 * (A.v & 3) + I.v] += t[I.v]; }); });
  wvm<0>(ev);
  tie(dummy);
  const u64 tag = make_tag(ev);
  const u32x2 tg = {(unsigned)tag, (unsigned)(tag >> 32)};
  auto epi = [&](auto ACT, unsigned so) {
    unsigned pk[4];
    sfor<0, 16>([&](auto J) {
      constexpr int e = 2 * (J.v & 7);
      const f32x16 &c = J.v < 8 ? c0 : c1, &s = J.v < 8 ? c2 : c3;
      f32x2 z = f32x2{c[e], c[e + 1]} - f32x2{s[e], s[e + 1]} * mean1;
      if constexpr (ACT.v) z = gelu2(z, gk1); else z *= r1;
      pk[J.v & 3] = pk2(z[0], z[1]);
      if constexpr ((J.v & 3) == 3) st16s<(J.v >> 2) * 1024>(u32x4{pk[0], pk[1], pk[2], pk[3]}, sWS, l16, so);
    });
  };
  if (g.wtype) epi(IC<1>{}, OFF_H + (T32 * 28 + 4 * g.s7) * 1024);
  else epi(IC<0>{}, OFF_QKV + (T32 * 84 + 28 + 4 * g.s7) * 1024);
  ld16s<0>(gr, ftok * 16, sWS, OFF_ST0);
  bar();
  ST(3);
  {
    u32x4 wf[3][2];
    float m0, r0;
    auto rd = [&](auto UU, auto NN) {
      const unsigned ad = (NN.v ? a1 : a0) + UU.v * 2048;
      wf[UU.v % 3][NN.v] = trfrag(ad, ad + 512);
    };
    a0 = tra(8, R1, 0); a1 = tra(8, R1, 64);
    rd(IC<0>{}, IC<0>{}); rd(IC<0>{}, IC<1>{}); rd(IC<1>{}, IC<0>{}); rd(IC<1>{}, IC<1>{});
    sfor<0, 28>([&](auto UU) {
      constexpr int U = UU.v;
      mmx<U == 0>(c0, wf[U % 3][0], xf[U]);
      if constexpr (U == UH) {
        wvm<0>(gr);
        bar();
        granule(gr, sWS, ftok * 16, tg);
      }
      if constexpr (U + 2 < 28) rd(IC<U + 2>{}, IC<0>{});
      if constexpr (U >= 2 && U < 6) {
        constexpr int A = U - 2;
        const u32x2 t = ldsr<u32x2>(XT + (th * 32 + t32) * 128 + (((4 * kh + A) ^ (t32 & 7)) << 4) + h * 8);
        xv[A >> 1][2 * (A & 1)] = t.x; xv[A >> 1][2 * (A & 1) + 1] = t.y;
      }
      SB();
      mmx<U == 0>(c1, wf[U % 3][1], xf[U]);
      if constexpr (U + 2 < 28) rd(IC<U + 2>{}, IC<1>{});
      if constexpr (U == UH + 1) { r0 = __builtin_amdgcn_rsqf(u2f(gr[2])); m0 = -u2f(gr[0]) * r0; tie(r0); tie(m0); }
      if constexpr (U >= 20) {
        constexpr int I = U - 20;
        float a = fmaf(bflo(xv[I >> 2][I & 3]), r0, m0), b = fmaf(bfhi(xv[I >> 2][I & 3]), r0, m0);
        tie(a); tie(b);
        xn[2 * I] = a; xn[2 * I + 1] = b;
      }
      SB();
    });
  }
  ST(4);
  bar();
  {
    const srd_t sH = mk(p.ws + fresh(OFF_H), 64 * 28 * 1024);
    const int tb = ftok + 1 + fresh(kh);
    const unsigned ob = (unsigned)((tb >> 5) * 28 * 1024 + ((tb & 31) + 32 * h) * 16);
    sfor<10, 14>([&](auto U) { ld16s<(U.v - 10) * 1024>(xf[U.v], ob, sH, (fresh(kh) * 14 + 10) * 1024); });
  }
  cs_load(XT);
  settle(c0, c1);
  epi(IC<0>{}, OFF_QKV + (fresh(T32) * 84 + (g.wtype ? 0 : 56) + 4 * g.s7) * 1024);
  ST(5);
  bar();
  ST(6);
  c0 = xn;
  {
    a0 = tra(4, RA + fresh(kh) * 14 * 2048, fresh(kh) * 64); a1 = a0 ^ 64u; tie(a1);
    const unsigned hb = lnv(HREG + fresh(w) * 10240);
    u32x4 wf[3][2];
    auto rd = [&](auto UU) {
      if constexpr (UU.v < 10) xf[UU.v] = ldsr<u32x4>(hb + UU.v * 1024);
      wf[UU.v % 3][0] = trfrag(a0 + UU.v * 2048, a0 + UU.v * 2048 + 1024); wf[UU.v % 3][1] = trfrag(a1 + UU.v * 2048, a1 + UU.v * 2048 + 1024);
    };
    rd(IC<0>{}); rd(IC<1>{});
    sfor<0, 14>([&](auto UU) {
      constexpr int U = UU.v;
      if constexpr (U == 10) wvm4<4>(xf[10], xf[11], xf[12], xf[13]);
      mma(c0, wf[U % 3][0], xf[U]);
      if constexpr (U + 2 < 14) rd(IC<U + 2>{});
      mmx<U == 0>(c1, wf[U % 3][1], xf[U]);
      SB();
    });
    settle(c0, c1);
    sfor<0, 4>([&](auto A) { ldsw<f32x4>(hb + A.v * 1024, quad(c1, A.v)); });
  }
  wvm<0>();
  bar();
  ST(27);
  {
    f32x4 o[4];
    const unsigned pb = lnv(HREG + (fresh(w) ^ 2) * 10240);
    sfor<0, 4>([&](auto A) { o[A.v] = ldsr<f32x4>(pb + A.v * 1024); });
    sfor<0, 4>([&](auto A) {
      const f32x4 t = quad(c0, A.v) + o[A.v];
      x1p[A.v >> 1][2 * (A.v & 1)] = pk2(t[0], t[1]);
      x1p[A.v >> 1][2 * (A.v & 1) + 1] = pk2(t[2], t[3]);
    });
    const unsigned so = OFF_X1 + (fresh(tc) * 28 + 4 * g.s7 + 2 * kh) * 1024;
    st16s(x1p[0], sWS, l16, so); st16s<1024>(x1p[1], sWS, l16, so);
    float a1 = 0.f, a2 = 0.f;
    sfor<0, 8>([&](auto I) { a1 = dot2(x1p[I.v >> 2][I.v & 3], ONE2, a1); a2 = dot2(x1p[I.v >> 2][I.v & 3], x1p[I.v >> 2][I.v & 3], a2); });
    st8s(u32x2{f2u(swap_add(a1)), f2u(swap_add(a2))}, sWS, h == 0 ? ftok * 8 : OOB, OFF_ST1 + (2 * fresh(g.s7) + kh) * 2048 * 8);
  }
  ST(7);
  wvm<0>();
  bar();
  ST(8);
  bar();
  GeluK gk3;
  float mean3;
  {
    a0 = tra(4, R1, 0); a1 = tra(4, R1, 64);
    xr[0] = lnv(RA + fresh(w) * 4096); xr[1] = lnv(R2 + fresh(w) * 4096);
    poll((GAS u64*)(p.ws + OFF_FLG), 9, lane >= 7 ? FL_CS + 2 * g.s14 + (lane & 1) : FL_X + (T32 >> 1) * 7 + lane, tag);
    ST(11);
    auto X1 = [&](auto UU) {
      constexpr int c = UU.v / 4, i = UU.v % 4;
      dma16s(l16, sWS, OFF_X1 + (T32 * 28 + 4 * rot7(c, fresh(g.rot)) + i) * 1024, SlotP3::off(c) + fresh(w) * 4096 + i * 1024);
    };
    u32x2 sp[7], cs3;
    sfor<0, 8>(X1);
    sfor<0, 7>([&](auto I) { ld8s<0>(sp[I.v], (T32 * 32 + t32 + h * 7 * 2048) * 8, sWS, OFF_ST1 + I.v * 2048 * 8); });
    ld8s<0>(cs3, lane * 8, sWS, OFF_CS3 + g.s14 * 512);
    sfor<8, 12>(X1);
    u32x4 wf[3][2];
    auto rd = [&](auto UU, auto NN) { const unsigned ad = (NN.v ? a1 : a0) + UU.v * 2048; wf[UU.v % 3][NN.v] = trfrag(ad, ad + 1024); };
    auto rx = [&](auto UU) {
      constexpr int U = UU.v;
      wvm<(U < 2 ? 19 - U : U < 8 ? 17 : U <= 18 ? 9 : 27 - U)>();
      xf[U] = ldsr<u32x4>(SlotP3::addr(xr, U));
    };
    rd(IC<0>{}, IC<0>{}); rd(IC<0>{}, IC<1>{}); rd(IC<1>{}, IC<0>{}); rd(IC<1>{}, IC<1>{});
    rx(IC<0>{}); rx(IC<1>{});
    sfor<0, 28>([&](auto UU) {
      constexpr int U = UU.v;
      mmx<U == 0>(c0, wf[U % 3][0], xf[U]);
      if constexpr (U + 2 < 28) { rx(IC<U + 2>{}); rd(IC<U + 2>{}, IC<0>{}); }
      SB();
      mmx<U == 0>(c1, wf[U % 3][1], xf[U]);
      if constexpr (U + 2 < 28) rd(IC<U + 2>{}, IC<1>{});
      if constexpr (U + 12 < 28) X1(IC<U + 12>{});
      if constexpr (U == 17) {
        float sm = 0.f, sq = 0.f;
        sfor<0, 7>([&](auto I) { tie(sp[I.v]); sm += u2f(sp[I.v].x); sq += u2f(sp[I.v].y); });
        mean3 = swap_add(sm) * (1.f / D);
        const float ve = fmaxf(swap_add(sq) * (1.f / D) - mean3 * mean3, 0.f) + 1e-5f;
        gk3 = gelu_k(__builtin_amdgcn_rsqf(ve), __builtin_amdgcn_sqrtf(ve));
        tie(gk3); tie(mean3);
      }
      if constexpr (U == 19) { tie(cs3); ldsw<u32x2>(R2 + fresh(w) * 4096 + lane * 8, cs3); }
      SB();
    });
  }
  bar();
  ST(13);
  unsigned csb = R2 + fresh(w) * 4096 + h * 16;
  tie(csb);
  auto CS3 = [&](int t) { sfor<0, 8>([&](auto A) { cst[A.v] = ldsr<f32x4>(csb + t * 128 + A.v * 32); }); };
  const unsigned so3 = OFF_H3 + (fresh(T32) * 112 + 8 * g.s14) * 1024;
  unsigned pk[4];
  auto G = [&](auto JJ) {
    constexpr int j = JJ.v, t = j >> 3, e = 2 * (j & 7);
    const f32x16& c = t == 0 ? c0 : t == 1 ? c1 : t == 2 ? c2 : c3;
    const f32x2 z = gelu2(f32x2{c[e], c[e + 1]} - f32x2{cst[4 * (t & 1) + (e >> 2)][e & 3], cst[4 * (t & 1) + (e >> 2)][(e & 3) + 1]} * mean3, gk3);
    pk[j & 3] = pk2(z[0], z[1]);
    if constexpr ((j & 3) == 3) st16s<(j >> 2 & 3) * 1024>(u32x4{pk[0], pk[1], pk[2], pk[3]}, sWS, l16, so3 + (j >> 4) * 4096);
  };
  CS3(0);
  settle(c0, c1);
  sfor<0, 16>([&](auto J) { G(J); });
  bar();
  ST(24);
  {
    u32x4 wf[4];
    a0 = tra(4, RA, 0); a1 = tra(4, RA, 64);
    auto rd = [&](auto MM) { const unsigned ad = (MM.v < 28 ? a0 : a1) + (MM.v % 28) * 2048; wf[MM.v & 3] = trfrag(ad, ad + 1024); };
    rd(IC<0>{}); rd(IC<1>{}); rd(IC<2>{});
    sfor<0, 56>([&](auto MM) {
      constexpr int M = MM.v;
      mmx<M % 28 == 0>(M < 28 ? c2 : c3, wf[M & 3], xf[M % 28]);
      if constexpr (M + 3 < 56) rd(IC<M + 3>{});
      if constexpr (M == M3) { wvm<0>(); asm volatile("s_barrier" ::: "memory"); }
      if constexpr (M == 50) CS3(2);
      SB();
    });
  }
  bar();
  ST(14);
  settle(c2, c3);
  sfor<16, 24>([&](auto J) { G(J); });
  ST(15);
  u32x4 af[4][4];
  unsigned av[2] = {lnv(OFF_H3 + fresh(tc) * 112 * 1024), lnv(OFF_H3 + (fresh(tc) ^ 1) * 112 * 1024)}, as;
  auto ACT1 = [&](auto SS, auto II) {
    constexpr int s = SS.v;
    if constexpr (s == 14 && II.v == 0) { av[0] = lnv(OFF_O + fresh(tc) * 32 * 1024); av[1] = lnv(OFF_O + (fresh(tc) ^ 1) * 32 * 1024); }
    if constexpr (II.v == 0) as = s < 14 ? p4_k32(w, s, fresh(g.rot)) * 2048 : (4 * w + ((s + fresh(g.rot)) & 3)) * 2048;
    ld16s<(II.v & 1) * 1024>(af[s & 3][II.v], av[II.v >> 1], sWS, as);
  };
  unsigned bs[4][2];
  sfor<0, 4>([&](auto I) { bs[I.v][0] = tra(4, p4_slot(fresh(w), I.v ? 3 * I.v - 1 : 0), fresh(kh) * 64); bs[I.v][1] = bs[I.v][0] ^ 64u; tie(bs[I.v][1]); });
  u32x4 wf[2][4];
  auto RW = [&](auto SS, auto I) {
    constexpr int j = SS.v % 9;
    const unsigned aa = bs[(j + 1) / 3][I.v & 1] + (j < 2 ? j : (j + 1) % 3) * 4096 + (I.v >> 1) * 2048;
    wf[SS.v & 1][I.v] = trfrag(aa, aa + 1024);
  };
  bar();
  sfor<0, 12>([&](auto K) { ACT1(IC<K.v / 4>{}, IC<K.v % 4>{}); });
  tie(c3);
  sfor<24, 32>([&](auto J) { G(J); });
  ST(16);
  sfor<0, 4>([&](auto I) { RW(IC<0>{}, I); });
  f32x16 d0, d1, d2, d3;
  const unsigned exw = lnv(R1 + fresh(w) * 12288), own = lnv(p4_slot(fresh(w), 0));
  sfor<0, 18>([&](auto SS) {
    constexpr int s = SS.v;
    if constexpr (s == 4 || s == 8 || s == 11 || s == 15) bar();
    if constexpr (s % 3 == 0) ST(17 + s / 3);
    wvm4<(s == 0 ? 10 : s == 1 ? 4 : s <= 15 ? 8 : 4 * (17 - s))>(af[s & 3][0], af[s & 3][1], af[s & 3][2], af[s & 3][3]);
    if constexpr (s == 1) asm volatile("s_barrier" ::: "memory");
    f32x16 &a0 = s < 14 ? c0 : d0, &a1 = s < 14 ? c1 : d1, &a2 = s < 14 ? c2 : d2, &a3 = s < 14 ? c3 : d3;
    auto side = [&](auto KK) {
      constexpr int k = 4 * s + KK.v;
      if constexpr (s + 3 <= 17) ACT1(IC<s + 3>{}, KK);
      if constexpr (k == 56) { settle(c0, c1); settle(c2, c3); }
      if constexpr (k >= 56 && k < 60) {
        sfor<0, 3>([&](auto J) { constexpr int A = 3 * (k - 56) + J.v; ldsw<f32x4>(exw + A * 1024, quad(A < 4 ? c1 : A < 8 ? c2 : c3, A & 3)); });
        ldsw<f32x4>(own + (k - 56) * 1024, quad(c0, k - 56));
      }
    };
    sfor<0, 2>([&](auto U) {
      mmx<s % 14 == 0 && U.v == 0>(a0, wf[s & 1][2 * U.v], af[s & 3][U.v]);
      if constexpr (s + 1 < 18) RW(IC<s + 1>{}, IC<2 * U.v>{});
      mmx<s % 14 == 0 && U.v == 0>(a1, wf[s & 1][2 * U.v], af[s & 3][2 + U.v]);
      side(IC<2 * U.v>{});
      SB();
      mmx<s % 14 == 0 && U.v == 0>(a2, wf[s & 1][2 * U.v + 1], af[s & 3][U.v]);
      if constexpr (s + 1 < 18) RW(IC<s + 1>{}, IC<2 * U.v + 1>{});
      mmx<s % 14 == 0 && U.v == 0>(a3, wf[s & 1][2 * U.v + 1], af[s & 3][2 + U.v]);
      side(IC<2 * U.v + 1>{});
      SB();
    });
  });
  ST(23);
  settle(d0, d1); tie(d2); tie(d3);
  const unsigned e3[3] = {lnv(RA + (fresh(w) ^ 1) * 12288), lnv(RA + (fresh(w) ^ 2) * 12288), lnv(RA + (fresh(w) ^ 3) * 12288)};
  unsigned oo = ftok * 896 + h * 16 + fresh(g.s7) * 128 + kh * 64;
  tie(oo);
  sfor<0, 12>([&](auto A) { ldsw<f32x4>(exw - R1 + A.v * 1024, quad(A.v < 4 ? d1 : A.v < 8 ? d2 : d3, A.v & 3)); });
  bar();
  sfor<0, 12>([&](auto A) {
    const f32x4 v = ldsr<f32x4>(e3[A.v / 4] + A.v * 1024);
    sfor<0, 4>([&](auto I) { d0[4 * (A.v & 3) + I.v] += v[I.v]; });
  });
  sfor<0, 4>([&](auto A) {
    const f32x4 v = ldsr<f32x4>(own + 4096 + A.v * 1024);
    sfor<0, 4>([&](auto I) { c0[4 * A.v + I.v] = v[I.v]; });
  });
  const srd_t sO = mk(p.out, 2048 * 896);
  sfor<0, 2>([&](auto U) {
    u32x4 ov;
    sfor<0, 4>([&](auto I) {
      constexpr int e = 8 * U.v + 2 * I.v;
      ov[I.v] = pk2(fmaf(d0[e], c0[e], bflo(xv[U.v][I.v])), fmaf(d0[e + 1], c0[e + 1], bfhi(xv[U.v][I.v])));
    });
    __builtin_amdgcn_raw_buffer_store_b128(cross(ov), sO, oo + U.v * 32, 0, 0);
  });
}

DI void spare_path(const Params& p, const int w, const int xc, const int r STARG) {
  const int tid = threadIdx.x, lane = tid & 63, t32 = lane & 31, h = lane >> 5, tb = w & 3, ch = w >> 2;
  constexpr unsigned WPHI = 160768, ZERO = 159744, DUMMY = 158720;
  const int rr = lane >> 3, cp = lane & 7, rs = r - 28;
  const srd_t sWS = mk(p.ws, WS_USED), sX = mk(p.x, 2048 * 896);
  GAS u64* fl = (GAS u64*)(p.ws + OFF_FLG);
  const unsigned srow = 512 * (xc >> 1) + 64 * ((xc & 1) * 4 + rs) + 8 * w + rr;
  u32x2 ev;
  ld8s<0>(ev, 0u, sWS, OFF_FLG);
  u32x4 xr7[7];
  sfor<0, 7>([&](auto K) { ld16p(xr7[K.v], srow * 896 + cp * 16, sX, K.v * 128); });
  const int hd = 2 * xc + (rs >> 1), hf = rs & 1;
  wvm<0>(ev);
  const u64 tag = make_tag(ev);
  const u32x2 tg = {(unsigned)tag, (unsigned)(tag >> 32)};
  {
    float a1 = 0.f, a2 = 0.f;
    sfor<0, 7>([&](auto K) { tie(xr7[K.v]); sfor<0, 4>([&](auto I) { a1 = dot2(xr7[K.v][I.v], ONE2, a1); a2 = dot2(xr7[K.v][I.v], xr7[K.v][I.v], a2); }); });
    sfor<0, 3>([&](auto K) { a1 += __shfl_xor(a1, 1 << K.v); a2 += __shfl_xor(a2, 1 << K.v); });
    const float mean = a1 * (1.f / D), ve = fmaxf(a2 * (1.f / D) - mean * mean, 0.f) + 1e-5f;
    st16s(u32x4{f2u(mean), tg.x, f2u(ve), tg.y}, sWS, cp == 0 ? srow * 16 : OOB, OFF_ST0);
  }
  ST(1);
  const int bi = (xc >> 1) * 4 + rs, par = xc & 1;
  const int q = (lane & 15) >> 2, pp = lane & 3, blk = (lane >> 4) & 1;
  const unsigned acol = (unsigned)((((2 * blk + (pp >> 1)) ^ ((q >> 1) << 2)) << 4) + (pp & 1) * 8);
  const unsigned aperm = (4 * h + q) * 128 + acol;
  const srd_t sW3 = mk(p.w3, 448 * 3584);
  const unsigned wsw = (cp ^ (((rr >> 1) & 1) << 2)) << 4;
  {
    const srd_t s0 = bi < 7 ? mk(p.wqkv, 448 * 2688) : sW3, sP = mk(p.wphi, 16 * 1568);
    const unsigned pitch = bi < 7 ? 2688 : 3584, col = (14 * par + (bi < 7 ? bi : bi - 7)) * 128;
    sfor<0, 7>([&](auto K) { dma16(rr * pitch + wsw, s0, (8 * K.v + w) * 8 * pitch + col, RA + (8 * K.v + w) * 1024); });
    dma16(lane * 16, sP, hd * 1568 + (w & 1) * 1024, WPHI + (w & 1) * 1024);
  }
  if (tid < 16) ldsw<unsigned>(ZERO + tid * 4, 0u);
  auto sums = [&](int cb) {
    wvm<0>();
    bar();
    cs_write(R2 + w * 128, colsum<7>(RA + (aperm ^ (ch * 64)) + tb * 7 * 2048), lane);
    bar();
    if (tb == 0) {
      const f32x16 cs = cs_read(R2 + ch * 512 + h * 16, 128);
      sfor<0, 4>([&](auto A) { st16s<A.v * 32>(__builtin_bit_cast(u32x4, quad(cs, A.v)), sWS, t32 == 0 ? h * 16 : OOB, OFF_CS3 + cb * 256 + ch * 128); });
      wvm<0>();
    }
    bar();
    publish(sWS, tg, FL_CS + cb, tid == 0);
  };
  sums(bi < 7 ? 28 + 7 * par + bi : 14 * par + bi - 7);
  ST(9);
  if (bi < 5) {
    sfor<0, 7>([&](auto K) { dma16(rr * 3584 + wsw, sW3, (8 * K.v + w) * 8 * 3584 + (14 * par + 9 + bi) * 128, RA + (8 * K.v + w) * 1024); });
    sums(14 * par + 9 + bi);
  }
  ST(2);
  const int gq = 8 * hf + w;
  const unsigned qkvs = OFF_QKV + gq * 4 * 84 * 1024;
  const int b01 = (28 * hd + (lane & 1) * 27) >> 6;
  const int U0 = (28 * hd) >> 4, o = (28 * hd) & 15;
  const unsigned f2 = (o >= 8 ? 2 : 0) * 1024;
  const unsigned img = w * 18432;
  u32x4 wp[3];
  unsigned vw[6];
  sfor<0, 3>([&](auto I) {
    unsigned a[2];
    sfor<0, 2>([&](auto T) {
      const int d0 = 16 * I.v + 8 * T.v + 4 * h - o;
      const bool ok = d0 >= 0 && d0 < 28;
      a[T.v] = (ok ? WPHI + (d0 + q) * 56 : ZERO) + (16 * blk + 4 * pp) * 2;
      vw[2 * I.v + T.v] = ok ? img + 16384 + t32 * 64 + (((d0 >> 2) ^ ((t32 >> 1) & 7)) << 3) : DUMMY + lane * 8;
    });
    wp[I.v] = trfrag(a[0], a[1]);
  });
  u32x4 kp[4][3], vp[4][3], qp[4][3];
  auto gather = [&](u32x4 (&d)[4][3], unsigned base) {
    sfor<0, 4>([&](auto T) {
      const unsigned so = qkvs + (T.v * 84 + base + U0) * 1024;
      ld16s<0>(d[T.v][0], lane * 16, sWS, so); ld16s<1024>(d[T.v][1], lane * 16, sWS, so); ld16s<0>(d[T.v][2], lane * 16, sWS, so + f2);
    });
  };
  auto feat = [&](u32x4 (&k)[3], u32x4 (&out)[4]) {
    sfor<0, 3>([&](auto I) { tie(k[I.v]); });
    f32x16 f = mfma(wp[0], k[0], zero16());
    f = mfma(wp[1], k[1], f);
    f = mfma(wp[2], k[2], f);
    dual_softmax(f, h != 0, out);
  };
  const unsigned fq = gq * 14 + b01;
  poll(fl, 2, FL_A + fq + 7, tag);
  ST(3);
  gather(kp, 28);
  if (h) ldsw<u32x2>(img + 16384 + t32 * 64 + ((7 ^ ((t32 >> 1) & 7)) << 3), u32x2{0x3f80u, 0u});
  sfor<0, 4>([&](auto T) {
    wvm<9 - 3 * T.v>();
    u32x4 ph[4];
    feat(kp[T.v], ph);
    const unsigned pr = img + T.v * 4096 + t32 * 128, sw = h ^ (((t32 >> 1) & 1) << 3) ^ (t32 & 1) ^ (((t32 >> 2) & 3) << 1);
    sfor<0, 8>([&](auto A) { ldsw<u32x2>(pr + (((2 * A.v) ^ sw) << 3), u32x2{ph[A.v >> 1][2 * (A.v & 1)], ph[A.v >> 1][2 * (A.v & 1) + 1]}); });
  });
  ST(4);
  poll(fl, 4, FL_Q + fq + ((lane & 2) ? 7 : 0), tag);
  ST(5);
  gather(qp, 0);
  gather(vp, 56);
  u32x4 phq[4][4];
  wvm<21>();
  feat(qp[0], phq[0]);
  f32x16 kvp = zero16(), kvm = zero16();
  unsigned aa[2], ba[2];
  sfor<0, 2>([&](auto T) {
    const int row = 8 * h + 4 * T.v + q, pc = 4 * blk + pp;
    aa[T.v] = img + row * 128 + ((pc ^ ((q >> 1) << 3) ^ (q & 1) ^ ((2 * h + T.v) << 1)) << 3);
    ba[T.v] = img + 16384 + row * 64 + ((pc ^ (row >> 1)) << 3);
  });
  sfor<0, 4>([&](auto T) {
    wvm<9 - 3 * T.v>();
    sfor<0, 3>([&](auto I) { tie(vp[T.v][I.v]); });
    sfor<0, 6>([&](auto A) { ldsw<u32x2>(vw[A.v], u32x2{vp[T.v][A.v >> 1][2 * (A.v & 1)], vp[T.v][A.v >> 1][2 * (A.v & 1) + 1]}); });
    sfor<0, 2>([&](auto U) {
      constexpr unsigned oa = T.v * 4096 + U.v * 2048;
      const u32x4 bf = trfrag(ba[0] + U.v * 1024, ba[1] + U.v * 1024);
      kvp = mfma(trfrag(aa[0] + oa, aa[1] + oa), bf, kvp);
      kvm = mfma(trfrag((aa[0] ^ 64u) + oa, (aa[1] ^ 64u) + oa), bf, kvm);
    });
  });
  sfor<0, 8>([&](auto A) { ldsw<f32x4>(img + A.v * 1024 + lane * 16, quad(A.v < 4 ? kvp : kvm, A.v & 3)); });
  bar();
  f32x4 own = {0.f, 0.f, 0.f, 0.f};
  sfor<0, 8>([&](auto S) { own += ldsr<f32x4>(S.v * 18432 + w * 1024 + lane * 16); });
  st16s(__builtin_bit_cast(u32x4, own), sWS, lane * 16, OFF_KVP + ((hd * 2 + hf) * 8 + w) * 1024);
  feat(qp[1], phq[1]);
  wvm<0>();
  bar();
  publish(sWS, tg, FL_KV + hd * 2 + hf, tid == 0);
  ST(6);
  feat(qp[2], phq[2]);
  poll(fl, 1, FL_KV + hd * 2 + (hf ^ 1), tag);
  u32x4 oth;
  ld16s<0>(oth, lane * 16, sWS, OFF_KVP + ((hd * 2 + (hf ^ 1)) * 8 + w) * 1024);
  feat(qp[3], phq[3]);
  wvm<0>(oth);
  own += __builtin_bit_cast(f32x4, oth);
  ldsw<u32x2>(147456 + (w >> 1) * 1024 + lane * 16 + (w & 1) * 8, u32x2{pk2(own[0], own[1]), pk2(own[2], own[3])});
  bar();
  ST(7);
  u32x4 kvf[4];
  sfor<0, 4>([&](auto I) { kvf[I.v] = ldsr<u32x4>(147456 + I.v * 1024 + lane * 16); });
  sfor<0, 4>([&](auto T) {
    f32x16 n = mfma(kvf[0], phq[T.v][0], zero16());
    n = mfma(kvf[1], phq[T.v][1], n);
    n = mfma(kvf[2], phq[T.v][2], n);
    n = mfma(kvf[3], phq[T.v][3], n);
    const float rd = __builtin_amdgcn_rcpf(swap_hi(n[12]) + 1e-6f);
#pragma unroll
    for (int i = 0; i < 16; ++i) n[i] *= rd;
    u32x4 o1 = pack8(n, 1);
    if (h) { o1[2] = 0u; o1[3] = 0u; }
    const unsigned so = OFF_O + ((4 * gq + T.v) * 32 + 2 * hd) * 1024;
    st16s(pack8(n, 0), sWS, lane * 16, so); st16s<1024>(o1, sWS, lane * 16, so);
  });
  wvm<0>();
  publish(sWS, tg, FL_O + gq * 16 + hd, lane == 0);
  ST(8);
  if (xc == 0 && rs == 0 && w == 0) {
    sfor<0, 4>([&](auto I) { poll(fl, 56, FL_Q + 56 * I.v + lane, tag); });
    publish(sWS, tg, FL_E, lane == 0);
  }
}

__global__ __launch_bounds__(512, 2) void fused(Params p) {
  u64 pc;
  asm volatile("s_getpc_b64 %0" : "=s"(pc));
  const unsigned t0 = rtn();
  unsigned tp = 0;
  int w = __builtin_amdgcn_readfirstlane(threadIdx.x >> 6);
  asm volatile("s_nop 4" : "+s"(w));
  const int xc = blockIdx.x & 7, r = blockIdx.x >> 3;
  unsigned dummy = 0;
  if (r < 28 && w >= 4) first_loads(p, w - 4, xc, r);
  if (r < 28 && w < 4) code_prefetch(dummy, pc, (((r & 1) * 4 + w) * 64 + (threadIdx.x & 63)) * 128);
  SB();
  if (r < 28) {
    if (w < 4) compute_path(p, dummy, w, xc, r STPASS);
    else loader_path(p, w - 4, xc, r STPASS);
  }
  else spare_path(p, w, xc, r STPASS);
  while (rtn() - t0 < 4000 + tp) __builtin_amdgcn_s_sleep(2);
}

extern "C" void kernel_launch(void* const* d_in, const int* in_sizes, int n_in, void* d_out, int out_size, void* d_ws, size_t ws_size,
                              hipStream_t stream) {
  Params p;
  p.x = (const uint16_t*)d_in[0]; p.wqkv = (const uint16_t*)d_in[1]; p.wphi = (const uint16_t*)d_in[2]; p.wo = (const uint16_t*)d_in[3];
  p.w1 = (const uint16_t*)d_in[5]; p.w2 = (const uint16_t*)d_in[6]; p.w3 = (const uint16_t*)d_in[7]; p.w4 = (const uint16_t*)d_in[8];
  p.out = (uint16_t*)d_out; p.ws = (char*)d_ws;
  (void)hipFuncSetAttribute((const void*)fused, hipFuncAttributeMaxDynamicSharedMemorySize, LDS_BYTES);
  fused<<<256, 512, LDS_BYTES, stream>>>(p);
}
```

```cpp
#include <hip/hip_runtime.h>
#include <stdint.h>

#define LAS __attribute__((address_space(3)))
#define GAS __attribute__((address_space(1)))
#define DI __device__ __forceinline__
typedef short s16x4 __attribute__((ext_vector_type(4)));
typedef __bf16 bf16x8 __attribute__((ext_vector_type(8)));
typedef __bf16 bf16x2 __attribute__((ext_vector_type(2)));
typedef float f32x16 __attribute__((ext_vector_type(16)));
typedef float f32x4 __attribute__((ext_vector_type(4)));
typedef float f32x2 __attribute__((ext_vector_type(2)));
typedef unsigned u32x4 __attribute__((ext_vector_type(4)));
typedef unsigned u32x2 __attribute__((ext_vector_type(2)));
typedef unsigned long long u64;
typedef __amdgpu_buffer_rsrc_t srd_t;

constexpr int D = 448;
constexpr float LOG2E = 1.4426950408889634f;
constexpr unsigned OFF_FLG = 0;
constexpr unsigned OFF_H = 262144;
constexpr unsigned OFF_X1 = OFF_H + 64 * 28 * 1024;
constexpr unsigned OFF_QKV = OFF_X1 + 64 * 28 * 1024;
constexpr unsigned OFF_H3 = OFF_QKV + 64 * 84 * 1024;
constexpr unsigned OFF_O = OFF_H3 + 64 * 112 * 1024;
constexpr unsigned OFF_KVP = OFF_O + 64 * 32 * 1024;
constexpr unsigned OFF_ST0 = OFF_KVP + 32 * 8192;
constexpr unsigned OFF_ST1 = OFF_ST0 + 2048 * 16;
constexpr unsigned OFF_CS3 = OFF_ST1 + 14 * 2048 * 8;
constexpr unsigned WS_USED = OFF_CS3 + 42 * 256;
constexpr size_t OFF_STAMP = 64u << 20;
constexpr int FL_E = 0, FL_A = 1, FL_Q = FL_A + 16 * 14, FL_X = FL_Q + 16 * 14, FL_3A = FL_X + 32 * 7, FL_3B = FL_3A + 16 * 14,
              FL_O = FL_3B + 16 * 14, FL_KV = FL_O + 256, FL_CS = FL_KV + 32;
static_assert((FL_CS + 42) * 128 <= 262144, "flag block");
constexpr unsigned RA = 0, R1 = 57344, R2 = 114688, LDS_BYTES = 163840;
constexpr unsigned OOB = 0x80000000u;
constexpr unsigned CODE_BYTES = 48128;
constexpr unsigned SPIN_CAP = 1u << 18;

struct Params {
  const uint16_t *x, *wqkv, *wphi, *wo, *w1, *w2, *w3, *w4;
  uint16_t* out;
  char* ws;
};

template <int I> struct IC { static constexpr int v = I; };
template <int B, int E, class F> DI void sfor(F&& f) {
  if constexpr (B < E) { f(IC<B>{}); sfor<B + 1, E>(f); }
}
DI f32x16 mfma(u32x4 a, u32x4 b, f32x16 c) {
  return __builtin_amdgcn_mfma_f32_32x32x16_bf16(__builtin_bit_cast(bf16x8, a), __builtin_bit_cast(bf16x8, b), c, 0, 0, 0);
}
DI void mma(f32x16& c, u32x4 a, u32x4 b) { asm volatile("v_mfma_f32_32x32x16_bf16 %0, %1, %2, %0" : "+v"(c) : "v"(a), "v"(b) : "memory"); }
DI void mma0(f32x16& c, u32x4 a, u32x4 b) { asm volatile("v_mfma_f32_32x32x16_bf16 %0, %1, %2, 0" : "=&v"(c) : "v"(a), "v"(b) : "memory"); }
template <bool FIRST> DI void mmx(f32x16& c, u32x4 a, u32x4 b) { if constexpr (FIRST) mma0(c, a, b); else mma(c, a, b); }
DI void settle(f32x16& a, f32x16& b) { asm volatile("s_nop 11" : "+v"(a), "+v"(b)); }
DI unsigned pk2(float a, float b) {
  f32x2 v = {a, b};
  return __builtin_bit_cast(unsigned, __builtin_convertvector(v, bf16x2));
}
DI float u2f(unsigned u) { return __builtin_bit_cast(float, u); }
DI unsigned f2u(float f) { return __builtin_bit_cast(unsigned, f); }
DI float bflo(unsigned u) { return __builtin_bit_cast(float, u << 16); }
DI float bfhi(unsigned u) { return __builtin_bit_cast(float, u & 0xffff0000u); }
DI u32x4 trfrag(unsigned a0, unsigned a1) {
  u32x2 lo = __builtin_bit_cast(u32x2, __builtin_amdgcn_ds_read_tr16_b64_v4i16((LAS s16x4*)(uintptr_t)a0));
  u32x2 hi = __builtin_bit_cast(u32x2, __builtin_amdgcn_ds_read_tr16_b64_v4i16((LAS s16x4*)(uintptr_t)a1));
  return u32x4{lo.x, lo.y, hi.x, hi.y};
}
template <class T> DI T ldsr(unsigned a) { return *(LAS T*)(uintptr_t)a; }
template <class T> DI void ldsw(unsigned a, T v) { *(LAS T*)(uintptr_t)a = v; }
DI srd_t mk(const void* p, unsigned bytes) { return __builtin_amdgcn_make_buffer_rsrc((void*)p, 0, (int)bytes, 0x00020000); }
DI void dma16(unsigned voff, srd_t r, unsigned soff, unsigned lds) {
  asm volatile("s_mov_b32 m0, %3\n\ts_nop 0\n\tbuffer_load_dwordx4 %0, %1, %2 offen lds" ::"v"(voff), "s"(r), "s"(soff), "s"(lds) : "memory");
}
DI void dma16s(unsigned voff, srd_t r, unsigned soff, unsigned lds) {
  asm volatile("s_mov_b32 m0, %3\n\ts_nop 0\n\tbuffer_load_dwordx4 %0, %1, %2 offen sc1 lds" ::"v"(voff), "s"(r), "s"(soff), "s"(lds) : "memory");
}
DI void ld4p(unsigned& d, unsigned voff, srd_t r) {
  asm volatile("buffer_load_dword %0, %1, %2, 0 offen" : "=v"(d) : "v"(voff), "s"(r) : "memory");
}
DI void ld16p(u32x4& d, unsigned voff, srd_t r, unsigned soff) {
  asm volatile("buffer_load_dwordx4 %0, %1, %2, %3 offen" : "=v"(d) : "v"(voff), "s"(r), "s"(soff) : "memory");
}
template <int IMM> DI void ld16s(u32x4& d, unsigned voff, srd_t r, unsigned soff) {
  asm volatile("buffer_load_dwordx4 %0, %1, %2, %3 offen offset:%c4 sc1" : "=v"(d) : "v"(voff), "s"(r), "s"(soff), "i"(IMM) : "memory");
}
template <int IMM> DI void ld8s(u32x2& d, unsigned voff, srd_t r, unsigned soff) {
  asm volatile("buffer_load_dwordx2 %0, %1, %2, %3 offen offset:%c4 sc1" : "=v"(d) : "v"(voff), "s"(r), "s"(soff), "i"(IMM) : "memory");
}
template <int IMM = 0> DI void st16s(u32x4 v, srd_t r, unsigned voff, unsigned soff) {
  asm volatile("buffer_store_dwordx4 %0, %1, %2, %3 offen offset:%c4 sc1\n\ts_nop 1" ::"v"(v), "v"(voff), "s"(r), "s"(soff), "i"(IMM) : "memory");
}
DI void st8s(u32x2 v, srd_t r, unsigned voff, unsigned soff) {
  asm volatile("buffer_store_dwordx2 %0, %1, %2, %3 offen sc1" ::"v"(v), "v"(voff), "s"(r), "s"(soff) : "memory");
}
template <int N> DI void wvm() { asm volatile("s_waitcnt vmcnt(%c0)" ::"i"(N) : "memory"); }
template <class T> DI void tie(T& a) { asm volatile("" : "+v"(a)); }
template <int N, class T> DI void wvm(T& a) { asm volatile("s_waitcnt vmcnt(%c1)" : "+v"(a) : "i"(N) : "memory"); }
DI void wlg4(u32x4& a, u32x4& b, u32x4& c, u32x4& d) { asm volatile("s_waitcnt lgkmcnt(0)" : "+v"(a), "+v"(b), "+v"(c), "+v"(d)::"memory"); }
template <int N> DI void wvm4(u32x4& a, u32x4& b, u32x4& c, u32x4& d) {
  asm volatile("s_waitcnt vmcnt(%c4)" : "+v"(a), "+v"(b), "+v"(c), "+v"(d) : "i"(N) : "memory");
}
template <int N> DI void wvm2(u32x4& a, u32x4& b) { asm volatile("s_waitcnt vmcnt(%c2)" : "+v"(a), "+v"(b) : "i"(N) : "memory"); }
DI int fresh(int v) { asm volatile("" : "+s"(v)); return v; }
DI void bar() { asm volatile("s_waitcnt lgkmcnt(0)\n\ts_barrier" ::: "memory"); }
#define SB() __builtin_amdgcn_sched_barrier(0)
DI void halves(float v, float& lo, float& hi) {
  auto r = __builtin_amdgcn_permlane32_swap(f2u(v), f2u(v), false, false);
  lo = u2f(r[0]); hi = u2f(r[1]);
}
DI float swap_add(float v) { float a, b; halves(v, a, b); return a + b; }
DI float swap_max(float v) { float a, b; halves(v, a, b); return fmaxf(a, b); }
DI float swap_hi(float v) { float a, b; halves(v, a, b); return b; }
typedef f32x4 GeluK;
DI GeluK gelu_k(float r, float ir) {
  const float c0 = -2.f * 0.7978845608028654f * LOG2E, c1 = c0 * 0.044715f;
  return GeluK{c0 * r, c1 * r * r * r, ir, 0.f};
}
DI f32x2 gelu2(f32x2 z, GeluK k) {
  const f32x2 u = z * (z * z * k[1] + k[0]);
  const f32x2 e = {__builtin_amdgcn_exp2f(u[0]), __builtin_amdgcn_exp2f(u[1])};
  const f32x2 d = e * k[2] + k[2];
  return z * f32x2{__builtin_amdgcn_rcpf(d[0]), __builtin_amdgcn_rcpf(d[1])};
}
DI float dot2(unsigned a, unsigned b, float c) {
  return __builtin_amdgcn_fdot2_f32_bf16(__builtin_bit_cast(bf16x2, a), __builtin_bit_cast(bf16x2, b), c, false);
}
constexpr unsigned ONE2 = 0x3f803f80u;
DI f32x16 zero16() { f32x16 z; for (int i = 0; i < 16; ++i) z[i] = 0.f; return z; }
DI u32x4 pack8(const f32x16& a, int u) {
  return u32x4{pk2(a[8 * u], a[8 * u + 1]), pk2(a[8 * u + 2], a[8 * u + 3]), pk2(a[8 * u + 4], a[8 * u + 5]), pk2(a[8 * u + 6], a[8 * u + 7])};
}
DI void poll(GAS u64* fl, int n, int idx, u64 tag) {
  const bool on = (int)(threadIdx.x & 63) < n;
#pragma unroll 1
  for (unsigned spins = 0; spins < SPIN_CAP; ++spins) {
    u64 v = tag;
    if (on) v = __hip_atomic_load(fl + idx * 16, __ATOMIC_RELAXED, __HIP_MEMORY_SCOPE_AGENT);
    if (__all(v == tag)) break;
    __builtin_amdgcn_s_sleep(1);
  }
  wvm<0>();
}
DI void publish(srd_t ws, u32x2 tg, unsigned idx, bool on) { st8s(tg, ws, on ? idx * 128 : OOB, OFF_FLG); }
DI bool all_set(u32x2 f, u32x2 tg) { return __all(f.x == tg.x && f.y == tg.y); }

#define ST(i) do {} while (0)

template <int NU> DI f32x16 colsum(unsigned a) {
  const u32x4 ones = {ONE2, ONE2, ONE2, ONE2};
  f32x16 c = zero16();
  sfor<0, NU>([&](auto UU) { c = mfma(trfrag(a + UU.v * 2048, a + UU.v * 2048 + 1024), ones, c); });
  return c;
}
DI void cs_write(unsigned dst, const f32x16& c, int lane) {
  if ((lane & 31) == 0) {
    const unsigned d = dst + (lane >> 5) * 16;
    sfor<0, 4>([&](auto A) { ldsw<f32x4>(d + A.v * 32, f32x4{c[4 * A.v], c[4 * A.v + 1], c[4 * A.v + 2], c[4 * A.v + 3]}); });
  }
}
DI float max3(float a, float b, float c) { float r; asm("v_max3_f32 %0, %1, %2, %3" : "=v"(r) : "v"(a), "v"(b), "v"(c)); return r; }
DI float min3(float a, float b, float c) { float r; asm("v_min3_f32 %0, %1, %2, %3" : "=v"(r) : "v"(a), "v"(b), "v"(c)); return r; }
DI void dual_softmax(f32x16 p, bool hi, u32x4 (&o)[4]) {
  p[0] += 0.f;
#pragma unroll
  for (int i = 12; i < 16; ++i) p[i] = hi ? p[0] : p[i];
  float mx = max3(p[0], p[1], p[2]), mn = min3(p[0], p[1], p[2]);
#pragma unroll
  for (int i = 3; i < 15; i += 2) { mx = max3(mx, p[i], p[i + 1]); mn = min3(mn, p[i], p[i + 1]); }
  float a, b;
  halves(max3(mx, p[15], p[15]), a, b);
  const float cp = -LOG2E * max3(a, b, b);
  halves(min3(mn, p[15], p[15]), a, b);
  const float cm = LOG2E * min3(a, b, b);
  f32x2 ep[8], em[8];
#pragma unroll
  for (int i = 0; i < 8; ++i) {
    const f32x2 v = {p[2 * i], p[2 * i + 1]}, x = v * LOG2E + cp, y = v * -LOG2E + cm;
    ep[i] = f32x2{__builtin_amdgcn_exp2f(x[0]), __builtin_amdgcn_exp2f(x[1])};
    em[i] = f32x2{__builtin_amdgcn_exp2f(y[0]), __builtin_amdgcn_exp2f(y[1])};
  }
  const float keep = hi ? 0.f : 1.f;
  const f32x2 sp = ((ep[0] + ep[1]) + (ep[2] + ep[3])) + ((ep[4] + ep[5]) + (ep[6] + ep[7]) * keep);
  const f32x2 sn = ((em[0] + em[1]) + (em[2] + em[3])) + ((em[4] + em[5]) + (em[6] + em[7]) * keep);
  const float rp = __builtin_amdgcn_rcpf(swap_add(sp[0] + sp[1])), rm = __builtin_amdgcn_rcpf(swap_add(sn[0] + sn[1]));
#pragma unroll
  for (int i = 0; i < 8; ++i) {
    const f32x2 x = ep[i] * (i < 6 ? rp : rp * keep), y = em[i] * (i < 6 ? rm : rm * keep);
    o[i >> 2][i & 3] = pk2(x[0], x[1]);
    o[2 + (i >> 2)][i & 3] = pk2(y[0], y[1]);
  }
}
DI void granule(u32x4& g, srd_t ws, unsigned voff, u32x2 tg) {
#pragma unroll 1
  for (unsigned spins = 0; spins < SPIN_CAP && !__all(g[1] == tg.x && g[3] == tg.y); ++spins) {
    ld16s<0>(g, voff, ws, OFF_ST0);
    wvm<0>(g);
  }
}
DI f32x16 cs_read(unsigned a, unsigned str) {
  f32x4 v[4][4];
  sfor<0, 4>([&](auto P) { sfor<0, 4>([&](auto A) { v[P.v][A.v] = ldsr<f32x4>(a + P.v * str + A.v * 32); }); });
  f32x16 r;
  sfor<0, 4>([&](auto A) {
    const f32x4 t = (v[0][A.v] + v[1][A.v]) + (v[2][A.v] + v[3][A.v]);
    sfor<0, 4>([&](auto I) { r[4 * A.v + I.v] = t[I.v]; });
  });
  return r;
}
DI f32x4 quad(const f32x16& c, int a) { return f32x4{c[4 * a], c[4 * a + 1], c[4 * a + 2], c[4 * a + 3]}; }
DI u32x4 cross(u32x4 v) {
  auto a = __builtin_amdgcn_permlane32_swap(v[0], v[2], false, false);
  auto b = __builtin_amdgcn_permlane32_swap(v[1], v[3], false, false);
  return u32x4{a[0], b[0], a[1], b[1]};
}

#define STARG
#define STPASS
DI void code_prefetch(unsigned& d, u64 pc, unsigned voff) {
  asm volatile("buffer_load_dword %0, %1, %2, 0 offen" : "=v"(d) : "v"(voff), "s"(mk((const void*)(pc & ~127ull), CODE_BYTES)) : "memory");
}
DI u64 make_tag(u32x2 ev) {
  return (((u64)(unsigned)__builtin_amdgcn_readfirstlane(ev.y) << 32) | (unsigned)__builtin_amdgcn_readfirstlane(ev.x)) + 0x9E3779B97F4A7C15ull;
}

struct SlotP1 {
  static constexpr unsigned off(int c) { return R2 - 16384 + ((c + 1) % 4) * 16384; }
  static DI unsigned addr(const unsigned (&xr)[4], int U) { return xr[U & 3] + ((U / 4 + 1) % 4) * 16384; }
};
struct SlotP3 {
  static constexpr unsigned off(int c) { return c < 3 ? RA + c * 16384 : c < 6 ? R2 + (c - 3) * 16384 : RA; }
  static DI unsigned addr(const unsigned (&xr)[4], int U) { return xr[U >= 12 && U < 24] + (U / 4 % 3) * 16384 + (U & 3) * 1024; }
};
constexpr unsigned HREG = R2;
constexpr unsigned XT = R2 + 40960;
constexpr int UH = 12;
constexpr int M3 = 20;
template <int TS, class Slot>
DI void pass_a(f32x16& c0, f32x16& c1, const unsigned a0, const unsigned a1, const unsigned (&xr)[4], u32x4* xf) {
  auto rx = [&](auto UU) { xf[UU.v] = ldsr<u32x4>(Slot::addr(xr, UU.v)); };
  u32x4 wf[3][2];
  auto rd = [&](auto UU, auto NN) {
    const unsigned ad = (NN.v ? a1 : a0) + UU.v * 2048;
    wf[UU.v % 3][NN.v] = trfrag(ad, ad + TS);
  };
  bar();
  rx(IC<0>{}); rd(IC<0>{}, IC<0>{}); rd(IC<0>{}, IC<1>{}); rx(IC<1>{}); rd(IC<1>{}, IC<0>{}); rd(IC<1>{}, IC<1>{});
  sfor<0, 28>([&](auto UU) {
    constexpr int U = UU.v;
    if constexpr (U % 4 == 2 && U < 26) asm volatile("s_barrier" ::: "memory");
    mmx<U == 0>(c0, wf[U % 3][0], xf[U]);
    if constexpr (U + 2 < 28) { rx(IC<U + 2>{}); rd(IC<U + 2>{}, IC<0>{}); }
    SB();
    mmx<U == 0>(c1, wf[U % 3][1], xf[U]);
    if constexpr (U + 2 < 28) rd(IC<U + 2>{}, IC<1>{});
    SB();
  });
}
DI int rot7(int c, int rot) { const int v = c + rot; return v >= 7 ? v - 7 : v; }
struct Geo {
  int r7, s7, g4, tt, s14, rot;
  bool wtype;
  DI Geo(int xc, int r) {
    r7 = r / 7;
    s7 = r - 7 * r7;
    g4 = 4 * (xc >> 1) + r7;
    tt = 4 * xc + r7;
    s14 = 7 * (xc & 1) + s7;
    rot = (s7 + 2 * r7) % 7;
    wtype = (xc & 1) == 0;
  }
};
DI unsigned p4_slot(int kq, int j) {
  return j < 2 ? R2 + 16384 + (2 * kq + j) * 4096 : j < 5 ? R1 + (3 * kq + j - 2) * 4096 : j < 8 ? RA + (3 * kq + j - 5) * 4096 : (kq < 2 ? R1 : RA) + 49152 + (kq & 1) * 4096;
}
DI int p4_k32(int kq, int s, int rot) {
  const int a = 7 * kq + rot7(s < 7 ? s : s - 7, rot);
  return 4 * (a >> 1) + (a & 1) + (s < 7 ? 0 : 2);
}

DI void first_loads(const Params& p, const int w, const int xc, const int r) {
  const int lane = threadIdx.x & 63;
  const Geo g(xc, r);
  const int rr = lane >> 3, cp = lane & 7;
  const unsigned wsw = (unsigned)((cp ^ (((rr >> 1) & 1) << 2)) << 4);
  const srd_t sX = mk(p.x, 2048 * 896), sQ = mk(p.wqkv, 448 * 2688);
  const srd_t sA = g.wtype ? mk(p.w1, 448 * 896) : sQ;
  const unsigned pitchA = g.wtype ? 896 : 2688;
  const unsigned colA = g.wtype ? g.s7 : 7 + g.s7;
  const unsigned wvA = rr * pitchA + wsw;
  const unsigned xv0 = rr * 896 + ((cp ^ (rr >> 1)) << 4), xv1 = xv0 ^ 64u;
  const unsigned T32 = 4 * g.g4 + w, xs = T32 * 32 * 896;
  auto G1 = [&](int c) {
    const int cg = rot7(c, g.rot);
    sfor<0, 2>([&](auto J) { dma16(wvA, sA, (8 * cg + 2 * w + J.v) * 8 * pitchA + colA * 128, RA + (8 * c + 2 * w + J.v) * 1024); });
    sfor<0, 4>([&](auto I) { dma16((I.v & 1) ? xv1 : xv0, sX, xs + I.v * 8 * 896 + cg * 128, SlotP1::off(c) + w * 4096 + I.v * 1024); });
  };
  __builtin_amdgcn_s_setprio(3);
  G1(0); G1(1);
}

DI void loader_path(const Params& p, const int w, const int xc, const int r STARG) {
  const int lane = threadIdx.x & 63, t32 = lane & 31, h = lane >> 5;
  const Geo g(xc, r);
  const int rr = lane >> 3, cp = lane & 7;
  const unsigned wsw = (unsigned)((cp ^ (((rr >> 1) & 1) << 2)) << 4);
  const srd_t sWS = mk(p.ws, WS_USED), sX = mk(p.x, 2048 * 896), sQ = mk(p.wqkv, 448 * 2688);
  const srd_t sA = g.wtype ? mk(p.w1, 448 * 896) : sQ;
  const unsigned pitchA = g.wtype ? 896 : 2688;
  const unsigned colA = g.wtype ? g.s7 : 7 + g.s7, colB = g.wtype ? g.s7 : 14 + g.s7;
  const unsigned wvA = rr * pitchA + wsw;
  const unsigned xv0 = rr * 896 + ((cp ^ (rr >> 1)) << 4), xv1 = xv0 ^ 64u;
  const unsigned T32 = 4 * g.g4 + w, xs = T32 * 32 * 896;
  auto G1 = [&](int c) {
    const int cg = rot7(c, g.rot);
    sfor<0, 2>([&](auto J) { dma16(wvA, sA, (8 * cg + 2 * w + J.v) * 8 * pitchA + colA * 128, RA + (8 * c + 2 * w + J.v) * 1024); });
    sfor<0, 4>([&](auto I) { dma16((I.v & 1) ? xv1 : xv0, sX, xs + I.v * 8 * 896 + cg * 128, SlotP1::off(c) + w * 4096 + I.v * 1024); });
  };
  u32x2 ev;
  ld8s<0>(ev, 0u, sWS, OFF_FLG);
  SB();
  const unsigned wv896 = rr * 896 + wsw, wv3 = rr * 3584 + wsw, wvB = rr * 2688 + wsw;
  auto WB = [&](int k) { dma16(wvB, sQ, (8 * rot7(k >> 1, g.rot) + 4 * (k & 1) + w) * 8 * 2688 + colB * 128, R1 + (4 * k + w) * 1024); };
  const int q = (lane & 15) >> 2, pp = lane & 3, blk = (lane >> 4) & 1;
  const unsigned ca = (4 * h + q) * 128 + (unsigned)((((2 * blk + (pp >> 1)) ^ ((q >> 1) << 2)) << 4) + (pp & 1) * 8);
  const u32x4 ones = {ONE2, ONE2, ONE2, ONE2};
  f32x16 s0 = zero16(), s1 = zero16();
  auto CS = [&](unsigned a) { s0 = mfma(trfrag(a, a + 1024), ones, s0); s1 = mfma(trfrag(a ^ 64u, (a ^ 64u) + 1024), ones, s1); };
  const unsigned cw = RA + ca + w * 2048;
  u32x4 gr;
  wvm<7>(); bar(); G1(2); CS(cw);
  wvm<7>(); bar(); G1(3); CS(cw + 8192);
  wvm<6>(ev); bar(); G1(4); CS(cw + 2 * 8192);
  wvm<6>(); bar(); G1(5); CS(cw + 3 * 8192);
  ld16s<0>(gr, (T32 * 32 + t32) * 16, sWS, OFF_ST0);
  wvm<7>(); bar(); G1(6); CS(cw + 4 * 8192);
  const u64 tag = make_tag(ev);
  const u32x2 tg = {(unsigned)tag, (unsigned)(tag >> 32)};
  wvm<6>(gr); bar(); sfor<0, 3>([&](auto K) { WB(K.v); });
  CS(cw + 5 * 8192);
  granule(gr, sWS, (T32 * 32 + t32) * 16, tg);
  const float ve1 = u2f(gr[2]);
  float r1 = __builtin_amdgcn_rsqf(ve1);
  const float v2 = (ve1 - 1e-5f) * r1 * r1 + 1e-5f;
  GeluK k1 = gelu_k(r1 * __builtin_amdgcn_rsqf(v2), __builtin_amdgcn_sqrtf(ve1 * v2));
  k1[3] = u2f(gr[0]);
  tie(k1); tie(r1);
  cs_write(R2 - 16384 + w * 128, s0, lane); cs_write(R2 - 16384 + 512 + w * 128, s1, lane);
  wvm<3>(); bar();
  {
    const unsigned a = R2 - 16384 + h * 512 + t32 * 4, c6 = (RA + ca + 24 * 2048) ^ ((w & 1) * 64);
    u32x4 f[4];
    sfor<0, 4>([&](auto U) { f[U.v] = trfrag(c6 + U.v * 2048, c6 + U.v * 2048 + 1024); });
    const float t = (ldsr<float>(a) + ldsr<float>(a + 128)) + (ldsr<float>(a + 256) + ldsr<float>(a + 384));
    s0 = zero16();
    sfor<0, 4>([&](auto U) { s0 = mfma(f[U.v], ones, s0); });
    sfor<3, 6>([&](auto K) { WB(K.v); });
    ldsw<f32x4>(R2 + 2048 + w * 1024 + lane * 16, k1);
    ldsw<float>(R2 + 6144 + w * 256 + lane * 4, r1);
    ldsw<float>(R2 + 1024 + lane * 4, t);
    cs_write(R2 + 1280 + (w & 1) * 128, s0, lane);
  }
  GAS u64* fl = (GAS u64*)(p.ws + OFF_FLG);
  const bool l0 = threadIdx.x == 256;
  bar();
  __builtin_amdgcn_s_setprio(0);
  sfor<6, 14>([&](auto K) { WB(K.v); });
  sfor<0, 2>([&](auto J) { dma16(rr * 896 + ((cp ^ rr) << 4), sX, (g.tt * 64 + (2 * w + J.v) * 8) * 896 + g.s7 * 128, XT + (2 * w + J.v) * 1024); });
  const srd_t sW2 = mk(p.w2, 448 * 896);
  auto W2 = [&](int k) { const int pc = 8 * rot7(k >> 1, g.rot) + 4 * (k & 1) + w; dma16(wv896, sW2, pc * 8 * 896 + g.s7 * 128, RA + pc * 1024); };
  sfor<0, 4>([&](auto K) { W2(K.v); });
  wvm<4>(); bar();
  sfor<4, 14>([&](auto K) { W2(K.v); });
  const srd_t sW4 = mk(p.w4, 1792 * 896), sWo = mk(p.wo, 448 * 896), sW3 = mk(p.w3, 448 * 3584);
  u32x4 w4r[9][4], w3r[14];
  auto L4 = [&](auto SS) {
    constexpr int s = SS.v;
    sfor<0, 4>([&](auto I) {
      u32x4& d = s < 9 ? w3r[4 * (s - 5) + I.v] : w4r[s < 9 ? 0 : s - 9][I.v];
      if constexpr (s < 14) ld16p(d, wv896, sW4, (32 * p4_k32(w, s, g.rot) + 8 * I.v) * 896 + g.s7 * 128);
      else ld16p(d, wv896, sWo, (28 * (4 * w + ((s + g.rot) & 3)) + 8 * I.v) * 896 + g.s7 * 128);
    });
  };
  bar();
  publish(sWS, tg, FL_A + g.g4 * 14 + g.s14, l0);
  poll(fl, 1, FL_CS + 28 + g.s14, tag);
  asm volatile("s_mov_b32 m0, %3\n\ts_nop 0\n\tbuffer_load_dword %0, %1, %2 offen sc1 lds" ::"v"(lane * 4), "s"(sWS), "s"(OFF_CS3 + (28 + g.s14) * 256), "s"(XT) : "memory");
  {
    const int th = w & 1, kh = w >> 1, tc = 2 * g.tt + th;
    const int gsel = min(max(tc + ((lane & 4) ? 2 * th - 1 : 0), 0), 63) >> 2;
    poll(fl, 8, FL_A + gsel * 14 + min(3 * kh + (lane & 3), 6), tag);
    const srd_t sH = mk(p.ws + OFF_H, 64 * 28 * 1024);
    const int tok = tc * 32 + t32, ta = tok - 1 - kh, tb = tok + 1 + kh;
    const unsigned oa = (unsigned)((ta >> 5) * 28 * 1024 + ((ta & 31) + 32 * h) * 16), ob = (unsigned)((tb >> 5) * 28 * 1024 + ((tb & 31) + 32 * h) * 16);
    sfor<0, 6>([&](auto U) { dma16s(oa, sH, (kh * 14 + U.v) * 1024, HREG + w * 10240 + U.v * 1024); });
    bar();
    dma16s(oa, sH, (kh * 14 + 6) * 1024, HREG + w * 10240 + 6144);
    sfor<7, 10>([&](auto U) { dma16s(ob, sH, (kh * 14 + U.v) * 1024, HREG + w * 10240 + U.v * 1024); });
  }
  wvm<0>();
  bar();
  auto o3 = [&](int k) { return (unsigned)((8 * rot7(k >> 1, g.rot) + 4 * (k & 1) + w) * 8 * 3584 + g.s14 * 256); };
  sfor<0, 12>([&](auto K) { dma16(wv3, sW3, o3(K.v), R1 + (4 * K.v + w) * 1024); });
  bar();
  publish(sWS, tg, FL_Q + g.g4 * 14 + g.s14, l0);
  sfor<12, 14>([&](auto K) { dma16(wv3, sW3, o3(K.v), R1 + (4 * K.v + w) * 1024); });
  sfor<0, 8>([&](auto K) { ld16p(w3r[K.v], wv3, sW3, o3(K.v) + 128); });
  bar();
  publish(sWS, tg, FL_X + g.tt * 7 + g.s7, l0);
  wvm<9>(); bar();
  sfor<8, 14>([&](auto K) { ld16p(w3r[K.v], wv3, sW3, o3(K.v) + 128); });
  sfor<9, 18>(L4);
  wvm<36>();
  bar();
  sfor<0, 14>([&](auto K) { tie(w3r[K.v]); ldsw<u32x4>(RA + (4 * K.v + w) * 1024 + lane * 16, w3r[K.v]); });
  auto W4 = [&](auto SS) {
    sfor<0, 4>([&](auto I) { dma16(wv896, sW4, (32 * p4_k32(w, SS.v, g.rot) + 8 * I.v) * 896 + g.s7 * 128, p4_slot(w, SS.v) + I.v * 1024); });
  };
  sfor<0, 2>(W4);
  wvm<8>();
  bar();
  sfor<2, 5>(W4);
  bar();
  publish(sWS, tg, FL_3A + g.g4 * 14 + g.s14, l0);
  sfor<5, 8>(L4);
  bar();
  W4(IC<8>{});
  const unsigned f3 = (g.tt >> 1) * 14 + (7 * w >> 1) + (lane & 3);
  poll(fl, 4, FL_3A + f3, tag);
  bar();
  sfor<0, 12>([&](auto K) { tie(w3r[K.v]); ldsw<u32x4>(p4_slot(w, 5 + K.v / 4) + (K.v & 3) * 1024 + lane * 16, w3r[K.v]); });
  bar();
  publish(sWS, tg, FL_3B + g.g4 * 14 + g.s14, l0);
  auto S4 = [&](auto J) { sfor<0, 4>([&](auto I) { tie(w4r[J.v][I.v]); ldsw<u32x4>(p4_slot(w, J.v) + I.v * 1024 + lane * 16, w4r[J.v][I.v]); }); };
  poll(fl, 4, FL_3B + f3, tag);
  bar();
  u32x4 x1[2];
  sfor<0, 2>([&](auto U) { ld16s<U.v * 1024>(x1[U.v], lane * 16, sWS, OFF_X1 + ((2 * g.tt + (w & 1)) * 28 + 4 * g.s7 + (w & 2)) * 1024); });
  sfor<0, 4>(S4);
  wvm2<0>(x1[0], x1[1]);
  bar(); sfor<4, 8>(S4);
  poll(fl, 4, FL_O + (g.tt >> 1) * 16 + 4 * w + (lane & 3), tag);
  bar(); S4(IC<8>{});
  bar();
  __builtin_amdgcn_s_setprio(3);
  sfor<0, 4>([&](auto A) {
    f32x4 b = ldsr<f32x4>(p4_slot(w, 0) + A.v * 1024 + lane * 16);
    f32x4 o[3];
    sfor<0, 3>([&](auto P) { o[P.v] = ldsr<f32x4>(R1 + (w ^ (P.v + 1)) * 12288 + (4 * P.v + A.v) * 1024 + lane * 16); });
    const unsigned y0 = x1[A.v >> 1][2 * (A.v & 1)], y1 = x1[A.v >> 1][2 * (A.v & 1) + 1];
    const float xx[4] = {bflo(y0), bfhi(y0), bflo(y1), bfhi(y1)};
    sfor<0, 4>([&](auto I) {
      float t = b[I.v] + o[0][I.v], u = o[1][I.v] + o[2][I.v];
      tie(t); tie(u);
      t = (t + xx[I.v]) + u;
      b[I.v] = t * __builtin_amdgcn_rcpf(1.f + __builtin_amdgcn_exp2f(-LOG2E * t));
    });
    ldsw<f32x4>(p4_slot(w, 1) + A.v * 1024 + lane * 16, b);
  });
  bar();
}

DI void compute_path(const Params& p, unsigned& dummy, const int w, const int xc, const int r STARG) {
  const int tid = threadIdx.x, lane = tid & 63, t32 = lane & 31, h = lane >> 5, th = w & 1, kh = w >> 1;
  const Geo g(xc, r);
  const srd_t sWS = mk(p.ws, WS_USED);
  __builtin_amdgcn_s_setprio(2);
  unsigned dm[4];
  {
    const unsigned id = ((xc * 28 + r) * 4 + w) * 35 + lane;
    const bool on = lane < 35;
    asm volatile("s_sleep 24");
    ld4p(dm[0], on && id < 3136 ? id * 128 : OOB, mk(p.w2, 448 * 896));
    ld4p(dm[1], on && id - 3136 < 12544 ? (id - 3136) * 128 : OOB, mk(p.w3, 448 * 3584));
    ld4p(dm[2], on && id - 15680 < 12544 ? (id - 15680) * 128 : OOB, mk(p.w4, 1792 * 896));
    ld4p(dm[3], on && id - 28224 < 3136 ? (id - 28224) * 128 : OOB, mk(p.wo, 448 * 896));
  }
  u32x2 ev;
  ld8s<0>(ev, 0u, sWS, OFF_FLG);
  const unsigned T32 = 4 * g.g4 + w, tc = 2 * g.tt + th;
  const int ftok = tc * 32 + t32;
  u32x4 xv[2], x1p[2], gr;
  f32x16 xn;
  const int q = (lane & 15) >> 2, pp = lane & 3, blk = (lane >> 4) & 1;
  unsigned aperm = (4 * h + q) * 128 + (((2 * blk + (pp >> 1)) ^ ((q >> 1) << 2)) << 4) + (pp & 1) * 8, l16 = lane * 16;
  auto tra = [&](int hs, unsigned base, unsigned x) {
    tie(aperm);
    unsigned a = base + (aperm ^ x);
    if (hs == 8) a += l16 & 512;
    tie(a);
    return a;
  };
  auto lnv = [&](unsigned base) { tie(l16); unsigned a = base + l16; tie(a); return a; };
  u32x4 xf[28];
  unsigned xr[4], a0 = tra(8, RA, 0), a1 = tra(8, RA, 64);
  sfor<0, 4>([&](auto I) { xr[I.v] = R2 - 16384 + w * 4096 + t32 * 128 + (((2 * I.v + h) ^ ((t32 >> 1) & 7)) << 4); tie(xr[I.v]); });
  f32x16 c0, c1, c2, c3;
  pass_a<512, SlotP1>(c0, c1, a0, a1, xr, xf);
  bar();
  settle(c0, c1);
  f32x4 cst[8];
  const GeluK gk1 = ldsr<f32x4>(R2 + 2048 + w * 1024 + lane * 16);
  const float mean1 = gk1[3], r1 = ldsr<float>(R2 + 6144 + w * 256 + lane * 4);
  auto cs_load = [&](unsigned a) {
    sfor<0, 8>([&](auto A) { cst[A.v] = ldsr<f32x4>(a + A.v * 32 + h * 16); });
    sfor<0, 8>([&](auto A) { sfor<0, 4>([&](auto I) { (A.v < 4 ? c2 : c3)[4 * (A.v & 3) + I.v] = cst[A.v][I.v]; }); });
  };
  cs_load(R2 + 1024);
  sfor<0, 8>([&](auto A) { const f32x4 t = ldsr<f32x4>(R2 + 1280 + A.v * 32 + h * 16); sfor<0, 4>([&](auto I) { (A.v < 4 ? c2 : c3)[4 * (A.v & 3) + I.v] += t[I.v]; }); });
  wvm<0>(ev);
  tie(dummy); tie(dm[0]); tie(dm[1]); tie(dm[2]); tie(dm[3]);
  const u64 tag = make_tag(ev);
  const u32x2 tg = {(unsigned)tag, (unsigned)(tag >> 32)};
  auto epi = [&](auto ACT, unsigned so) {
    unsigned pk[4];
    sfor<0, 16>([&](auto J) {
      constexpr int e = 2 * (J.v & 7);
      const f32x16 &c = J.v < 8 ? c0 : c1, &s = J.v < 8 ? c2 : c3;
      f32x2 z = f32x2{c[e], c[e + 1]} - f32x2{s[e], s[e + 1]} * mean1;
      if constexpr (ACT.v) z = gelu2(z, gk1); else z *= r1;
      pk[J.v & 3] = pk2(z[0], z[1]);
      if constexpr ((J.v & 3) == 3) st16s<(J.v >> 2) * 1024>(u32x4{pk[0], pk[1], pk[2], pk[3]}, sWS, l16, so);
    });
  };
  if (g.wtype) epi(IC<1>{}, OFF_H + (T32 * 28 + 4 * g.s7) * 1024);
  else epi(IC<0>{}, OFF_QKV + (T32 * 84 + 28 + 4 * g.s7) * 1024);
  ld16s<0>(gr, ftok * 16, sWS, OFF_ST0);
  bar();
  ST(3);
  {
    u32x4 wf[3][2];
    float m0, r0;
    auto rd = [&](auto UU, auto NN) {
      const unsigned ad = (NN.v ? a1 : a0) + UU.v * 2048;
      wf[UU.v % 3][NN.v] = trfrag(ad, ad + 512);
    };
    a0 = tra(8, R1, 0); a1 = tra(8, R1, 64);
    rd(IC<0>{}, IC<0>{}); rd(IC<0>{}, IC<1>{}); rd(IC<1>{}, IC<0>{}); rd(IC<1>{}, IC<1>{});
    sfor<0, 28>([&](auto UU) {
      constexpr int U = UU.v;
      mmx<U == 0>(c0, wf[U % 3][0], xf[U]);
      if constexpr (U == UH) {
        wvm<0>(gr);
        bar();
        granule(gr, sWS, ftok * 16, tg);
      }
      if constexpr (U + 2 < 28) rd(IC<U + 2>{}, IC<0>{});
      if constexpr (U >= 2 && U < 6) {
        constexpr int A = U - 2;
        const u32x2 t = ldsr<u32x2>(XT + (th * 32 + t32) * 128 + (((4 * kh + A) ^ (t32 & 7)) << 4) + h * 8);
        xv[A >> 1][2 * (A & 1)] = t.x; xv[A >> 1][2 * (A & 1) + 1] = t.y;
      }
      SB();
      mmx<U == 0>(c1, wf[U % 3][1], xf[U]);
      if constexpr (U + 2 < 28) rd(IC<U + 2>{}, IC<1>{});
      if constexpr (U == UH + 1) { r0 = __builtin_amdgcn_rsqf(u2f(gr[2])); m0 = -u2f(gr[0]) * r0; tie(r0); tie(m0); }
      if constexpr (U >= 20) {
        constexpr int I = U - 20;
        float a = fmaf(bflo(xv[I >> 2][I & 3]), r0, m0), b = fmaf(bfhi(xv[I >> 2][I & 3]), r0, m0);
        tie(a); tie(b);
        xn[2 * I] = a; xn[2 * I + 1] = b;
      }
      SB();
    });
  }
  ST(4);
  bar();
  {
    const srd_t sH = mk(p.ws + fresh(OFF_H), 64 * 28 * 1024);
    const int tb = ftok + 1 + fresh(kh);
    const unsigned ob = (unsigned)((tb >> 5) * 28 * 1024 + ((tb & 31) + 32 * h) * 16);
    sfor<10, 14>([&](auto U) { ld16s<(U.v - 10) * 1024>(xf[U.v], ob, sH, (fresh(kh) * 14 + 10) * 1024); });
  }
  cs_load(XT);
  settle(c0, c1);
  epi(IC<0>{}, OFF_QKV + (fresh(T32) * 84 + (g.wtype ? 0 : 56) + 4 * g.s7) * 1024);
  ST(5);
  bar();
  ST(6);
  c0 = xn;
  {
    a0 = tra(4, RA + fresh(kh) * 14 * 2048, fresh(kh) * 64); a1 = a0 ^ 64u; tie(a1);
    const unsigned hb = lnv(HREG + fresh(w) * 10240);
    u32x4 wf[3][2];
    auto rd = [&](auto UU) {
      if constexpr (UU.v < 10) xf[UU.v] = ldsr<u32x4>(hb + UU.v * 1024);
      wf[UU.v % 3][0] = trfrag(a0 + UU.v * 2048, a0 + UU.v * 2048 + 1024); wf[UU.v % 3][1] = trfrag(a1 + UU.v * 2048, a1 + UU.v * 2048 + 1024);
    };
    rd(IC<0>{}); rd(IC<1>{});
    sfor<0, 14>([&](auto UU) {
      constexpr int U = UU.v;
      if constexpr (U == 10) wvm4<4>(xf[10], xf[11], xf[12], xf[13]);
      mma(c0, wf[U % 3][0], xf[U]);
      if constexpr (U + 2 < 14) rd(IC<U + 2>{});
      mmx<U == 0>(c1, wf[U % 3][1], xf[U]);
      SB();
    });
    settle(c0, c1);
    sfor<0, 4>([&](auto A) { ldsw<f32x4>(hb + A.v * 1024, quad(c1, A.v)); });
  }
  wvm<0>();
  bar();
  ST(27);
  {
    f32x4 o[4];
    const unsigned pb = lnv(HREG + (fresh(w) ^ 2) * 10240);
    sfor<0, 4>([&](auto A) { o[A.v] = ldsr<f32x4>(pb + A.v * 1024); });
    sfor<0, 4>([&](auto A) {
      const f32x4 t = quad(c0, A.v) + o[A.v];
      x1p[A.v >> 1][2 * (A.v & 1)] = pk2(t[0], t[1]);
      x1p[A.v >> 1][2 * (A.v & 1) + 1] = pk2(t[2], t[3]);
    });
    const unsigned so = OFF_X1 + (fresh(tc) * 28 + 4 * g.s7 + 2 * kh) * 1024;
    st16s(x1p[0], sWS, l16, so); st16s<1024>(x1p[1], sWS, l16, so);
    float a1 = 0.f, a2 = 0.f;
    sfor<0, 8>([&](auto I) { a1 = dot2(x1p[I.v >> 2][I.v & 3], ONE2, a1); a2 = dot2(x1p[I.v >> 2][I.v & 3], x1p[I.v >> 2][I.v & 3], a2); });
    st8s(u32x2{f2u(swap_add(a1)), f2u(swap_add(a2))}, sWS, h == 0 ? ftok * 8 : OOB, OFF_ST1 + (2 * fresh(g.s7) + kh) * 2048 * 8);
  }
  ST(7);
  wvm<0>();
  bar();
  ST(8);
  bar();
  GeluK gk3;
  float mean3;
  {
    a0 = tra(4, R1, 0); a1 = tra(4, R1, 64);
    xr[0] = lnv(RA + fresh(w) * 4096); xr[1] = lnv(R2 + fresh(w) * 4096);
    poll((GAS u64*)(p.ws + OFF_FLG), 9, lane >= 7 ? FL_CS + 2 * g.s14 + (lane & 1) : FL_X + (T32 >> 1) * 7 + lane, tag);
    ST(11);
    auto X1 = [&](auto UU) {
      constexpr int c = UU.v / 4, i = UU.v % 4;
      dma16s(l16, sWS, OFF_X1 + (T32 * 28 + 4 * rot7(c, fresh(g.rot)) + i) * 1024, SlotP3::off(c) + fresh(w) * 4096 + i * 1024);
    };
    u32x2 sp[7], cs3;
    sfor<0, 8>(X1);
    sfor<0, 7>([&](auto I) { ld8s<0>(sp[I.v], (T32 * 32 + t32 + h * 7 * 2048) * 8, sWS, OFF_ST1 + I.v * 2048 * 8); });
    ld8s<0>(cs3, lane * 8, sWS, OFF_CS3 + g.s14 * 512);
    sfor<8, 12>(X1);
    u32x4 wf[3][2];
    auto rd = [&](auto UU, auto NN) { const unsigned ad = (NN.v ? a1 : a0) + UU.v * 2048; wf[UU.v % 3][NN.v] = trfrag(ad, ad + 1024); };
    auto rx = [&](auto UU) {
      constexpr int U = UU.v;
      wvm<(U < 2 ? 19 - U : U < 8 ? 17 : U <= 18 ? 9 : 27 - U)>();
      xf[U] = ldsr<u32x4>(SlotP3::addr(xr, U));
    };
    rd(IC<0>{}, IC<0>{}); rd(IC<0>{}, IC<1>{}); rd(IC<1>{}, IC<0>{}); rd(IC<1>{}, IC<1>{});
    rx(IC<0>{}); rx(IC<1>{});
    sfor<0, 28>([&](auto UU) {
      constexpr int U = UU.v;
      mmx<U == 0>(c0, wf[U % 3][0], xf[U]);
      if constexpr (U + 2 < 28) { rx(IC<U + 2>{}); rd(IC<U + 2>{}, IC<0>{}); }
      SB();
      mmx<U == 0>(c1, wf[U % 3][1], xf[U]);
      if constexpr (U + 2 < 28) rd(IC<U + 2>{}, IC<1>{});
      if constexpr (U + 12 < 28) X1(IC<U + 12>{});
      if constexpr (U == 17) {
        float sm = 0.f, sq = 0.f;
        sfor<0, 7>([&](auto I) { tie(sp[I.v]); sm += u2f(sp[I.v].x); sq += u2f(sp[I.v].y); });
        mean3 = swap_add(sm) * (1.f / D);
        const float ve = fmaxf(swap_add(sq) * (1.f / D) - mean3 * mean3, 0.f) + 1e-5f;
        gk3 = gelu_k(__builtin_amdgcn_rsqf(ve), __builtin_amdgcn_sqrtf(ve));
        tie(gk3); tie(mean3);
      }
      if constexpr (U == 19) { tie(cs3); ldsw<u32x2>(R2 + fresh(w) * 4096 + lane * 8, cs3); }
      SB();
    });
  }
  bar();
  ST(13);
  unsigned csb = R2 + fresh(w) * 4096 + h * 16;
  tie(csb);
  auto CS3 = [&](int t) { sfor<0, 8>([&](auto A) { cst[A.v] = ldsr<f32x4>(csb + t * 128 + A.v * 32); }); };
  const unsigned so3 = OFF_H3 + (fresh(T32) * 112 + 8 * g.s14) * 1024;
  unsigned pk[4];
  auto G = [&](auto JJ) {
    constexpr int j = JJ.v, t = j >> 3, e = 2 * (j & 7);
    const f32x16& c = t == 0 ? c0 : t == 1 ? c1 : t == 2 ? c2 : c3;
    const f32x2 z = gelu2(f32x2{c[e], c[e + 1]} - f32x2{cst[4 * (t & 1) + (e >> 2)][e & 3], cst[4 * (t & 1) + (e >> 2)][(e & 3) + 1]} * mean3, gk3);
    pk[j & 3] = pk2(z[0], z[1]);
    if constexpr ((j & 3) == 3) st16s<(j >> 2 & 3) * 1024>(u32x4{pk[0], pk[1], pk[2], pk[3]}, sWS, l16, so3 + (j >> 4) * 4096);
  };
  CS3(0);
  settle(c0, c1);
  sfor<0, 16>([&](auto J) { G(J); });
  bar();
  ST(24);
  {
    u32x4 wf[4];
    a0 = tra(4, RA, 0); a1 = tra(4, RA, 64);
    auto rd = [&](auto MM) { const unsigned ad = (MM.v < 28 ? a0 : a1) + (MM.v % 28) * 2048; wf[MM.v & 3] = trfrag(ad, ad + 1024); };
    rd(IC<0>{}); rd(IC<1>{}); rd(IC<2>{});
    sfor<0, 56>([&](auto MM) {
      constexpr int M = MM.v;
      mmx<M % 28 == 0>(M < 28 ? c2 : c3, wf[M & 3], xf[M % 28]);
      if constexpr (M + 3 < 56) rd(IC<M + 3>{});
      if constexpr (M == M3) { wvm<0>(); asm volatile("s_barrier" ::: "memory"); }
      if constexpr (M == 50) CS3(2);
      SB();
    });
  }
  bar();
  ST(14);
  settle(c2, c3);
  sfor<16, 24>([&](auto J) { G(J); });
  ST(15);
  u32x4 af[4][4];
  unsigned av[2] = {lnv(OFF_H3 + fresh(tc) * 112 * 1024), lnv(OFF_H3 + (fresh(tc) ^ 1) * 112 * 1024)}, as;
  auto ACT1 = [&](auto SS, auto II) {
    constexpr int s = SS.v;
    if constexpr (s == 14 && II.v == 0) { av[0] = lnv(OFF_O + fresh(tc) * 32 * 1024); av[1] = lnv(OFF_O + (fresh(tc) ^ 1) * 32 * 1024); }
    if constexpr (II.v == 0) as = s < 14 ? p4_k32(w, s, fresh(g.rot)) * 2048 : (4 * w + ((s + fresh(g.rot)) & 3)) * 2048;
    ld16s<(II.v & 1) * 1024>(af[s & 3][II.v], av[II.v >> 1], sWS, as);
  };
  unsigned bs[4][2];
  sfor<0, 4>([&](auto I) { bs[I.v][0] = tra(4, p4_slot(fresh(w), I.v ? 3 * I.v - 1 : 0), fresh(kh) * 64); bs[I.v][1] = bs[I.v][0] ^ 64u; tie(bs[I.v][1]); });
  u32x4 wf[2][4];
  auto RW = [&](auto SS, auto I) {
    constexpr int j = SS.v % 9;
    const unsigned aa = bs[(j + 1) / 3][I.v & 1] + (j < 2 ? j : (j + 1) % 3) * 4096 + (I.v >> 1) * 2048;
    wf[SS.v & 1][I.v] = trfrag(aa, aa + 1024);
  };
  bar();
  sfor<0, 12>([&](auto K) { ACT1(IC<K.v / 4>{}, IC<K.v % 4>{}); });
  tie(c3);
  sfor<24, 32>([&](auto J) { G(J); });
  ST(16);
  sfor<0, 4>([&](auto I) { RW(IC<0>{}, I); });
  f32x16 d0, d1, d2, d3;
  const unsigned exw = lnv(R1 + fresh(w) * 12288), own = lnv(p4_slot(fresh(w), 0));
  sfor<0, 18>([&](auto SS) {
    constexpr int s = SS.v;
    if constexpr (s == 4 || s == 8 || s == 11 || s == 15) bar();
    if constexpr (s % 3 == 0) ST(17 + s / 3);
    wvm4<(s == 0 ? 10 : s == 1 ? 4 : s <= 15 ? 8 : 4 * (17 - s))>(af[s & 3][0], af[s & 3][1], af[s & 3][2], af[s & 3][3]);
    if constexpr (s == 1) asm volatile("s_barrier" ::: "memory");
    f32x16 &a0 = s < 14 ? c0 : d0, &a1 = s < 14 ? c1 : d1, &a2 = s < 14 ? c2 : d2, &a3 = s < 14 ? c3 : d3;
    auto side = [&](auto KK) {
      constexpr int k = 4 * s + KK.v;
      if constexpr (s + 3 <= 17) ACT1(IC<s + 3>{}, KK);
      if constexpr (k == 56) { settle(c0, c1); settle(c2, c3); }
      if constexpr (k >= 56 && k < 60) {
        sfor<0, 3>([&](auto J) { constexpr int A = 3 * (k - 56) + J.v; ldsw<f32x4>(exw + A * 1024, quad(A < 4 ? c1 : A < 8 ? c2 : c3, A & 3)); });
        ldsw<f32x4>(own + (k - 56) * 1024, quad(c0, k - 56));
      }
    };
    sfor<0, 2>([&](auto U) {
      mmx<s % 14 == 0 && U.v == 0>(a0, wf[s & 1][2 * U.v], af[s & 3][U.v]);
      if constexpr (s + 1 < 18) RW(IC<s + 1>{}, IC<2 * U.v>{});
      mmx<s % 14 == 0 && U.v == 0>(a1, wf[s & 1][2 * U.v], af[s & 3][2 + U.v]);
      side(IC<2 * U.v>{});
      SB();
      mmx<s % 14 == 0 && U.v == 0>(a2, wf[s & 1][2 * U.v + 1], af[s & 3][U.v]);
      if constexpr (s + 1 < 18) RW(IC<s + 1>{}, IC<2 * U.v + 1>{});
      mmx<s % 14 == 0 && U.v == 0>(a3, wf[s & 1][2 * U.v + 1], af[s & 3][2 + U.v]);
      side(IC<2 * U.v + 1>{});
      SB();
    });
  });
  ST(23);
  settle(d0, d1); tie(d2); tie(d3);
  const unsigned e3[3] = {lnv(RA + (fresh(w) ^ 1) * 12288), lnv(RA + (fresh(w) ^ 2) * 12288), lnv(RA + (fresh(w) ^ 3) * 12288)};
  unsigned oo = ftok * 896 + h * 16 + fresh(g.s7) * 128 + kh * 64;
  tie(oo);
  sfor<0, 12>([&](auto A) { ldsw<f32x4>(exw - R1 + A.v * 1024, quad(A.v < 4 ? d1 : A.v < 8 ? d2 : d3, A.v & 3)); });
  bar();
  sfor<0, 12>([&](auto A) {
    const f32x4 v = ldsr<f32x4>(e3[A.v / 4] + A.v * 1024);
    sfor<0, 4>([&](auto I) { d0[4 * (A.v & 3) + I.v] += v[I.v]; });
  });
  sfor<0, 4>([&](auto A) {
    const f32x4 v = ldsr<f32x4>(own + 4096 + A.v * 1024);
    sfor<0, 4>([&](auto I) { c0[4 * A.v + I.v] = v[I.v]; });
  });
  const srd_t sO = mk(p.out, 2048 * 896);
  sfor<0, 2>([&](auto U) {
    u32x4 ov;
    sfor<0, 4>([&](auto I) {
      constexpr int e = 8 * U.v + 2 * I.v;
      ov[I.v] = pk2(fmaf(d0[e], c0[e], bflo(xv[U.v][I.v])), fmaf(d0[e + 1], c0[e + 1], bfhi(xv[U.v][I.v])));
    });
    __builtin_amdgcn_raw_buffer_store_b128(cross(ov), sO, oo + U.v * 32, 0, 0);
  });
}

DI void spare_path(const Params& p, const int w, const int xc, const int r STARG) {
  const int tid = threadIdx.x, lane = tid & 63, t32 = lane & 31, h = lane >> 5, tb = w & 3, ch = w >> 2;
  constexpr unsigned WPHI = 160768, ZERO = 159744, DUMMY = 158720;
  const int rr = lane >> 3, cp = lane & 7, rs = r - 28;
  const srd_t sWS = mk(p.ws, WS_USED), sX = mk(p.x, 2048 * 896);
  GAS u64* fl = (GAS u64*)(p.ws + OFF_FLG);
  const unsigned srow = 512 * (xc >> 1) + 64 * ((xc & 1) * 4 + rs) + 8 * w + rr;
  u32x2 ev;
  ld8s<0>(ev, 0u, sWS, OFF_FLG);
  u32x4 xr7[7];
  sfor<0, 7>([&](auto K) { ld16p(xr7[K.v], srow * 896 + cp * 16, sX, K.v * 128); });
  const int hd = 2 * xc + (rs >> 1), hf = rs & 1;
  wvm<0>(ev);
  const u64 tag = make_tag(ev);
  const u32x2 tg = {(unsigned)tag, (unsigned)(tag >> 32)};
  {
    float a1 = 0.f, a2 = 0.f;
    sfor<0, 7>([&](auto K) { tie(xr7[K.v]); sfor<0, 4>([&](auto I) { a1 = dot2(xr7[K.v][I.v], ONE2, a1); a2 = dot2(xr7[K.v][I.v], xr7[K.v][I.v], a2); }); });
    sfor<0, 3>([&](auto K) { a1 += __shfl_xor(a1, 1 << K.v); a2 += __shfl_xor(a2, 1 << K.v); });
    const float mean = a1 * (1.f / D), ve = fmaxf(a2 * (1.f / D) - mean * mean, 0.f) + 1e-5f;
    st16s(u32x4{f2u(mean), tg.x, f2u(ve), tg.y}, sWS, cp == 0 ? srow * 16 : OOB, OFF_ST0);
  }
  ST(1);
  const int bi = (xc >> 1) * 4 + rs, par = xc & 1;
  const int q = (lane & 15) >> 2, pp = lane & 3, blk = (lane >> 4) & 1;
  const unsigned acol = (unsigned)((((2 * blk + (pp >> 1)) ^ ((q >> 1) << 2)) << 4) + (pp & 1) * 8);
  const unsigned aperm = (4 * h + q) * 128 + acol;
  const srd_t sW3 = mk(p.w3, 448 * 3584);
  const unsigned wsw = (cp ^ (((rr >> 1) & 1) << 2)) << 4;
  {
    const srd_t s0 = bi < 7 ? mk(p.wqkv, 448 * 2688) : sW3, sP = mk(p.wphi, 16 * 1568);
    const unsigned pitch = bi < 7 ? 2688 : 3584, col = (14 * par + (bi < 7 ? bi : bi - 7)) * 128;
    sfor<0, 7>([&](auto K) { dma16(rr * pitch + wsw, s0, (8 * K.v + w) * 8 * pitch + col, RA + (8 * K.v + w) * 1024); });
    dma16(lane * 16, sP, hd * 1568 + (w & 1) * 1024, WPHI + (w & 1) * 1024);
  }
  if (tid < 16) ldsw<unsigned>(ZERO + tid * 4, 0u);
  auto sums = [&](int cb) {
    wvm<0>();
    bar();
    cs_write(R2 + w * 128, colsum<7>(RA + (aperm ^ (ch * 64)) + tb * 7 * 2048), lane);
    bar();
    if (tb == 0) {
      const f32x16 cs = cs_read(R2 + ch * 512 + h * 16, 128);
      sfor<0, 4>([&](auto A) { st16s<A.v * 32>(__builtin_bit_cast(u32x4, quad(cs, A.v)), sWS, t32 == 0 ? h * 16 : OOB, OFF_CS3 + cb * 256 + ch * 128); });
      wvm<0>();
    }
    bar();
    publish(sWS, tg, FL_CS + cb, tid == 0);
  };
  sums(bi < 7 ? 28 + 7 * par + bi : 14 * par + bi - 7);
  ST(9);
  if (bi < 5) {
    sfor<0, 7>([&](auto K) { dma16(rr * 3584 + wsw, sW3, (8 * K.v + w) * 8 * 3584 + (14 * par + 9 + bi) * 128, RA + (8 * K.v + w) * 1024); });
    sums(14 * par + 9 + bi);
  }
  ST(2);
  const int gq = 8 * hf + w;
  const unsigned qkvs = OFF_QKV + gq * 4 * 84 * 1024;
  const int b01 = (28 * hd + (lane & 1) * 27) >> 6;
  const int U0 = (28 * hd) >> 4, o = (28 * hd) & 15;
  const unsigned f2 = (o >= 8 ? 2 : 0) * 1024;
  const unsigned img = w * 18432;
  u32x4 wp[3];
  unsigned vw[6];
  sfor<0, 3>([&](auto I) {
    unsigned a[2];
    sfor<0, 2>([&](auto T) {
      const int d0 = 16 * I.v + 8 * T.v + 4 * h - o;
      const bool ok = d0 >= 0 && d0 < 28;
      a[T.v] = (ok ? WPHI + (d0 + q) * 56 : ZERO) + (16 * blk + 4 * pp) * 2;
      vw[2 * I.v + T.v] = ok ? img + 16384 + t32 * 64 + (((d0 >> 2) ^ ((t32 >> 1) & 7)) << 3) : DUMMY + lane * 8;
    });
    wp[I.v] = trfrag(a[0], a[1]);
  });
  u32x4 kp[4][3], vp[4][3], qp[4][3];
  auto gather = [&](u32x4 (&d)[4][3], unsigned base) {
    sfor<0, 4>([&](auto T) {
      const unsigned so = qkvs + (T.v * 84 + base + U0) * 1024;
      ld16s<0>(d[T.v][0], lane * 16, sWS, so); ld16s<1024>(d[T.v][1], lane * 16, sWS, so); ld16s<0>(d[T.v][2], lane * 16, sWS, so + f2);
    });
  };
  auto feat = [&](u32x4 (&k)[3], u32x4 (&out)[4]) {
    sfor<0, 3>([&](auto I) { tie(k[I.v]); });
    f32x16 f = mfma(wp[0], k[0], zero16());
    f = mfma(wp[1], k[1], f);
    f = mfma(wp[2], k[2], f);
    dual_softmax(f, h != 0, out);
  };
  const unsigned fq = gq * 14 + b01;
  poll(fl, 2, FL_A + fq + 7, tag);
  ST(3);
  gather(kp, 28);
  if (h) ldsw<u32x2>(img + 16384 + t32 * 64 + ((7 ^ ((t32 >> 1) & 7)) << 3), u32x2{0x3f80u, 0u});
  sfor<0, 4>([&](auto T) {
    wvm<9 - 3 * T.v>();
    u32x4 ph[4];
    feat(kp[T.v], ph);
    const unsigned pr = img + T.v * 4096 + t32 * 128, sw = h ^ (((t32 >> 1) & 1) << 3) ^ (t32 & 1) ^ (((t32 >> 2) & 3) << 1);
    sfor<0, 8>([&](auto A) { ldsw<u32x2>(pr + (((2 * A.v) ^ sw) << 3), u32x2{ph[A.v >> 1][2 * (A.v & 1)], ph[A.v >> 1][2 * (A.v & 1) + 1]}); });
  });
  ST(4);
  poll(fl, 4, FL_Q + fq + ((lane & 2) ? 7 : 0), tag);
  ST(5);
  gather(qp, 0);
  gather(vp, 56);
  u32x4 phq[4][4];
  wvm<21>();
  feat(qp[0], phq[0]);
  f32x16 kvp = zero16(), kvm = zero16();
  unsigned aa[2], ba[2];
  sfor<0, 2>([&](auto T) {
    const int row = 8 * h + 4 * T.v + q, pc = 4 * blk + pp;
    aa[T.v] = img + row * 128 + ((pc ^ ((q >> 1) << 3) ^ (q & 1) ^ ((2 * h + T.v) << 1)) << 3);
    ba[T.v] = img + 16384 + row * 64 + ((pc ^ (row >> 1)) << 3);
  });
  sfor<0, 4>([&](auto T) {
    wvm<9 - 3 * T.v>();
    sfor<0, 3>([&](auto I) { tie(vp[T.v][I.v]); });
    sfor<0, 6>([&](auto A) { ldsw<u32x2>(vw[A.v], u32x2{vp[T.v][A.v >> 1][2 * (A.v & 1)], vp[T.v][A.v >> 1][2 * (A.v & 1) + 1]}); });
    sfor<0, 2>([&](auto U) {
      constexpr unsigned oa = T.v * 4096 + U.v * 2048;
      const u32x4 bf = trfrag(ba[0] + U.v * 1024, ba[1] + U.v * 1024);
      kvp = mfma(trfrag(aa[0] + oa, aa[1] + oa), bf, kvp);
      kvm = mfma(trfrag((aa[0] ^ 64u) + oa, (aa[1] ^ 64u) + oa), bf, kvm);
    });
  });
  sfor<0, 8>([&](auto A) { ldsw<f32x4>(img + A.v * 1024 + lane * 16, quad(A.v < 4 ? kvp : kvm, A.v & 3)); });
  bar();
  f32x4 own = {0.f, 0.f, 0.f, 0.f};
  sfor<0, 8>([&](auto S) { own += ldsr<f32x4>(S.v * 18432 + w * 1024 + lane * 16); });
  st16s(__builtin_bit_cast(u32x4, own), sWS, lane * 16, OFF_KVP + ((hd * 2 + hf) * 8 + w) * 1024);
  feat(qp[1], phq[1]);
  wvm<0>();
  bar();
  publish(sWS, tg, FL_KV + hd * 2 + hf, tid == 0);
  ST(6);
  feat(qp[2], phq[2]);
  poll(fl, 1, FL_KV + hd * 2 + (hf ^ 1), tag);
  u32x4 oth;
  ld16s<0>(oth, lane * 16, sWS, OFF_KVP + ((hd * 2 + (hf ^ 1)) * 8 + w) * 1024);
  feat(qp[3], phq[3]);
  wvm<0>(oth);
  own += __builtin_bit_cast(f32x4, oth);
  ldsw<u32x2>(147456 + (w >> 1) * 1024 + lane * 16 + (w & 1) * 8, u32x2{pk2(own[0], own[1]), pk2(own[2], own[3])});
  bar();
  ST(7);
  u32x4 kvf[4];
  sfor<0, 4>([&](auto I) { kvf[I.v] = ldsr<u32x4>(147456 + I.v * 1024 + lane * 16); });
  sfor<0, 4>([&](auto T) {
    f32x16 n = mfma(kvf[0], phq[T.v][0], zero16());
    n = mfma(kvf[1], phq[T.v][1], n);
    n = mfma(kvf[2], phq[T.v][2], n);
    n = mfma(kvf[3], phq[T.v][3], n);
    const float rd = __builtin_amdgcn_rcpf(swap_hi(n[12]) + 1e-6f);
#pragma unroll
    for (int i = 0; i < 16; ++i) n[i] *= rd;
    u32x4 o1 = pack8(n, 1);
    if (h) { o1[2] = 0u; o1[3] = 0u; }
    const unsigned so = OFF_O + ((4 * gq + T.v) * 32 + 2 * hd) * 1024;
    st16s(pack8(n, 0), sWS, lane * 16, so); st16s<1024>(o1, sWS, lane * 16, so);
  });
  wvm<0>();
  publish(sWS, tg, FL_O + gq * 16 + hd, lane == 0);
  ST(8);
  if (xc == 0 && rs == 0 && w == 0) {
    sfor<0, 4>([&](auto I) { poll(fl, 56, FL_Q + 56 * I.v + lane, tag); });
    publish(sWS, tg, FL_E, lane == 0);
  }
}

__global__ __launch_bounds__(512, 2) void fused(Params p) {
  u64 pc;
  asm volatile("s_getpc_b64 %0" : "=s"(pc));
  ST(0);
  int w = __builtin_amdgcn_readfirstlane(threadIdx.x >> 6);
  asm volatile("s_nop 4" : "+s"(w));
  const int xc = blockIdx.x & 7, r = blockIdx.x >> 3;
  unsigned dummy = 0;
  if (r < 28 && w >= 4) first_loads(p, w - 4, xc, r);
  if (r < 28 && w < 4) code_prefetch(dummy, pc, (((r & 1) * 4 + w) * 64 + (threadIdx.x & 63)) * 128);
  SB();
  if (r < 28) {
    if (w < 4) compute_path(p, dummy, w, xc, r STPASS);
    else loader_path(p, w - 4, xc, r STPASS);
  }
  else spare_path(p, w, xc, r STPASS);
}

extern "C" void kernel_launch(void* const* d_in, const int* in_sizes, int n_in, void* d_out, int out_size, void* d_ws, size_t ws_size,
                              hipStream_t stream) {
  Params p;
  p.x = (const uint16_t*)d_in[0]; p.wqkv = (const uint16_t*)d_in[1]; p.wphi = (const uint16_t*)d_in[2]; p.wo = (const uint16_t*)d_in[3];
  p.w1 = (const uint16_t*)d_in[5]; p.w2 = (const uint16_t*)d_in[6]; p.w3 = (const uint16_t*)d_in[7]; p.w4 = (const uint16_t*)d_in[8];
  p.out = (uint16_t*)d_out; p.ws = (char*)d_ws + ((ws_size - WS_USED) & ~(size_t)0xFFFFF);
  (void)hipFuncSetAttribute((const void*)fused, hipFuncAttributeMaxDynamicSharedMemorySize, LDS_BYTES);
  fused<<<256, 512, LDS_BYTES, stream>>>(p);
}
```

```cpp
#include <hip/hip_runtime.h>
#include <stdint.h>

#define LAS __attribute__((address_space(3)))
#define GAS __attribute__((address_space(1)))
#define DI __device__ __forceinline__
typedef short s16x4 __attribute__((ext_vector_type(4)));
typedef __bf16 bf16x8 __attribute__((ext_vector_type(8)));
typedef __bf16 bf16x2 __attribute__((ext_vector_type(2)));
typedef float f32x16 __attribute__((ext_vector_type(16)));
typedef float f32x4 __attribute__((ext_vector_type(4)));
typedef float f32x2 __attribute__((ext_vector_type(2)));
typedef unsigned u32x4 __attribute__((ext_vector_type(4)));
typedef unsigned u32x2 __attribute__((ext_vector_type(2)));
typedef unsigned long long u64;
typedef __amdgpu_buffer_rsrc_t srd_t;

constexpr int D = 448;
constexpr float LOG2E = 1.4426950408889634f;
constexpr unsigned OFF_FLG = 0;
constexpr unsigned OFF_H = 262144;
constexpr unsigned OFF_X1 = OFF_H + 64 * 28 * 1024;
constexpr unsigned OFF_QKV = OFF_X1 + 64 * 28 * 1024;
constexpr unsigned OFF_H3 = OFF_QKV + 64 * 84 * 1024;
constexpr unsigned OFF_O = OFF_H3 + 64 * 112 * 1024;
constexpr unsigned OFF_KVP = OFF_O + 64 * 32 * 1024;
constexpr unsigned OFF_ST0 = OFF_KVP + 32 * 8192;
constexpr unsigned OFF_ST1 = OFF_ST0 + 2048 * 16;
constexpr unsigned OFF_CS3 = OFF_ST1 + 14 * 2048 * 8;
constexpr unsigned WS_USED = OFF_CS3 + 42 * 256;
constexpr size_t OFF_STAMP = 64u << 20;
constexpr int FL_E = 0, FL_A = 1, FL_Q = FL_A + 16 * 14, FL_X = FL_Q + 16 * 14, FL_3A = FL_X + 32 * 7, FL_3B = FL_3A + 16 * 14,
              FL_O = FL_3B + 16 * 14, FL_KV = FL_O + 256, FL_CS = FL_KV + 32;
static_assert((FL_CS + 42) * 128 <= 262144, "flag block");
constexpr unsigned RA = 0, R1 = 57344, R2 = 114688, LDS_BYTES = 163840;
constexpr unsigned OOB = 0x80000000u;
constexpr unsigned CODE_BYTES = 48128;
constexpr unsigned SPIN_CAP = 1u << 18;

struct Params {
  const uint16_t *x, *wqkv, *wphi, *wo, *w1, *w2, *w3, *w4;
  uint16_t* out;
  char* ws;
};

template <int I> struct IC { static constexpr int v = I; };
template <int B, int E, class F> DI void sfor(F&& f) {
  if constexpr (B < E) { f(IC<B>{}); sfor<B + 1, E>(f); }
}
DI f32x16 mfma(u32x4 a, u32x4 b, f32x16 c) {
  return __builtin_amdgcn_mfma_f32_32x32x16_bf16(__builtin_bit_cast(bf16x8, a), __builtin_bit_cast(bf16x8, b), c, 0, 0, 0);
}
DI void mma(f32x16& c, u32x4 a, u32x4 b) { asm volatile("v_mfma_f32_32x32x16_bf16 %0, %1, %2, %0" : "+v"(c) : "v"(a), "v"(b) : "memory"); }
DI void mma0(f32x16& c, u32x4 a, u32x4 b) { asm volatile("v_mfma_f32_32x32x16_bf16 %0, %1, %2, 0" : "=&v"(c) : "v"(a), "v"(b) : "memory"); }
template <bool FIRST> DI void mmx(f32x16& c, u32x4 a, u32x4 b) { if constexpr (FIRST) mma0(c, a, b); else mma(c, a, b); }
DI void settle(f32x16& a, f32x16& b) { asm volatile("s_nop 11" : "+v"(a), "+v"(b)); }
DI unsigned pk2(float a, float b) {
  f32x2 v = {a, b};
  return __builtin_bit_cast(unsigned, __builtin_convertvector(v, bf16x2));
}
DI float u2f(unsigned u) { return __builtin_bit_cast(float, u); }
DI unsigned f2u(float f) { return __builtin_bit_cast(unsigned, f); }
DI float bflo(unsigned u) { return __builtin_bit_cast(float, u << 16); }
DI float bfhi(unsigned u) { return __builtin_bit_cast(float, u & 0xffff0000u); }
DI u32x4 trfrag(unsigned a0, unsigned a1) {
  u32x2 lo = __builtin_bit_cast(u32x2, __builtin_amdgcn_ds_read_tr16_b64_v4i16((LAS s16x4*)(uintptr_t)a0));
  u32x2 hi = __builtin_bit_cast(u32x2, __builtin_amdgcn_ds_read_tr16_b64_v4i16((LAS s16x4*)(uintptr_t)a1));
  return u32x4{lo.x, lo.y, hi.x, hi.y};
}
template <class T> DI T ldsr(unsigned a) { return *(LAS T*)(uintptr_t)a; }
template <class T> DI void ldsw(unsigned a, T v) { *(LAS T*)(uintptr_t)a = v; }
DI srd_t mk(const void* p, unsigned bytes) { return __builtin_amdgcn_make_buffer_rsrc((void*)p, 0, (int)bytes, 0x00020000); }
DI void dma16(unsigned voff, srd_t r, unsigned soff, unsigned lds) {
  asm volatile("s_mov_b32 m0, %3\n\ts_nop 0\n\tbuffer_load_dwordx4 %0, %1, %2 offen lds" ::"v"(voff), "s"(r), "s"(soff), "s"(lds) : "memory");
}
DI void dma16s(unsigned voff, srd_t r, unsigned soff, unsigned lds) {
  asm volatile("s_mov_b32 m0, %3\n\ts_nop 0\n\tbuffer_load_dwordx4 %0, %1, %2 offen sc1 lds" ::"v"(voff), "s"(r), "s"(soff), "s"(lds) : "memory");
}
DI void ld4p(unsigned& d, unsigned voff, srd_t r) {
  asm volatile("buffer_load_dword %0, %1, %2, 0 offen" : "=v"(d) : "v"(voff), "s"(r) : "memory");
}
DI void ld16p(u32x4& d, unsigned voff, srd_t r, unsigned soff) {
  asm volatile("buffer_load_dwordx4 %0, %1, %2, %3 offen" : "=v"(d) : "v"(voff), "s"(r), "s"(soff) : "memory");
}
template <int IMM> DI void ld16s(u32x4& d, unsigned voff, srd_t r, unsigned soff) {
  asm volatile("buffer_load_dwordx4 %0, %1, %2, %3 offen offset:%c4 sc1" : "=v"(d) : "v"(voff), "s"(r), "s"(soff), "i"(IMM) : "memory");
}
template <int IMM> DI void ld8s(u32x2& d, unsigned voff, srd_t r, unsigned soff) {
  asm volatile("buffer_load_dwordx2 %0, %1, %2, %3 offen offset:%c4 sc1" : "=v"(d) : "v"(voff), "s"(r), "s"(soff), "i"(IMM) : "memory");
}
template <int IMM = 0> DI void st16s(u32x4 v, srd_t r, unsigned voff, unsigned soff) {
  asm volatile("buffer_store_dwordx4 %0, %1, %2, %3 offen offset:%c4 sc1\n\ts_nop 1" ::"v"(v), "v"(voff), "s"(r), "s"(soff), "i"(IMM) : "memory");
}
DI void st8s(u32x2 v, srd_t r, unsigned voff, unsigned soff) {
  asm volatile("buffer_store_dwordx2 %0, %1, %2, %3 offen sc1" ::"v"(v), "v"(voff), "s"(r), "s"(soff) : "memory");
}
template <int N> DI void wvm() { asm volatile("s_waitcnt vmcnt(%c0)" ::"i"(N) : "memory"); }
template <class T> DI void tie(T& a) { asm volatile("" : "+v"(a)); }
template <int N, class T> DI void wvm(T& a) { asm volatile("s_waitcnt vmcnt(%c1)" : "+v"(a) : "i"(N) : "memory"); }
DI void wlg4(u32x4& a, u32x4& b, u32x4& c, u32x4& d) { asm volatile("s_waitcnt lgkmcnt(0)" : "+v"(a), "+v"(b), "+v"(c), "+v"(d)::"memory"); }
template <int N> DI void wvm4(u32x4& a, u32x4& b, u32x4& c, u32x4& d) {
  asm volatile("s_waitcnt vmcnt(%c4)" : "+v"(a), "+v"(b), "+v"(c), "+v"(d) : "i"(N) : "memory");
}
template <int N> DI void wvm2(u32x4& a, u32x4& b) { asm volatile("s_waitcnt vmcnt(%c2)" : "+v"(a), "+v"(b) : "i"(N) : "memory"); }
DI int fresh(int v) { asm volatile("" : "+s"(v)); return v; }
DI unsigned rtn() { u64 t; asm volatile("s_memrealtime %0\n\ts_waitcnt lgkmcnt(0)" : "=s"(t) :: "memory"); return (unsigned)t; }
DI void bar() { asm volatile("s_waitcnt lgkmcnt(0)\n\ts_barrier" ::: "memory"); }
#define SB() __builtin_amdgcn_sched_barrier(0)
DI void halves(float v, float& lo, float& hi) {
  auto r = __builtin_amdgcn_permlane32_swap(f2u(v), f2u(v), false, false);
  lo = u2f(r[0]); hi = u2f(r[1]);
}
DI float swap_add(float v) { float a, b; halves(v, a, b); return a + b; }
DI float swap_max(float v) { float a, b; halves(v, a, b); return fmaxf(a, b); }
DI float swap_hi(float v) { float a, b; halves(v, a, b); return b; }
typedef f32x4 GeluK;
DI GeluK gelu_k(float r, float ir) {
  const float c0 = -2.f * 0.7978845608028654f * LOG2E, c1 = c0 * 0.044715f;
  return GeluK{c0 * r, c1 * r * r * r, ir, 0.f};
}
DI f32x2 gelu2(f32x2 z, GeluK k) {
  const f32x2 u = z * (z * z * k[1] + k[0]);
  const f32x2 e = {__builtin_amdgcn_exp2f(u[0]), __builtin_amdgcn_exp2f(u[1])};
  const f32x2 d = e * k[2] + k[2];
  return z * f32x2{__builtin_amdgcn_rcpf(d[0]), __builtin_amdgcn_rcpf(d[1])};
}
DI float dot2(unsigned a, unsigned b, float c) {
  return __builtin_amdgcn_fdot2_f32_bf16(__builtin_bit_cast(bf16x2, a), __builtin_bit_cast(bf16x2, b), c, false);
}
constexpr unsigned ONE2 = 0x3f803f80u;
DI f32x16 zero16() { f32x16 z; for (int i = 0; i < 16; ++i) z[i] = 0.f; return z; }
DI u32x4 pack8(const f32x16& a, int u) {
  return u32x4{pk2(a[8 * u], a[8 * u + 1]), pk2(a[8 * u + 2], a[8 * u + 3]), pk2(a[8 * u + 4], a[8 * u + 5]), pk2(a[8 * u + 6], a[8 * u + 7])};
}
DI void poll(GAS u64* fl, int n, int idx, u64 tag) {
  const bool on = (int)(threadIdx.x & 63) < n;
#pragma unroll 1
  for (unsigned spins = 0; spins < SPIN_CAP; ++spins) {
    u64 v = tag;
    if (on) v = __hip_atomic_load(fl + idx * 16, __ATOMIC_RELAXED, __HIP_MEMORY_SCOPE_AGENT);
    if (__all(v == tag)) break;
    __builtin_amdgcn_s_sleep(1);
  }
  wvm<0>();
}
DI void publish(srd_t ws, u32x2 tg, unsigned idx, bool on) { st8s(tg, ws, on ? idx * 128 : OOB, OFF_FLG); }
DI bool all_set(u32x2 f, u32x2 tg) { return __all(f.x == tg.x && f.y == tg.y); }

#define ST(i) do {} while (0)

template <int NU> DI f32x16 colsum(unsigned a) {
  const u32x4 ones = {ONE2, ONE2, ONE2, ONE2};
  f32x16 c = zero16();
  sfor<0, NU>([&](auto UU) { c = mfma(trfrag(a + UU.v * 2048, a + UU.v * 2048 + 1024), ones, c); });
  return c;
}
DI void cs_write(unsigned dst, const f32x16& c, int lane) {
  if ((lane & 31) == 0) {
    const unsigned d = dst + (lane >> 5) * 16;
    sfor<0, 4>([&](auto A) { ldsw<f32x4>(d + A.v * 32, f32x4{c[4 * A.v], c[4 * A.v + 1], c[4 * A.v + 2], c[4 * A.v + 3]}); });
  }
}
DI float max3(float a, float b, float c) { float r; asm("v_max3_f32 %0, %1, %2, %3" : "=v"(r) : "v"(a), "v"(b), "v"(c)); return r; }
DI float min3(float a, float b, float c) { float r; asm("v_min3_f32 %0, %1, %2, %3" : "=v"(r) : "v"(a), "v"(b), "v"(c)); return r; }
DI void dual_softmax(f32x16 p, bool hi, u32x4 (&o)[4]) {
  p[0] += 0.f;
#pragma unroll
  for (int i = 12; i < 16; ++i) p[i] = hi ? p[0] : p[i];
  float mx = max3(p[0], p[1], p[2]), mn = min3(p[0], p[1], p[2]);
#pragma unroll
  for (int i = 3; i < 15; i += 2) { mx = max3(mx, p[i], p[i + 1]); mn = min3(mn, p[i], p[i + 1]); }
  float a, b;
  halves(max3(mx, p[15], p[15]), a, b);
  const float cp = -LOG2E * max3(a, b, b);
  halves(min3(mn, p[15], p[15]), a, b);
  const float cm = LOG2E * min3(a, b, b);
  f32x2 ep[8], em[8];
#pragma unroll
  for (int i = 0; i < 8; ++i) {
    const f32x2 v = {p[2 * i], p[2 * i + 1]}, x = v * LOG2E + cp, y = v * -LOG2E + cm;
    ep[i] = f32x2{__builtin_amdgcn_exp2f(x[0]), __builtin_amdgcn_exp2f(x[1])};
    em[i] = f32x2{__builtin_amdgcn_exp2f(y[0]), __builtin_amdgcn_exp2f(y[1])};
  }
  const float keep = hi ? 0.f : 1.f;
  const f32x2 sp = ((ep[0] + ep[1]) + (ep[2] + ep[3])) + ((ep[4] + ep[5]) + (ep[6] + ep[7]) * keep);
  const f32x2 sn = ((em[0] + em[1]) + (em[2] + em[3])) + ((em[4] + em[5]) + (em[6] + em[7]) * keep);
  const float rp = __builtin_amdgcn_rcpf(swap_add(sp[0] + sp[1])), rm = __builtin_amdgcn_rcpf(swap_add(sn[0] + sn[1]));
#pragma unroll
  for (int i = 0; i < 8; ++i) {
    const f32x2 x = ep[i] * (i < 6 ? rp : rp * keep), y = em[i] * (i < 6 ? rm : rm * keep);
    o[i >> 2][i & 3] = pk2(x[0], x[1]);
    o[2 + (i >> 2)][i & 3] = pk2(y[0], y[1]);
  }
}
DI void granule(u32x4& g, srd_t ws, unsigned voff, u32x2 tg) {
#pragma unroll 1
  for (unsigned spins = 0; spins < SPIN_CAP && !__all(g[1] == tg.x && g[3] == tg.y); ++spins) {
    ld16s<0>(g, voff, ws, OFF_ST0);
    wvm<0>(g);
  }
}
DI f32x16 cs_read(unsigned a, unsigned str) {
  f32x4 v[4][4];
  sfor<0, 4>([&](auto P) { sfor<0, 4>([&](auto A) { v[P.v][A.v] = ldsr<f32x4>(a + P.v * str + A.v * 32); }); });
  f32x16 r;
  sfor<0, 4>([&](auto A) {
    const f32x4 t = (v[0][A.v] + v[1][A.v]) + (v[2][A.v] + v[3][A.v]);
    sfor<0, 4>([&](auto I) { r[4 * A.v + I.v] = t[I.v]; });
  });
  return r;
}
DI f32x4 quad(const f32x16& c, int a) { return f32x4{c[4 * a], c[4 * a + 1], c[4 * a + 2], c[4 * a + 3]}; }
DI u32x4 cross(u32x4 v) {
  auto a = __builtin_amdgcn_permlane32_swap(v[0], v[2], false, false);
  auto b = __builtin_amdgcn_permlane32_swap(v[1], v[3], false, false);
  return u32x4{a[0], b[0], a[1], b[1]};
}

#define STARG , unsigned& tp, const unsigned t0
#define STPASS , tp, t0
DI void code_prefetch(unsigned& d, u64 pc, unsigned voff) {
  asm volatile("buffer_load_dword %0, %1, %2, 0 offen" : "=v"(d) : "v"(voff), "s"(mk((const void*)(pc & ~127ull), CODE_BYTES)) : "memory");
}
DI u64 make_tag(u32x2 ev) {
  return (((u64)(unsigned)__builtin_amdgcn_readfirstlane(ev.y) << 32) | (unsigned)__builtin_amdgcn_readfirstlane(ev.x)) + 0x9E3779B97F4A7C15ull;
}

struct SlotP1 {
  static constexpr unsigned off(int c) { return R2 - 16384 + ((c + 1) % 4) * 16384; }
  static DI unsigned addr(const unsigned (&xr)[4], int U) { return xr[U & 3] + ((U / 4 + 1) % 4) * 16384; }
};
struct SlotP3 {
  static constexpr unsigned off(int c) { return c < 3 ? RA + c * 16384 : c < 6 ? R2 + (c - 3) * 16384 : RA; }
  static DI unsigned addr(const unsigned (&xr)[4], int U) { return xr[U >= 12 && U < 24] + (U / 4 % 3) * 16384 + (U & 3) * 1024; }
};
constexpr unsigned HREG = R2;
constexpr unsigned XT = R2 + 40960;
constexpr int UH = 12;
constexpr int M3 = 20;
template <int TS, class Slot>
DI void pass_a(f32x16& c0, f32x16& c1, const unsigned a0, const unsigned a1, const unsigned (&xr)[4], u32x4* xf) {
  auto rx = [&](auto UU) { xf[UU.v] = ldsr<u32x4>(Slot::addr(xr, UU.v)); };
  u32x4 wf[3][2];
  auto rd = [&](auto UU, auto NN) {
    const unsigned ad = (NN.v ? a1 : a0) + UU.v * 2048;
    wf[UU.v % 3][NN.v] = trfrag(ad, ad + TS);
  };
  bar();
  rx(IC<0>{}); rd(IC<0>{}, IC<0>{}); rd(IC<0>{}, IC<1>{}); rx(IC<1>{}); rd(IC<1>{}, IC<0>{}); rd(IC<1>{}, IC<1>{});
  sfor<0, 28>([&](auto UU) {
    constexpr int U = UU.v;
    if constexpr (U % 4 == 2 && U < 26) asm volatile("s_barrier" ::: "memory");
    mmx<U == 0>(c0, wf[U % 3][0], xf[U]);
    if constexpr (U + 2 < 28) { rx(IC<U + 2>{}); rd(IC<U + 2>{}, IC<0>{}); }
    SB();
    mmx<U == 0>(c1, wf[U % 3][1], xf[U]);
    if constexpr (U + 2 < 28) rd(IC<U + 2>{}, IC<1>{});
    SB();
  });
}
DI int rot7(int c, int rot) { const int v = c + rot; return v >= 7 ? v - 7 : v; }
struct Geo {
  int r7, s7, g4, tt, s14, rot;
  bool wtype;
  DI Geo(int xc, int r) {
    r7 = r / 7;
    s7 = r - 7 * r7;
    g4 = 4 * (xc >> 1) + r7;
    tt = 4 * xc + r7;
    s14 = 7 * (xc & 1) + s7;
    rot = (s7 + 2 * r7) % 7;
    wtype = (xc & 1) == 0;
  }
};
DI unsigned p4_slot(int kq, int j) {
  return j < 2 ? R2 + 16384 + (2 * kq + j) * 4096 : j < 5 ? R1 + (3 * kq + j - 2) * 4096 : j < 8 ? RA + (3 * kq + j - 5) * 4096 : (kq < 2 ? R1 : RA) + 49152 + (kq & 1) * 4096;
}
DI int p4_k32(int kq, int s, int rot) {
  const int a = 7 * kq + rot7(s < 7 ? s : s - 7, rot);
  return 4 * (a >> 1) + (a & 1) + (s < 7 ? 0 : 2);
}

DI void first_loads(const Params& p, const int w, const int xc, const int r) {
  const int lane = threadIdx.x & 63;
  const Geo g(xc, r);
  const int rr = lane >> 3, cp = lane & 7;
  const unsigned wsw = (unsigned)((cp ^ (((rr >> 1) & 1) << 2)) << 4);
  const srd_t sX = mk(p.x, 2048 * 896), sQ = mk(p.wqkv, 448 * 2688);
  const srd_t sA = g.wtype ? mk(p.w1, 448 * 896) : sQ;
  const unsigned pitchA = g.wtype ? 896 : 2688;
  const unsigned colA = g.wtype ? g.s7 : 7 + g.s7;
  const unsigned wvA = rr * pitchA + wsw;
  const unsigned xv0 = rr * 896 + ((cp ^ (rr >> 1)) << 4), xv1 = xv0 ^ 64u;
  const unsigned T32 = 4 * g.g4 + w, xs = T32 * 32 * 896;
  auto G1 = [&](int c) {
    const int cg = rot7(c, g.rot);
    sfor<0, 2>([&](auto J) { dma16(wvA, sA, (8 * cg + 2 * w + J.v) * 8 * pitchA + colA * 128, RA + (8 * c + 2 * w + J.v) * 1024); });
    sfor<0, 4>([&](auto I) { dma16((I.v & 1) ? xv1 : xv0, sX, xs + I.v * 8 * 896 + cg * 128, SlotP1::off(c) + w * 4096 + I.v * 1024); });
  };
  __builtin_amdgcn_s_setprio(3);
  G1(0); G1(1);
}

DI void loader_path(const Params& p, const int w, const int xc, const int r STARG) {
  const int lane = threadIdx.x & 63, t32 = lane & 31, h = lane >> 5;
  const Geo g(xc, r);
  const int rr = lane >> 3, cp = lane & 7;
  const unsigned wsw = (unsigned)((cp ^ (((rr >> 1) & 1) << 2)) << 4);
  const srd_t sWS = mk(p.ws, WS_USED), sX = mk(p.x, 2048 * 896), sQ = mk(p.wqkv, 448 * 2688);
  const srd_t sA = g.wtype ? mk(p.w1, 448 * 896) : sQ;
  const unsigned pitchA = g.wtype ? 896 : 2688;
  const unsigned colA = g.wtype ? g.s7 : 7 + g.s7, colB = g.wtype ? g.s7 : 14 + g.s7;
  const unsigned wvA = rr * pitchA + wsw;
  const unsigned xv0 = rr * 896 + ((cp ^ (rr >> 1)) << 4), xv1 = xv0 ^ 64u;
  const unsigned T32 = 4 * g.g4 + w, xs = T32 * 32 * 896;
  auto G1 = [&](int c) {
    const int cg = rot7(c, g.rot);
    sfor<0, 2>([&](auto J) { dma16(wvA, sA, (8 * cg + 2 * w + J.v) * 8 * pitchA + colA * 128, RA + (8 * c + 2 * w + J.v) * 1024); });
    sfor<0, 4>([&](auto I) { dma16((I.v & 1) ? xv1 : xv0, sX, xs + I.v * 8 * 896 + cg * 128, SlotP1::off(c) + w * 4096 + I.v * 1024); });
  };
  u32x2 ev;
  ld8s<0>(ev, 0u, sWS, OFF_FLG);
  SB();
  const unsigned wv896 = rr * 896 + wsw, wv3 = rr * 3584 + wsw, wvB = rr * 2688 + wsw;
  auto WB = [&](int k) { dma16(wvB, sQ, (8 * rot7(k >> 1, g.rot) + 4 * (k & 1) + w) * 8 * 2688 + colB * 128, R1 + (4 * k + w) * 1024); };
  const int q = (lane & 15) >> 2, pp = lane & 3, blk = (lane >> 4) & 1;
  const unsigned ca = (4 * h + q) * 128 + (unsigned)((((2 * blk + (pp >> 1)) ^ ((q >> 1) << 2)) << 4) + (pp & 1) * 8);
  const u32x4 ones = {ONE2, ONE2, ONE2, ONE2};
  f32x16 s0 = zero16(), s1 = zero16();
  auto CS = [&](unsigned a) { s0 = mfma(trfrag(a, a + 1024), ones, s0); s1 = mfma(trfrag(a ^ 64u, (a ^ 64u) + 1024), ones, s1); };
  const unsigned cw = RA + ca + w * 2048;
  u32x4 gr;
  wvm<7>(); bar(); G1(2); CS(cw);
  wvm<7>(); bar(); G1(3); CS(cw + 8192);
  wvm<6>(ev); bar(); G1(4); CS(cw + 2 * 8192);
  wvm<6>(); bar(); G1(5); CS(cw + 3 * 8192);
  ld16s<0>(gr, (T32 * 32 + t32) * 16, sWS, OFF_ST0);
  wvm<7>(); bar(); G1(6); CS(cw + 4 * 8192);
  const u64 tag = make_tag(ev);
  const u32x2 tg = {(unsigned)tag, (unsigned)(tag >> 32)};
  wvm<6>(gr); bar(); sfor<0, 3>([&](auto K) { WB(K.v); });
  CS(cw + 5 * 8192);
  granule(gr, sWS, (T32 * 32 + t32) * 16, tg);
  const float ve1 = u2f(gr[2]);
  float r1 = __builtin_amdgcn_rsqf(ve1);
  const float v2 = (ve1 - 1e-5f) * r1 * r1 + 1e-5f;
  GeluK k1 = gelu_k(r1 * __builtin_amdgcn_rsqf(v2), __builtin_amdgcn_sqrtf(ve1 * v2));
  k1[3] = u2f(gr[0]);
  tie(k1); tie(r1);
  cs_write(R2 - 16384 + w * 128, s0, lane); cs_write(R2 - 16384 + 512 + w * 128, s1, lane);
  wvm<3>(); bar();
  {
    const unsigned a = R2 - 16384 + h * 512 + t32 * 4, c6 = (RA + ca + 24 * 2048) ^ ((w & 1) * 64);
    u32x4 f[4];
    sfor<0, 4>([&](auto U) { f[U.v] = trfrag(c6 + U.v * 2048, c6 + U.v * 2048 + 1024); });
    const float t = (ldsr<float>(a) + ldsr<float>(a + 128)) + (ldsr<float>(a + 256) + ldsr<float>(a + 384));
    s0 = zero16();
    sfor<0, 4>([&](auto U) { s0 = mfma(f[U.v], ones, s0); });
    sfor<3, 6>([&](auto K) { WB(K.v); });
    ldsw<f32x4>(R2 + 2048 + w * 1024 + lane * 16, k1);
    ldsw<float>(R2 + 6144 + w * 256 + lane * 4, r1);
    ldsw<float>(R2 + 1024 + lane * 4, t);
    cs_write(R2 + 1280 + (w & 1) * 128, s0, lane);
  }
  GAS u64* fl = (GAS u64*)(p.ws + OFF_FLG);
  const bool l0 = threadIdx.x == 256;
  bar();
  __builtin_amdgcn_s_setprio(0);
  sfor<6, 14>([&](auto K) { WB(K.v); });
  sfor<0, 2>([&](auto J) { dma16(rr * 896 + ((cp ^ rr) << 4), sX, (g.tt * 64 + (2 * w + J.v) * 8) * 896 + g.s7 * 128, XT + (2 * w + J.v) * 1024); });
  const srd_t sW2 = mk(p.w2, 448 * 896);
  auto W2 = [&](int k) { const int pc = 8 * rot7(k >> 1, g.rot) + 4 * (k & 1) + w; dma16(wv896, sW2, pc * 8 * 896 + g.s7 * 128, RA + pc * 1024); };
  sfor<0, 4>([&](auto K) { W2(K.v); });
  wvm<4>(); bar();
  sfor<4, 14>([&](auto K) { W2(K.v); });
  const srd_t sW4 = mk(p.w4, 1792 * 896), sWo = mk(p.wo, 448 * 896), sW3 = mk(p.w3, 448 * 3584);
  u32x4 w4r[9][4], w3r[14];
  auto L4 = [&](auto SS) {
    constexpr int s = SS.v;
    sfor<0, 4>([&](auto I) {
      u32x4& d = s < 9 ? w3r[4 * (s - 5) + I.v] : w4r[s < 9 ? 0 : s - 9][I.v];
      if constexpr (s < 14) ld16p(d, wv896, sW4, (32 * p4_k32(w, s, g.rot) + 8 * I.v) * 896 + g.s7 * 128);
      else ld16p(d, wv896, sWo, (28 * (4 * w + ((s + g.rot) & 3)) + 8 * I.v) * 896 + g.s7 * 128);
    });
  };
  bar();
  publish(sWS, tg, FL_A + g.g4 * 14 + g.s14, l0);
  poll(fl, 1, FL_CS + 28 + g.s14, tag);
  asm volatile("s_mov_b32 m0, %3\n\ts_nop 0\n\tbuffer_load_dword %0, %1, %2 offen sc1 lds" ::"v"(lane * 4), "s"(sWS), "s"(OFF_CS3 + (28 + g.s14) * 256), "s"(XT) : "memory");
  {
    const int th = w & 1, kh = w >> 1, tc = 2 * g.tt + th;
    const int gsel = min(max(tc + ((lane & 4) ? 2 * th - 1 : 0), 0), 63) >> 2;
    poll(fl, 8, FL_A + gsel * 14 + min(3 * kh + (lane & 3), 6), tag);
    const srd_t sH = mk(p.ws + OFF_H, 64 * 28 * 1024);
    const int tok = tc * 32 + t32, ta = tok - 1 - kh, tb = tok + 1 + kh;
    const unsigned oa = (unsigned)((ta >> 5) * 28 * 1024 + ((ta & 31) + 32 * h) * 16), ob = (unsigned)((tb >> 5) * 28 * 1024 + ((tb & 31) + 32 * h) * 16);
    sfor<0, 6>([&](auto U) { dma16s(oa, sH, (kh * 14 + U.v) * 1024, HREG + w * 10240 + U.v * 1024); });
    bar();
    dma16s(oa, sH, (kh * 14 + 6) * 1024, HREG + w * 10240 + 6144);
    sfor<7, 10>([&](auto U) { dma16s(ob, sH, (kh * 14 + U.v) * 1024, HREG + w * 10240 + U.v * 1024); });
  }
  wvm<0>();
  bar();
  auto o3 = [&](int k) { return (unsigned)((8 * rot7(k >> 1, g.rot) + 4 * (k & 1) + w) * 8 * 3584 + g.s14 * 256); };
  sfor<0, 12>([&](auto K) { dma16(wv3, sW3, o3(K.v), R1 + (4 * K.v + w) * 1024); });
  bar();
  publish(sWS, tg, FL_Q + g.g4 * 14 + g.s14, l0);
  sfor<12, 14>([&](auto K) { dma16(wv3, sW3, o3(K.v), R1 + (4 * K.v + w) * 1024); });
  sfor<0, 8>([&](auto K) { ld16p(w3r[K.v], wv3, sW3, o3(K.v) + 128); });
  bar();
  publish(sWS, tg, FL_X + g.tt * 7 + g.s7, l0);
  wvm<9>(); bar();
  sfor<8, 14>([&](auto K) { ld16p(w3r[K.v], wv3, sW3, o3(K.v) + 128); });
  sfor<9, 18>(L4);
  wvm<36>();
  bar();
  sfor<0, 14>([&](auto K) { tie(w3r[K.v]); ldsw<u32x4>(RA + (4 * K.v + w) * 1024 + lane * 16, w3r[K.v]); });
  auto W4 = [&](auto SS) {
    sfor<0, 4>([&](auto I) { dma16(wv896, sW4, (32 * p4_k32(w, SS.v, g.rot) + 8 * I.v) * 896 + g.s7 * 128, p4_slot(w, SS.v) + I.v * 1024); });
  };
  sfor<0, 2>(W4);
  wvm<8>();
  bar();
  sfor<2, 5>(W4);
  bar();
  publish(sWS, tg, FL_3A + g.g4 * 14 + g.s14, l0);
  sfor<5, 8>(L4);
  bar();
  W4(IC<8>{});
  const unsigned f3 = (g.tt >> 1) * 14 + (7 * w >> 1) + (lane & 3);
  poll(fl, 4, FL_3A + f3, tag);
  bar();
  sfor<0, 12>([&](auto K) { tie(w3r[K.v]); ldsw<u32x4>(p4_slot(w, 5 + K.v / 4) + (K.v & 3) * 1024 + lane * 16, w3r[K.v]); });
  bar();
  publish(sWS, tg, FL_3B + g.g4 * 14 + g.s14, l0);
  auto S4 = [&](auto J) { sfor<0, 4>([&](auto I) { tie(w4r[J.v][I.v]); ldsw<u32x4>(p4_slot(w, J.v) + I.v * 1024 + lane * 16, w4r[J.v][I.v]); }); };
  poll(fl, 4, FL_3B + f3, tag);
  bar();
  u32x4 x1[2];
  sfor<0, 2>([&](auto U) { ld16s<U.v * 1024>(x1[U.v], lane * 16, sWS, OFF_X1 + ((2 * g.tt + (w & 1)) * 28 + 4 * g.s7 + (w & 2)) * 1024); });
  sfor<0, 4>(S4);
  wvm2<0>(x1[0], x1[1]);
  bar(); sfor<4, 8>(S4);
  poll(fl, 4, FL_O + (g.tt >> 1) * 16 + 4 * w + (lane & 3), tag);
  bar(); S4(IC<8>{});
  bar();
  __builtin_amdgcn_s_setprio(3);
  sfor<0, 4>([&](auto A) {
    f32x4 b = ldsr<f32x4>(p4_slot(w, 0) + A.v * 1024 + lane * 16);
    f32x4 o[3];
    sfor<0, 3>([&](auto P) { o[P.v] = ldsr<f32x4>(R1 + (w ^ (P.v + 1)) * 12288 + (4 * P.v + A.v) * 1024 + lane * 16); });
    const unsigned y0 = x1[A.v >> 1][2 * (A.v & 1)], y1 = x1[A.v >> 1][2 * (A.v & 1) + 1];
    const float xx[4] = {bflo(y0), bfhi(y0), bflo(y1), bfhi(y1)};
    sfor<0, 4>([&](auto I) {
      float t = b[I.v] + o[0][I.v], u = o[1][I.v] + o[2][I.v];
      tie(t); tie(u);
      t = (t + xx[I.v]) + u;
      b[I.v] = t * __builtin_amdgcn_rcpf(1.f + __builtin_amdgcn_exp2f(-LOG2E * t));
    });
    ldsw<f32x4>(p4_slot(w, 1) + A.v * 1024 + lane * 16, b);
  });
  bar();
}

DI void compute_path(const Params& p, unsigned& dummy, const int w, const int xc, const int r STARG) {
  const int tid = threadIdx.x, lane = tid & 63, t32 = lane & 31, h = lane >> 5, th = w & 1, kh = w >> 1;
  const Geo g(xc, r);
  const srd_t sWS = mk(p.ws, WS_USED);
  __builtin_amdgcn_s_setprio(2);
  unsigned dm[4];
  {
    const unsigned id = ((xc * 28 + r) * 4 + w) * 35 + lane;
    const bool on = lane < 35;
    ld4p(dm[0], on && id < 3136 ? id * 128 : OOB, mk(p.w2, 448 * 896));
    ld4p(dm[1], on && id - 3136 < 12544 ? (id - 3136) * 128 : OOB, mk(p.w3, 448 * 3584));
    ld4p(dm[2], on && id - 15680 < 12544 ? (id - 15680) * 128 : OOB, mk(p.w4, 1792 * 896));
    ld4p(dm[3], on && id - 28224 < 3136 ? (id - 28224) * 128 : OOB, mk(p.wo, 448 * 896));
  }
  u32x2 ev;
  ld8s<0>(ev, 0u, sWS, OFF_FLG);
  const unsigned T32 = 4 * g.g4 + w, tc = 2 * g.tt + th;
  const int ftok = tc * 32 + t32;
  u32x4 xv[2], x1p[2], gr;
  f32x16 xn;
  const int q = (lane & 15) >> 2, pp = lane & 3, blk = (lane >> 4) & 1;
  unsigned aperm = (4 * h + q) * 128 + (((2 * blk + (pp >> 1)) ^ ((q >> 1) << 2)) << 4) + (pp & 1) * 8, l16 = lane * 16;
  auto tra = [&](int hs, unsigned base, unsigned x) {
    tie(aperm);
    unsigned a = base + (aperm ^ x);
    if (hs == 8) a += l16 & 512;
    tie(a);
    return a;
  };
  auto lnv = [&](unsigned base) { tie(l16); unsigned a = base + l16; tie(a); return a; };
  u32x4 xf[28];
  unsigned xr[4], a0 = tra(8, RA, 0), a1 = tra(8, RA, 64);
  sfor<0, 4>([&](auto I) { xr[I.v] = R2 - 16384 + w * 4096 + t32 * 128 + (((2 * I.v + h) ^ ((t32 >> 1) & 7)) << 4); tie(xr[I.v]); });
  f32x16 c0, c1, c2, c3;
  pass_a<512, SlotP1>(c0, c1, a0, a1, xr, xf);
  bar();

  tp = rtn() - t0;
  settle(c0, c1);
  f32x4 cst[8];
  const GeluK gk1 = ldsr<f32x4>(R2 + 2048 + w * 1024 + lane * 16);
  const float mean1 = gk1[3], r1 = ldsr<float>(R2 + 6144 + w * 256 + lane * 4);
  auto cs_load = [&](unsigned a) {
    sfor<0, 8>([&](auto A) { cst[A.v] = ldsr<f32x4>(a + A.v * 32 + h * 16); });
    sfor<0, 8>([&](auto A) { sfor<0, 4>([&](auto I) { (A.v < 4 ? c2 : c3)[4 * (A.v & 3) + I.v] = cst[A.v][I.v]; }); });
  };
  cs_load(R2 + 1024);
  sfor<0, 8>([&](auto A) { const f32x4 t = ldsr<f32x4>(R2 + 1280 + A.v * 32 + h * 16); sfor<0, 4>([&](auto I) { (A.v < 4 ? c2 : c3)[4 * (A.v & 3) + I.v] += t[I.v]; }); });
  wvm<0>(ev);
  tie(dummy); tie(dm[0]); tie(dm[1]); tie(dm[2]); tie(dm[3]);
  const u64 tag = make_tag(ev);
  const u32x2 tg = {(unsigned)tag, (unsigned)(tag >> 32)};
  auto epi = [&](auto ACT, unsigned so) {
    unsigned pk[4];
    sfor<0, 16>([&](auto J) {
      constexpr int e = 2 * (J.v & 7);
      const f32x16 &c = J.v < 8 ? c0 : c1, &s = J.v < 8 ? c2 : c3;
      f32x2 z = f32x2{c[e], c[e + 1]} - f32x2{s[e], s[e + 1]} * mean1;
      if constexpr (ACT.v) z = gelu2(z, gk1); else z *= r1;
      pk[J.v & 3] = pk2(z[0], z[1]);
      if constexpr ((J.v & 3) == 3) st16s<(J.v >> 2) * 1024>(u32x4{pk[0], pk[1], pk[2], pk[3]}, sWS, l16, so);
    });
  };
  if (g.wtype) epi(IC<1>{}, OFF_H + (T32 * 28 + 4 * g.s7) * 1024);
  else epi(IC<0>{}, OFF_QKV + (T32 * 84 + 28 + 4 * g.s7) * 1024);
  ld16s<0>(gr, ftok * 16, sWS, OFF_ST0);
  bar();
  ST(3);
  {
    u32x4 wf[3][2];
    float m0, r0;
    auto rd = [&](auto UU, auto NN) {
      const unsigned ad = (NN.v ? a1 : a0) + UU.v * 2048;
      wf[UU.v % 3][NN.v] = trfrag(ad, ad + 512);
    };
    a0 = tra(8, R1, 0); a1 = tra(8, R1, 64);
    rd(IC<0>{}, IC<0>{}); rd(IC<0>{}, IC<1>{}); rd(IC<1>{}, IC<0>{}); rd(IC<1>{}, IC<1>{});
    sfor<0, 28>([&](auto UU) {
      constexpr int U = UU.v;
      mmx<U == 0>(c0, wf[U % 3][0], xf[U]);
      if constexpr (U == UH) {
        wvm<0>(gr);
        bar();
        granule(gr, sWS, ftok * 16, tg);
      }
      if constexpr (U + 2 < 28) rd(IC<U + 2>{}, IC<0>{});
      if constexpr (U >= 2 && U < 6) {
        constexpr int A = U - 2;
        const u32x2 t = ldsr<u32x2>(XT + (th * 32 + t32) * 128 + (((4 * kh + A) ^ (t32 & 7)) << 4) + h * 8);
        xv[A >> 1][2 * (A & 1)] = t.x; xv[A >> 1][2 * (A & 1) + 1] = t.y;
      }
      SB();
      mmx<U == 0>(c1, wf[U % 3][1], xf[U]);
      if constexpr (U + 2 < 28) rd(IC<U + 2>{}, IC<1>{});
      if constexpr (U == UH + 1) { r0 = __builtin_amdgcn_rsqf(u2f(gr[2])); m0 = -u2f(gr[0]) * r0; tie(r0); tie(m0); }
      if constexpr (U >= 20) {
        constexpr int I = U - 20;
        float a = fmaf(bflo(xv[I >> 2][I & 3]), r0, m0), b = fmaf(bfhi(xv[I >> 2][I & 3]), r0, m0);
        tie(a); tie(b);
        xn[2 * I] = a; xn[2 * I + 1] = b;
      }
      SB();
    });
  }
  ST(4);
  bar();
  {
    const srd_t sH = mk(p.ws + fresh(OFF_H), 64 * 28 * 1024);
    const int tb = ftok + 1 + fresh(kh);
    const unsigned ob = (unsigned)((tb >> 5) * 28 * 1024 + ((tb & 31) + 32 * h) * 16);
    sfor<10, 14>([&](auto U) { ld16s<(U.v - 10) * 1024>(xf[U.v], ob, sH, (fresh(kh) * 14 + 10) * 1024); });
  }
  cs_load(XT);
  settle(c0, c1);
  epi(IC<0>{}, OFF_QKV + (fresh(T32) * 84 + (g.wtype ? 0 : 56) + 4 * g.s7) * 1024);
  ST(5);
  bar();
  ST(6);
  c0 = xn;
  {
    a0 = tra(4, RA + fresh(kh) * 14 * 2048, fresh(kh) * 64); a1 = a0 ^ 64u; tie(a1);
    const unsigned hb = lnv(HREG + fresh(w) * 10240);
    u32x4 wf[3][2];
    auto rd = [&](auto UU) {
      if constexpr (UU.v < 10) xf[UU.v] = ldsr<u32x4>(hb + UU.v * 1024);
      wf[UU.v % 3][0] = trfrag(a0 + UU.v * 2048, a0 + UU.v * 2048 + 1024); wf[UU.v % 3][1] = trfrag(a1 + UU.v * 2048, a1 + UU.v * 2048 + 1024);
    };
    rd(IC<0>{}); rd(IC<1>{});
    sfor<0, 14>([&](auto UU) {
      constexpr int U = UU.v;
      if constexpr (U == 10) wvm4<4>(xf[10], xf[11], xf[12], xf[13]);
      mma(c0, wf[U % 3][0], xf[U]);
      if constexpr (U + 2 < 14) rd(IC<U + 2>{});
      mmx<U == 0>(c1, wf[U % 3][1], xf[U]);
      SB();
    });
    settle(c0, c1);
    sfor<0, 4>([&](auto A) { ldsw<f32x4>(hb + A.v * 1024, quad(c1, A.v)); });
  }
  wvm<0>();
  bar();
  ST(27);
  {
    f32x4 o[4];
    const unsigned pb = lnv(HREG + (fresh(w) ^ 2) * 10240);
    sfor<0, 4>([&](auto A) { o[A.v] = ldsr<f32x4>(pb + A.v * 1024); });
    sfor<0, 4>([&](auto A) {
      const f32x4 t = quad(c0, A.v) + o[A.v];
      x1p[A.v >> 1][2 * (A.v & 1)] = pk2(t[0], t[1]);
      x1p[A.v >> 1][2 * (A.v & 1) + 1] = pk2(t[2], t[3]);
    });
    const unsigned so = OFF_X1 + (fresh(tc) * 28 + 4 * g.s7 + 2 * kh) * 1024;
    st16s(x1p[0], sWS, l16, so); st16s<1024>(x1p[1], sWS, l16, so);
    float a1 = 0.f, a2 = 0.f;
    sfor<0, 8>([&](auto I) { a1 = dot2(x1p[I.v >> 2][I.v & 3], ONE2, a1); a2 = dot2(x1p[I.v >> 2][I.v & 3], x1p[I.v >> 2][I.v & 3], a2); });
    st8s(u32x2{f2u(swap_add(a1)), f2u(swap_add(a2))}, sWS, h == 0 ? ftok * 8 : OOB, OFF_ST1 + (2 * fresh(g.s7) + kh) * 2048 * 8);
  }
  ST(7);
  wvm<0>();
  bar();
  ST(8);
  bar();
  GeluK gk3;
  float mean3;
  {
    a0 = tra(4, R1, 0); a1 = tra(4, R1, 64);
    xr[0] = lnv(RA + fresh(w) * 4096); xr[1] = lnv(R2 + fresh(w) * 4096);
    poll((GAS u64*)(p.ws + OFF_FLG), 9, lane >= 7 ? FL_CS + 2 * g.s14 + (lane & 1) : FL_X + (T32 >> 1) * 7 + lane, tag);
    ST(11);
    auto X1 = [&](auto UU) {
      constexpr int c = UU.v / 4, i = UU.v % 4;
      dma16s(l16, sWS, OFF_X1 + (T32 * 28 + 4 * rot7(c, fresh(g.rot)) + i) * 1024, SlotP3::off(c) + fresh(w) * 4096 + i * 1024);
    };
    u32x2 sp[7], cs3;
    sfor<0, 8>(X1);
    sfor<0, 7>([&](auto I) { ld8s<0>(sp[I.v], (T32 * 32 + t32 + h * 7 * 2048) * 8, sWS, OFF_ST1 + I.v * 2048 * 8); });
    ld8s<0>(cs3, lane * 8, sWS, OFF_CS3 + g.s14 * 512);
    sfor<8, 12>(X1);
    u32x4 wf[3][2];
    auto rd = [&](auto UU, auto NN) { const unsigned ad = (NN.v ? a1 : a0) + UU.v * 2048; wf[UU.v % 3][NN.v] = trfrag(ad, ad + 1024); };
    auto rx = [&](auto UU) {
      constexpr int U = UU.v;
      wvm<(U < 2 ? 19 - U : U < 8 ? 17 : U <= 18 ? 9 : 27 - U)>();
      xf[U] = ldsr<u32x4>(SlotP3::addr(xr, U));
    };
    rd(IC<0>{}, IC<0>{}); rd(IC<0>{}, IC<1>{}); rd(IC<1>{}, IC<0>{}); rd(IC<1>{}, IC<1>{});
    rx(IC<0>{}); rx(IC<1>{});
    sfor<0, 28>([&](auto UU) {
      constexpr int U = UU.v;
      mmx<U == 0>(c0, wf[U % 3][0], xf[U]);
      if constexpr (U + 2 < 28) { rx(IC<U + 2>{}); rd(IC<U + 2>{}, IC<0>{}); }
      SB();
      mmx<U == 0>(c1, wf[U % 3][1], xf[U]);
      if constexpr (U + 2 < 28) rd(IC<U + 2>{}, IC<1>{});
      if constexpr (U + 12 < 28) X1(IC<U + 12>{});
      if constexpr (U == 17) {
        float sm = 0.f, sq = 0.f;
        sfor<0, 7>([&](auto I) { tie(sp[I.v]); sm += u2f(sp[I.v].x); sq += u2f(sp[I.v].y); });
        mean3 = swap_add(sm) * (1.f / D);
        const float ve = fmaxf(swap_add(sq) * (1.f / D) - mean3 * mean3, 0.f) + 1e-5f;
        gk3 = gelu_k(__builtin_amdgcn_rsqf(ve), __builtin_amdgcn_sqrtf(ve));
        tie(gk3); tie(mean3);
      }
      if constexpr (U == 19) { tie(cs3); ldsw<u32x2>(R2 + fresh(w) * 4096 + lane * 8, cs3); }
      SB();
    });
  }
  bar();
  ST(13);
  unsigned csb = R2 + fresh(w) * 4096 + h * 16;
  tie(csb);
  auto CS3 = [&](int t) { sfor<0, 8>([&](auto A) { cst[A.v] = ldsr<f32x4>(csb + t * 128 + A.v * 32); }); };
  const unsigned so3 = OFF_H3 + (fresh(T32) * 112 + 8 * g.s14) * 1024;
  unsigned pk[4];
  auto G = [&](auto JJ) {
    constexpr int j = JJ.v, t = j >> 3, e = 2 * (j & 7);
    const f32x16& c = t == 0 ? c0 : t == 1 ? c1 : t == 2 ? c2 : c3;
    const f32x2 z = gelu2(f32x2{c[e], c[e + 1]} - f32x2{cst[4 * (t & 1) + (e >> 2)][e & 3], cst[4 * (t & 1) + (e >> 2)][(e & 3) + 1]} * mean3, gk3);
    pk[j & 3] = pk2(z[0], z[1]);
    if constexpr ((j & 3) == 3) st16s<(j >> 2 & 3) * 1024>(u32x4{pk[0], pk[1], pk[2], pk[3]}, sWS, l16, so3 + (j >> 4) * 4096);
  };
  CS3(0);
  settle(c0, c1);
  sfor<0, 16>([&](auto J) { G(J); });
  bar();
  ST(24);
  {
    u32x4 wf[4];
    a0 = tra(4, RA, 0); a1 = tra(4, RA, 64);
    auto rd = [&](auto MM) { const unsigned ad = (MM.v < 28 ? a0 : a1) + (MM.v % 28) * 2048; wf[MM.v & 3] = trfrag(ad, ad + 1024); };
    rd(IC<0>{}); rd(IC<1>{}); rd(IC<2>{});
    sfor<0, 56>([&](auto MM) {
      constexpr int M = MM.v;
      mmx<M % 28 == 0>(M < 28 ? c2 : c3, wf[M & 3], xf[M % 28]);
      if constexpr (M + 3 < 56) rd(IC<M + 3>{});
      if constexpr (M == M3) { wvm<0>(); asm volatile("s_barrier" ::: "memory"); }
      if constexpr (M == 50) CS3(2);
      SB();
    });
  }
  bar();
  ST(14);
  settle(c2, c3);
  sfor<16, 24>([&](auto J) { G(J); });
  ST(15);
  u32x4 af[4][4];
  unsigned av[2] = {lnv(OFF_H3 + fresh(tc) * 112 * 1024), lnv(OFF_H3 + (fresh(tc) ^ 1) * 112 * 1024)}, as;
  auto ACT1 = [&](auto SS, auto II) {
    constexpr int s = SS.v;
    if constexpr (s == 14 && II.v == 0) { av[0] = lnv(OFF_O + fresh(tc) * 32 * 1024); av[1] = lnv(OFF_O + (fresh(tc) ^ 1) * 32 * 1024); }
    if constexpr (II.v == 0) as = s < 14 ? p4_k32(w, s, fresh(g.rot)) * 2048 : (4 * w + ((s + fresh(g.rot)) & 3)) * 2048;
    ld16s<(II.v & 1) * 1024>(af[s & 3][II.v], av[II.v >> 1], sWS, as);
  };
  unsigned bs[4][2];
  sfor<0, 4>([&](auto I) { bs[I.v][0] = tra(4, p4_slot(fresh(w), I.v ? 3 * I.v - 1 : 0), fresh(kh) * 64); bs[I.v][1] = bs[I.v][0] ^ 64u; tie(bs[I.v][1]); });
  u32x4 wf[2][4];
  auto RW = [&](auto SS, auto I) {
    constexpr int j = SS.v % 9;
    const unsigned aa = bs[(j + 1) / 3][I.v & 1] + (j < 2 ? j : (j + 1) % 3) * 4096 + (I.v >> 1) * 2048;
    wf[SS.v & 1][I.v] = trfrag(aa, aa + 1024);
  };
  bar();
  sfor<0, 12>([&](auto K) { ACT1(IC<K.v / 4>{}, IC<K.v % 4>{}); });
  tie(c3);
  sfor<24, 32>([&](auto J) { G(J); });
  ST(16);
  sfor<0, 4>([&](auto I) { RW(IC<0>{}, I); });
  f32x16 d0, d1, d2, d3;
  const unsigned exw = lnv(R1 + fresh(w) * 12288), own = lnv(p4_slot(fresh(w), 0));
  sfor<0, 18>([&](auto SS) {
    constexpr int s = SS.v;
    if constexpr (s == 4 || s == 8 || s == 11 || s == 15) bar();
    if constexpr (s % 3 == 0) ST(17 + s / 3);
    wvm4<(s == 0 ? 10 : s == 1 ? 4 : s <= 15 ? 8 : 4 * (17 - s))>(af[s & 3][0], af[s & 3][1], af[s & 3][2], af[s & 3][3]);
    if constexpr (s == 1) asm volatile("s_barrier" ::: "memory");
    f32x16 &a0 = s < 14 ? c0 : d0, &a1 = s < 14 ? c1 : d1, &a2 = s < 14 ? c2 : d2, &a3 = s < 14 ? c3 : d3;
    auto side = [&](auto KK) {
      constexpr int k = 4 * s + KK.v;
      if constexpr (s + 3 <= 17) ACT1(IC<s + 3>{}, KK);
      if constexpr (k == 56) { settle(c0, c1); settle(c2, c3); }
      if constexpr (k >= 56 && k < 60) {
        sfor<0, 3>([&](auto J) { constexpr int A = 3 * (k - 56) + J.v; ldsw<f32x4>(exw + A * 1024, quad(A < 4 ? c1 : A < 8 ? c2 : c3, A & 3)); });
        ldsw<f32x4>(own + (k - 56) * 1024, quad(c0, k - 56));
      }
    };
    sfor<0, 2>([&](auto U) {
      mmx<s % 14 == 0 && U.v == 0>(a0, wf[s & 1][2 * U.v], af[s & 3][U.v]);
      if constexpr (s + 1 < 18) RW(IC<s + 1>{}, IC<2 * U.v>{});
      mmx<s % 14 == 0 && U.v == 0>(a1, wf[s & 1][2 * U.v], af[s & 3][2 + U.v]);
      side(IC<2 * U.v>{});
      SB();
      mmx<s % 14 == 0 && U.v == 0>(a2, wf[s & 1][2 * U.v + 1], af[s & 3][U.v]);
      if constexpr (s + 1 < 18) RW(IC<s + 1>{}, IC<2 * U.v + 1>{});
      mmx<s % 14 == 0 && U.v == 0>(a3, wf[s & 1][2 * U.v + 1], af[s & 3][2 + U.v]);
      side(IC<2 * U.v + 1>{});
      SB();
    });
  });
  ST(23);
  settle(d0, d1); tie(d2); tie(d3);
  const unsigned e3[3] = {lnv(RA + (fresh(w) ^ 1) * 12288), lnv(RA + (fresh(w) ^ 2) * 12288), lnv(RA + (fresh(w) ^ 3) * 12288)};
  unsigned oo = ftok * 896 + h * 16 + fresh(g.s7) * 128 + kh * 64;
  tie(oo);
  sfor<0, 12>([&](auto A) { ldsw<f32x4>(exw - R1 + A.v * 1024, quad(A.v < 4 ? d1 : A.v < 8 ? d2 : d3, A.v & 3)); });
  bar();
  sfor<0, 12>([&](auto A) {
    const f32x4 v = ldsr<f32x4>(e3[A.v / 4] + A.v * 1024);
    sfor<0, 4>([&](auto I) { d0[4 * (A.v & 3) + I.v] += v[I.v]; });
  });
  sfor<0, 4>([&](auto A) {
    const f32x4 v = ldsr<f32x4>(own + 4096 + A.v * 1024);
    sfor<0, 4>([&](auto I) { c0[4 * A.v + I.v] = v[I.v]; });
  });
  const srd_t sO = mk(p.out, 2048 * 896);
  sfor<0, 2>([&](auto U) {
    u32x4 ov;
    sfor<0, 4>([&](auto I) {
      constexpr int e = 8 * U.v + 2 * I.v;
      ov[I.v] = pk2(fmaf(d0[e], c0[e], bflo(xv[U.v][I.v])), fmaf(d0[e + 1], c0[e + 1], bfhi(xv[U.v][I.v])));
    });
    __builtin_amdgcn_raw_buffer_store_b128(cross(ov), sO, oo + U.v * 32, 0, 0);
  });
}

DI void spare_path(const Params& p, const int w, const int xc, const int r STARG) {
  const int tid = threadIdx.x, lane = tid & 63, t32 = lane & 31, h = lane >> 5, tb = w & 3, ch = w >> 2;
  constexpr unsigned WPHI = 160768, ZERO = 159744, DUMMY = 158720;
  const int rr = lane >> 3, cp = lane & 7, rs = r - 28;
  const srd_t sWS = mk(p.ws, WS_USED), sX = mk(p.x, 2048 * 896);
  GAS u64* fl = (GAS u64*)(p.ws + OFF_FLG);
  const unsigned srow = 512 * (xc >> 1) + 64 * ((xc & 1) * 4 + rs) + 8 * w + rr;
  u32x2 ev;
  ld8s<0>(ev, 0u, sWS, OFF_FLG);
  u32x4 xr7[7];
  sfor<0, 7>([&](auto K) { ld16p(xr7[K.v], srow * 896 + cp * 16, sX, K.v * 128); });
  const int hd = 2 * xc + (rs >> 1), hf = rs & 1;
  wvm<0>(ev);
  const u64 tag = make_tag(ev);
  const u32x2 tg = {(unsigned)tag, (unsigned)(tag >> 32)};
  {
    float a1 = 0.f, a2 = 0.f;
    sfor<0, 7>([&](auto K) { tie(xr7[K.v]); sfor<0, 4>([&](auto I) { a1 = dot2(xr7[K.v][I.v], ONE2, a1); a2 = dot2(xr7[K.v][I.v], xr7[K.v][I.v], a2); }); });
    sfor<0, 3>([&](auto K) { a1 += __shfl_xor(a1, 1 << K.v); a2 += __shfl_xor(a2, 1 << K.v); });
    const float mean = a1 * (1.f / D), ve = fmaxf(a2 * (1.f / D) - mean * mean, 0.f) + 1e-5f;
    st16s(u32x4{f2u(mean), tg.x, f2u(ve), tg.y}, sWS, cp == 0 ? srow * 16 : OOB, OFF_ST0);
  }
  ST(1);
  const int bi = (xc >> 1) * 4 + rs, par = xc & 1;
  const int q = (lane & 15) >> 2, pp = lane & 3, blk = (lane >> 4) & 1;
  const unsigned acol = (unsigned)((((2 * blk + (pp >> 1)) ^ ((q >> 1) << 2)) << 4) + (pp & 1) * 8);
  const unsigned aperm = (4 * h + q) * 128 + acol;
  const srd_t sW3 = mk(p.w3, 448 * 3584);
  const unsigned wsw = (cp ^ (((rr >> 1) & 1) << 2)) << 4;
  {
    const srd_t s0 = bi < 7 ? mk(p.wqkv, 448 * 2688) : sW3, sP = mk(p.wphi, 16 * 1568);
    const unsigned pitch = bi < 7 ? 2688 : 3584, col = (14 * par + (bi < 7 ? bi : bi - 7)) * 128;
    sfor<0, 7>([&](auto K) { dma16(rr * pitch + wsw, s0, (8 * K.v + w) * 8 * pitch + col, RA + (8 * K.v + w) * 1024); });
    dma16(lane * 16, sP, hd * 1568 + (w & 1) * 1024, WPHI + (w & 1) * 1024);
  }
  if (tid < 16) ldsw<unsigned>(ZERO + tid * 4, 0u);
  auto sums = [&](int cb) {
    wvm<0>();
    bar();
    cs_write(R2 + w * 128, colsum<7>(RA + (aperm ^ (ch * 64)) + tb * 7 * 2048), lane);
    bar();
    if (tb == 0) {
      const f32x16 cs = cs_read(R2 + ch * 512 + h * 16, 128);
      sfor<0, 4>([&](auto A) { st16s<A.v * 32>(__builtin_bit_cast(u32x4, quad(cs, A.v)), sWS, t32 == 0 ? h * 16 : OOB, OFF_CS3 + cb * 256 + ch * 128); });
      wvm<0>();
    }
    bar();
    publish(sWS, tg, FL_CS + cb, tid == 0);
  };
  sums(bi < 7 ? 28 + 7 * par + bi : 14 * par + bi - 7);
  ST(9);
  if (bi < 5) {
    sfor<0, 7>([&](auto K) { dma16(rr * 3584 + wsw, sW3, (8 * K.v + w) * 8 * 3584 + (14 * par + 9 + bi) * 128, RA + (8 * K.v + w) * 1024); });
    sums(14 * par + 9 + bi);
  }
  ST(2);
  const int gq = 8 * hf + w;
  const unsigned qkvs = OFF_QKV + gq * 4 * 84 * 1024;
  const int b01 = (28 * hd + (lane & 1) * 27) >> 6;
  const int U0 = (28 * hd) >> 4, o = (28 * hd) & 15;
  const unsigned f2 = (o >= 8 ? 2 : 0) * 1024;
  const unsigned img = w * 18432;
  u32x4 wp[3];
  unsigned vw[6];
  sfor<0, 3>([&](auto I) {
    unsigned a[2];
    sfor<0, 2>([&](auto T) {
      const int d0 = 16 * I.v + 8 * T.v + 4 * h - o;
      const bool ok = d0 >= 0 && d0 < 28;
      a[T.v] = (ok ? WPHI + (d0 + q) * 56 : ZERO) + (16 * blk + 4 * pp) * 2;
      vw[2 * I.v + T.v] = ok ? img + 16384 + t32 * 64 + (((d0 >> 2) ^ ((t32 >> 1) & 7)) << 3) : DUMMY + lane * 8;
    });
    wp[I.v] = trfrag(a[0], a[1]);
  });
  u32x4 kp[4][3], vp[4][3], qp[4][3];
  auto gather = [&](u32x4 (&d)[4][3], unsigned base) {
    sfor<0, 4>([&](auto T) {
      const unsigned so = qkvs + (T.v * 84 + base + U0) * 1024;
      ld16s<0>(d[T.v][0], lane * 16, sWS, so); ld16s<1024>(d[T.v][1], lane * 16, sWS, so); ld16s<0>(d[T.v][2], lane * 16, sWS, so + f2);
    });
  };
  auto feat = [&](u32x4 (&k)[3], u32x4 (&out)[4]) {
    sfor<0, 3>([&](auto I) { tie(k[I.v]); });
    f32x16 f = mfma(wp[0], k[0], zero16());
    f = mfma(wp[1], k[1], f);
    f = mfma(wp[2], k[2], f);
    dual_softmax(f, h != 0, out);
  };
  const unsigned fq = gq * 14 + b01;
  poll(fl, 2, FL_A + fq + 7, tag);
  ST(3);
  gather(kp, 28);
  if (h) ldsw<u32x2>(img + 16384 + t32 * 64 + ((7 ^ ((t32 >> 1) & 7)) << 3), u32x2{0x3f80u, 0u});
  sfor<0, 4>([&](auto T) {
    wvm<9 - 3 * T.v>();
    u32x4 ph[4];
    feat(kp[T.v], ph);
    const unsigned pr = img + T.v * 4096 + t32 * 128, sw = h ^ (((t32 >> 1) & 1) << 3) ^ (t32 & 1) ^ (((t32 >> 2) & 3) << 1);
    sfor<0, 8>([&](auto A) { ldsw<u32x2>(pr + (((2 * A.v) ^ sw) << 3), u32x2{ph[A.v >> 1][2 * (A.v & 1)], ph[A.v >> 1][2 * (A.v & 1) + 1]}); });
  });
  ST(4);
  poll(fl, 4, FL_Q + fq + ((lane & 2) ? 7 : 0), tag);
  ST(5);
  gather(qp, 0);
  gather(vp, 56);
  u32x4 phq[4][4];
  wvm<21>();
  feat(qp[0], phq[0]);
  f32x16 kvp = zero16(), kvm = zero16();
  unsigned aa[2], ba[2];
  sfor<0, 2>([&](auto T) {
    const int row = 8 * h + 4 * T.v + q, pc = 4 * blk + pp;
    aa[T.v] = img + row * 128 + ((pc ^ ((q >> 1) << 3) ^ (q & 1) ^ ((2 * h + T.v) << 1)) << 3);
    ba[T.v] = img + 16384 + row * 64 + ((pc ^ (row >> 1)) << 3);
  });
  sfor<0, 4>([&](auto T) {
    wvm<9 - 3 * T.v>();
    sfor<0, 3>([&](auto I) { tie(vp[T.v][I.v]); });
    sfor<0, 6>([&](auto A) { ldsw<u32x2>(vw[A.v], u32x2{vp[T.v][A.v >> 1][2 * (A.v & 1)], vp[T.v][A.v >> 1][2 * (A.v & 1) + 1]}); });
    sfor<0, 2>([&](auto U) {
      constexpr unsigned oa = T.v * 4096 + U.v * 2048;
      const u32x4 bf = trfrag(ba[0] + U.v * 1024, ba[1] + U.v * 1024);
      kvp = mfma(trfrag(aa[0] + oa, aa[1] + oa), bf, kvp);
      kvm = mfma(trfrag((aa[0] ^ 64u) + oa, (aa[1] ^ 64u) + oa), bf, kvm);
    });
  });
  sfor<0, 8>([&](auto A) { ldsw<f32x4>(img + A.v * 1024 + lane * 16, quad(A.v < 4 ? kvp : kvm, A.v & 3)); });
  bar();
  f32x4 own = {0.f, 0.f, 0.f, 0.f};
  sfor<0, 8>([&](auto S) { own += ldsr<f32x4>(S.v * 18432 + w * 1024 + lane * 16); });
  st16s(__builtin_bit_cast(u32x4, own), sWS, lane * 16, OFF_KVP + ((hd * 2 + hf) * 8 + w) * 1024);
  feat(qp[1], phq[1]);
  wvm<0>();
  bar();
  publish(sWS, tg, FL_KV + hd * 2 + hf, tid == 0);
  ST(6);
  feat(qp[2], phq[2]);
  poll(fl, 1, FL_KV + hd * 2 + (hf ^ 1), tag);
  u32x4 oth;
  ld16s<0>(oth, lane * 16, sWS, OFF_KVP + ((hd * 2 + (hf ^ 1)) * 8 + w) * 1024);
  feat(qp[3], phq[3]);
  wvm<0>(oth);
  own += __builtin_bit_cast(f32x4, oth);
  ldsw<u32x2>(147456 + (w >> 1) * 1024 + lane * 16 + (w & 1) * 8, u32x2{pk2(own[0], own[1]), pk2(own[2], own[3])});
  bar();
  ST(7);
  u32x4 kvf[4];
  sfor<0, 4>([&](auto I) { kvf[I.v] = ldsr<u32x4>(147456 + I.v * 1024 + lane * 16); });
  sfor<0, 4>([&](auto T) {
    f32x16 n = mfma(kvf[0], phq[T.v][0], zero16());
    n = mfma(kvf[1], phq[T.v][1], n);
    n = mfma(kvf[2], phq[T.v][2], n);
    n = mfma(kvf[3], phq[T.v][3], n);
    const float rd = __builtin_amdgcn_rcpf(swap_hi(n[12]) + 1e-6f);
#pragma unroll
    for (int i = 0; i < 16; ++i) n[i] *= rd;
    u32x4 o1 = pack8(n, 1);
    if (h) { o1[2] = 0u; o1[3] = 0u; }
    const unsigned so = OFF_O + ((4 * gq + T.v) * 32 + 2 * hd) * 1024;
    st16s(pack8(n, 0), sWS, lane * 16, so); st16s<1024>(o1, sWS, lane * 16, so);
  });
  wvm<0>();
  publish(sWS, tg, FL_O + gq * 16 + hd, lane == 0);
  ST(8);
  if (xc == 0 && rs == 0 && w == 0) {
    sfor<0, 4>([&](auto I) { poll(fl, 56, FL_Q + 56 * I.v + lane, tag); });
    publish(sWS, tg, FL_E, lane == 0);
  }
}

__global__ __launch_bounds__(512, 2) void fused(Params p) {
  u64 pc;
  asm volatile("s_getpc_b64 %0" : "=s"(pc));
  const unsigned t0 = rtn();
  unsigned tp = 0;
  int w = __builtin_amdgcn_readfirstlane(threadIdx.x >> 6);
  asm volatile("s_nop 4" : "+s"(w));
  const int xc = blockIdx.x & 7, r = blockIdx.x >> 3;
  unsigned dummy = 0;
  if (r < 28 && w >= 4) first_loads(p, w - 4, xc, r);
  if (r < 28 && w < 4) code_prefetch(dummy, pc, (((r & 1) * 4 + w) * 64 + (threadIdx.x & 63)) * 128);
  SB();
  if (r < 28) {
    if (w < 4) compute_path(p, dummy, w, xc, r STPASS);
    else loader_path(p, w - 4, xc, r STPASS);
  }
  else spare_path(p, w, xc, r STPASS);
  while (rtn() - t0 < 4000 + tp) __builtin_amdgcn_s_sleep(2);
}

extern "C" void kernel_launch(void* const* d_in, const int* in_sizes, int n_in, void* d_out, int out_size, void* d_ws, size_t ws_size,
                              hipStream_t stream) {
  Params p;
  p.x = (const uint16_t*)d_in[0]; p.wqkv = (const uint16_t*)d_in[1]; p.wphi = (const uint16_t*)d_in[2]; p.wo = (const uint16_t*)d_in[3];
  p.w1 = (const uint16_t*)d_in[5]; p.w2 = (const uint16_t*)d_in[6]; p.w3 = (const uint16_t*)d_in[7]; p.w4 = (const uint16_t*)d_in[8];
  p.out = (uint16_t*)d_out; p.ws = (char*)d_ws + ((ws_size - WS_USED) & ~(size_t)0xFFFFF);
  (void)hipFuncSetAttribute((const void*)fused, hipFuncAttributeMaxDynamicSharedMemorySize, LDS_BYTES);
  fused<<<256, 512, LDS_BYTES, stream>>>(p);
}
```

```cpp
#include <hip/hip_runtime.h>
#include <stdint.h>

#define LAS __attribute__((address_space(3)))
#define GAS __attribute__((address_space(1)))
#define DI __device__ __forceinline__
typedef short s16x4 __attribute__((ext_vector_type(4)));
typedef __bf16 bf16x8 __attribute__((ext_vector_type(8)));
typedef __bf16 bf16x2 __attribute__((ext_vector_type(2)));
typedef float f32x16 __attribute__((ext_vector_type(16)));
typedef float f32x4 __attribute__((ext_vector_type(4)));
typedef float f32x2 __attribute__((ext_vector_type(2)));
typedef unsigned u32x4 __attribute__((ext_vector_type(4)));
typedef unsigned u32x2 __attribute__((ext_vector_type(2)));
typedef unsigned long long u64;
typedef __amdgpu_buffer_rsrc_t srd_t;

constexpr int D = 448;
constexpr float LOG2E = 1.4426950408889634f;
constexpr unsigned OFF_FLG = 0;
constexpr unsigned OFF_H = 262144;
constexpr unsigned OFF_X1 = OFF_H + 64 * 28 * 1024;
constexpr unsigned OFF_QKV = OFF_X1 + 64 * 28 * 1024;
constexpr unsigned OFF_H3 = OFF_QKV + 64 * 84 * 1024;
constexpr unsigned OFF_O = OFF_H3 + 64 * 112 * 1024;
constexpr unsigned OFF_KVP = OFF_O + 64 * 32 * 1024;
constexpr unsigned OFF_ST0 = OFF_KVP + 32 * 4096;
constexpr unsigned OFF_ST1 = OFF_ST0 + 2048 * 16;
constexpr unsigned OFF_CS3 = OFF_ST1 + 14 * 2048 * 8;
constexpr unsigned WS_USED = OFF_CS3 + 42 * 256;
constexpr size_t OFF_STAMP = 64u << 20;
constexpr int FL_E = 0, FL_A = 1, FL_Q = FL_A + 16 * 14, FL_3A = FL_Q + 16 * 14, FL_3B = FL_3A + 16 * 14,
              FL_O = FL_3B + 16 * 14, FL_KV = FL_O + 256, FL_CS = FL_KV + 32;
static_assert((FL_CS + 42) * 128 <= 262144, "flag block");
constexpr unsigned RA = 0, R1 = 57344, R2 = 114688, LDS_BYTES = 163840;
constexpr unsigned OOB = 0x80000000u;
constexpr unsigned CODE_BYTES = 46080;
constexpr unsigned SPIN_CAP = 1u << 18;

struct Params {
  const uint16_t *x, *wqkv, *wphi, *wo, *w1, *w2, *w3, *w4;
  uint16_t* out;
  char* ws;
};

template <int I> struct IC { static constexpr int v = I; };
template <int B, int E, class F> DI void sfor(F&& f) {
  if constexpr (B < E) { f(IC<B>{}); sfor<B + 1, E>(f); }
}
DI f32x16 mfma(u32x4 a, u32x4 b, f32x16 c) {
  return __builtin_amdgcn_mfma_f32_32x32x16_bf16(__builtin_bit_cast(bf16x8, a), __builtin_bit_cast(bf16x8, b), c, 0, 0, 0);
}
DI void mma(f32x16& c, u32x4 a, u32x4 b) { asm volatile("v_mfma_f32_32x32x16_bf16 %0, %1, %2, %0" : "+v"(c) : "v"(a), "v"(b) : "memory"); }
DI void mma0(f32x16& c, u32x4 a, u32x4 b) { asm volatile("v_mfma_f32_32x32x16_bf16 %0, %1, %2, 0" : "=&v"(c) : "v"(a), "v"(b) : "memory"); }
template <bool FIRST> DI void mmx(f32x16& c, u32x4 a, u32x4 b) { if constexpr (FIRST) mma0(c, a, b); else mma(c, a, b); }
DI void settle(f32x16& a, f32x16& b) { asm volatile("s_nop 11" : "+v"(a), "+v"(b)); }
DI unsigned pk2(float a, float b) {
  f32x2 v = {a, b};
  return __builtin_bit_cast(unsigned, __builtin_convertvector(v, bf16x2));
}
DI float u2f(unsigned u) { return __builtin_bit_cast(float, u); }
DI unsigned f2u(float f) { return __builtin_bit_cast(unsigned, f); }
DI float bflo(unsigned u) { return __builtin_bit_cast(float, u << 16); }
DI float bfhi(unsigned u) { return __builtin_bit_cast(float, u & 0xffff0000u); }
DI u32x4 trfrag(unsigned a0, unsigned a1) {
  u32x2 lo = __builtin_bit_cast(u32x2, __builtin_amdgcn_ds_read_tr16_b64_v4i16((LAS s16x4*)(uintptr_t)a0));
  u32x2 hi = __builtin_bit_cast(u32x2, __builtin_amdgcn_ds_read_tr16_b64_v4i16((LAS s16x4*)(uintptr_t)a1));
  return u32x4{lo.x, lo.y, hi.x, hi.y};
}
template <class T> DI T ldsr(unsigned a) { return *(LAS T*)(uintptr_t)a; }
template <class T> DI void ldsw(unsigned a, T v) { *(LAS T*)(uintptr_t)a = v; }
DI srd_t mk(const void* p, unsigned bytes) { return __builtin_amdgcn_make_buffer_rsrc((void*)p, 0, (int)bytes, 0x00020000); }
DI void dma16(unsigned voff, srd_t r, unsigned soff, unsigned lds) {
  asm volatile("s_mov_b32 m0, %3\n\ts_nop 0\n\tbuffer_load_dwordx4 %0, %1, %2 offen lds" ::"v"(voff), "s"(r), "s"(soff), "s"(lds) : "memory");
}
DI void dma16s(unsigned voff, srd_t r, unsigned soff, unsigned lds) {
  asm volatile("s_mov_b32 m0, %3\n\ts_nop 0\n\tbuffer_load_dwordx4 %0, %1, %2 offen sc1 lds" ::"v"(voff), "s"(r), "s"(soff), "s"(lds) : "memory");
}
DI void ld4p(unsigned& d, unsigned voff, srd_t r) {
  asm volatile("buffer_load_dword %0, %1, %2, 0 offen" : "=v"(d) : "v"(voff), "s"(r) : "memory");
}
DI void ld16p(u32x4& d, unsigned voff, srd_t r, unsigned soff) {
  asm volatile("buffer_load_dwordx4 %0, %1, %2, %3 offen" : "=v"(d) : "v"(voff), "s"(r), "s"(soff) : "memory");
}
template <int IMM> DI void ld16s(u32x4& d, unsigned voff, srd_t r, unsigned soff) {
  asm volatile("buffer_load_dwordx4 %0, %1, %2, %3 offen offset:%c4 sc1" : "=v"(d) : "v"(voff), "s"(r), "s"(soff), "i"(IMM) : "memory");
}
template <int IMM> DI void ld8s(u32x2& d, unsigned voff, srd_t r, unsigned soff) {
  asm volatile("buffer_load_dwordx2 %0, %1, %2, %3 offen offset:%c4 sc1" : "=v"(d) : "v"(voff), "s"(r), "s"(soff), "i"(IMM) : "memory");
}
template <int IMM = 0> DI void st16s(u32x4 v, srd_t r, unsigned voff, unsigned soff) {
  asm volatile("buffer_store_dwordx4 %0, %1, %2, %3 offen offset:%c4 sc1\n\ts_nop 1" ::"v"(v), "v"(voff), "s"(r), "s"(soff), "i"(IMM) : "memory");
}
DI void st8s(u32x2 v, srd_t r, unsigned voff, unsigned soff) {
  asm volatile("buffer_store_dwordx2 %0, %1, %2, %3 offen sc1" ::"v"(v), "v"(voff), "s"(r), "s"(soff) : "memory");
}
template <int N> DI void wvm() { asm volatile("s_waitcnt vmcnt(%c0)" ::"i"(N) : "memory"); }
template <class T> DI void tie(T& a) { asm volatile("" : "+v"(a)); }
template <int N, class T> DI void wvm(T& a) { asm volatile("s_waitcnt vmcnt(%c1)" : "+v"(a) : "i"(N) : "memory"); }
DI void wlg4(u32x4& a, u32x4& b, u32x4& c, u32x4& d) { asm volatile("s_waitcnt lgkmcnt(0)" : "+v"(a), "+v"(b), "+v"(c), "+v"(d)::"memory"); }
template <int N> DI void wvm4(u32x4& a, u32x4& b, u32x4& c, u32x4& d) {
  asm volatile("s_waitcnt vmcnt(%c4)" : "+v"(a), "+v"(b), "+v"(c), "+v"(d) : "i"(N) : "memory");
}
template <int N> DI void wvm2(u32x4& a, u32x4& b) { asm volatile("s_waitcnt vmcnt(%c2)" : "+v"(a), "+v"(b) : "i"(N) : "memory"); }
DI int fresh(int v) { asm volatile("" : "+s"(v)); return v; }
DI void bar() { asm volatile("s_waitcnt lgkmcnt(0)\n\ts_barrier" ::: "memory"); }
#define SB() __builtin_amdgcn_sched_barrier(0)
DI void halves(float v, float& lo, float& hi) {
  auto r = __builtin_amdgcn_permlane32_swap(f2u(v), f2u(v), false, false);
  lo = u2f(r[0]); hi = u2f(r[1]);
}
DI float swap_add(float v) { float a, b; halves(v, a, b); return a + b; }
DI float swap_max(float v) { float a, b; halves(v, a, b); return fmaxf(a, b); }
DI float swap_hi(float v) { float a, b; halves(v, a, b); return b; }
typedef f32x4 GeluK;
DI GeluK gelu_k(float r, float ir) {
  const float c0 = -2.f * 0.7978845608028654f * LOG2E, c1 = c0 * 0.044715f;
  return GeluK{c0 * r, c1 * r * r * r, ir, 0.f};
}
DI f32x2 gelu2(f32x2 z, GeluK k) {
  const f32x2 u = z * (z * z * k[1] + k[0]);
  const f32x2 e = {__builtin_amdgcn_exp2f(u[0]), __builtin_amdgcn_exp2f(u[1])};
  const f32x2 d = e * k[2] + k[2];
  return z * f32x2{__builtin_amdgcn_rcpf(d[0]), __builtin_amdgcn_rcpf(d[1])};
}
DI float dot2(unsigned a, unsigned b, float c) {
  return __builtin_amdgcn_fdot2_f32_bf16(__builtin_bit_cast(bf16x2, a), __builtin_bit_cast(bf16x2, b), c, false);
}
constexpr unsigned ONE2 = 0x3f803f80u;
DI f32x16 zero16() { f32x16 z; for (int i = 0; i < 16; ++i) z[i] = 0.f; return z; }
DI u32x4 pack8(const f32x16& a, int u) {
  return u32x4{pk2(a[8 * u], a[8 * u + 1]), pk2(a[8 * u + 2], a[8 * u + 3]), pk2(a[8 * u + 4], a[8 * u + 5]), pk2(a[8 * u + 6], a[8 * u + 7])};
}
DI void poll(GAS u64* fl, int n, int idx, u64 tag) {
  const bool on = (int)(threadIdx.x & 63) < n;
#pragma unroll 1
  for (unsigned spins = 0; spins < SPIN_CAP; ++spins) {
    u64 v = tag;
    if (on) v = __hip_atomic_load(fl + idx * 16, __ATOMIC_RELAXED, __HIP_MEMORY_SCOPE_AGENT);
    if (__all(v == tag)) break;
    __builtin_amdgcn_s_sleep(1);
  }
  wvm<0>();
}
DI void publish(srd_t ws, u32x2 tg, unsigned idx, bool on) { st8s(tg, ws, on ? idx * 128 : OOB, OFF_FLG); }
DI bool all_set(u32x2 f, u32x2 tg) { return __all(f.x == tg.x && f.y == tg.y); }

#define ST(i) do {} while (0)

template <int NU> DI f32x16 colsum(unsigned a) {
  const u32x4 ones = {ONE2, ONE2, ONE2, ONE2};
  f32x16 c = zero16();
  sfor<0, NU>([&](auto UU) { c = mfma(trfrag(a + UU.v * 2048, a + UU.v * 2048 + 1024), ones, c); });
  return c;
}
DI void cs_write(unsigned dst, const f32x16& c, int lane) {
  if ((lane & 31) == 0) {
    const unsigned d = dst + (lane >> 5) * 16;
    sfor<0, 4>([&](auto A) { ldsw<f32x4>(d + A.v * 32, f32x4{c[4 * A.v], c[4 * A.v + 1], c[4 * A.v + 2], c[4 * A.v + 3]}); });
  }
}
DI float max3(float a, float b, float c) { float r; asm("v_max3_f32 %0, %1, %2, %3" : "=v"(r) : "v"(a), "v"(b), "v"(c)); return r; }
DI float min3(float a, float b, float c) { float r; asm("v_min3_f32 %0, %1, %2, %3" : "=v"(r) : "v"(a), "v"(b), "v"(c)); return r; }
DI void dual_softmax(f32x16 p, bool hi, u32x4 (&o)[4]) {
  p[0] += 0.f;
#pragma unroll
  for (int i = 12; i < 16; ++i) p[i] = hi ? p[0] : p[i];
  float mx = max3(p[0], p[1], p[2]), mn = min3(p[0], p[1], p[2]);
#pragma unroll
  for (int i = 3; i < 15; i += 2) { mx = max3(mx, p[i], p[i + 1]); mn = min3(mn, p[i], p[i + 1]); }
  float a, b;
  halves(max3(mx, p[15], p[15]), a, b);
  const float cp = -LOG2E * max3(a, b, b);
  halves(min3(mn, p[15], p[15]), a, b);
  const float cm = LOG2E * min3(a, b, b);
  f32x2 ep[8], em[8];
#pragma unroll
  for (int i = 0; i < 8; ++i) {
    const f32x2 v = {p[2 * i], p[2 * i + 1]}, x = v * LOG2E + cp, y = v * -LOG2E + cm;
    ep[i] = f32x2{__builtin_amdgcn_exp2f(x[0]), __builtin_amdgcn_exp2f(x[1])};
    em[i] = f32x2{__builtin_amdgcn_exp2f(y[0]), __builtin_amdgcn_exp2f(y[1])};
  }
  const float keep = hi ? 0.f : 1.f;
  const f32x2 sp = ((ep[0] + ep[1]) + (ep[2] + ep[3])) + ((ep[4] + ep[5]) + (ep[6] + ep[7]) * keep);
  const f32x2 sn = ((em[0] + em[1]) + (em[2] + em[3])) + ((em[4] + em[5]) + (em[6] + em[7]) * keep);
  const float rp = __builtin_amdgcn_rcpf(swap_add(sp[0] + sp[1])), rm = __builtin_amdgcn_rcpf(swap_add(sn[0] + sn[1]));
#pragma unroll
  for (int i = 0; i < 8; ++i) {
    const f32x2 x = ep[i] * (i < 6 ? rp : rp * keep), y = em[i] * (i < 6 ? rm : rm * keep);
    o[i >> 2][i & 3] = pk2(x[0], x[1]);
    o[2 + (i >> 2)][i & 3] = pk2(y[0], y[1]);
  }
}
DI void granule(u32x4& g, srd_t ws, unsigned voff, u32x2 tg) {
#pragma unroll 1
  for (unsigned spins = 0; spins < SPIN_CAP && !__all(g[1] == tg.x && g[3] == tg.y); ++spins) {
    ld16s<0>(g, voff, ws, OFF_ST0);
    wvm<0>(g);
  }
}
DI f32x16 cs_read(unsigned a, unsigned str) {
  f32x4 v[4][4];
  sfor<0, 4>([&](auto P) { sfor<0, 4>([&](auto A) { v[P.v][A.v] = ldsr<f32x4>(a + P.v * str + A.v * 32); }); });
  f32x16 r;
  sfor<0, 4>([&](auto A) {
    const f32x4 t = (v[0][A.v] + v[1][A.v]) + (v[2][A.v] + v[3][A.v]);
    sfor<0, 4>([&](auto I) { r[4 * A.v + I.v] = t[I.v]; });
  });
  return r;
}
DI f32x4 quad(const f32x16& c, int a) { return f32x4{c[4 * a], c[4 * a + 1], c[4 * a + 2], c[4 * a + 3]}; }
DI u32x4 cross(u32x4 v) {
  auto a = __builtin_amdgcn_permlane32_swap(v[0], v[2], false, false);
  auto b = __builtin_amdgcn_permlane32_swap(v[1], v[3], false, false);
  return u32x4{a[0], b[0], a[1], b[1]};
}

#define STARG
#define STPASS
DI void code_prefetch(unsigned& d, u64 pc, unsigned voff) {
  asm volatile("buffer_load_dword %0, %1, %2, 0 offen" : "=v"(d) : "v"(voff), "s"(mk((const void*)(pc & ~127ull), CODE_BYTES)) : "memory");
}
DI u64 make_tag(u32x2 ev) {
  return (((u64)(unsigned)__builtin_amdgcn_readfirstlane(ev.y) << 32) | (unsigned)__builtin_amdgcn_readfirstlane(ev.x)) + 0x9E3779B97F4A7C15ull;
}

struct SlotP1 {
  static constexpr unsigned off(int c) { return R2 - 16384 + ((c + 1) % 4) * 16384; }
  static DI unsigned addr(const unsigned (&xr)[4], int U) { return xr[U & 3] + ((U / 4 + 1) % 4) * 16384; }
};
constexpr unsigned HREG = R2;
constexpr unsigned XT = R2 + 40960;
constexpr unsigned WPH = XT + 1024;
constexpr int NE = 2;
constexpr int UH = 12;
constexpr int NB = 1;
constexpr unsigned POISON = 0xFFFFFFFFu;
constexpr int M3 = 20;
template <int TS, class Slot>
DI void pass_a(f32x16& c0, f32x16& c1, const unsigned a0, const unsigned a1, const unsigned (&xr)[4], u32x4* xf) {
  auto rx = [&](auto UU) { xf[UU.v] = ldsr<u32x4>(Slot::addr(xr, UU.v)); };
  u32x4 wf[3][2];
  auto rd = [&](auto UU, auto NN) {
    const unsigned ad = (NN.v ? a1 : a0) + UU.v * 2048;
    wf[UU.v % 3][NN.v] = trfrag(ad, ad + TS);
  };
  bar();
  rx(IC<0>{}); rd(IC<0>{}, IC<0>{}); rd(IC<0>{}, IC<1>{}); rx(IC<1>{}); rd(IC<1>{}, IC<0>{}); rd(IC<1>{}, IC<1>{});
  sfor<0, 28>([&](auto UU) {
    constexpr int U = UU.v;
    if constexpr (U % 4 == 2 && U < 26) asm volatile("s_barrier" ::: "memory");
    mmx<U == 0>(c0, wf[U % 3][0], xf[U]);
    if constexpr (U + 2 < 28) { rx(IC<U + 2>{}); rd(IC<U + 2>{}, IC<0>{}); }
    SB();
    mmx<U == 0>(c1, wf[U % 3][1], xf[U]);
    if constexpr (U + 2 < 28) rd(IC<U + 2>{}, IC<1>{});
    SB();
  });
}
DI void phi_frags(u32x4 (&wp)[3], int hd, unsigned wphi, unsigned zero, int lane) {
  const int o = (28 * hd) & 15, h = lane >> 5, q = (lane & 15) >> 2;
  const unsigned c = ((lane >> 4) & 1) * 32 + (lane & 3) * 8;
  sfor<0, 3>([&](auto I) {
    unsigned a[2];
    sfor<0, 2>([&](auto T) { const int d0 = 16 * I.v + 8 * T.v + 4 * h - o; a[T.v] = (d0 >= 0 && d0 < 28 ? wphi + (d0 + q) * 56 : zero) + c; });
    wp[I.v] = trfrag(a[0], a[1]);
  });
}
DI void head_loads(u32x4 (&d)[3], int hd, unsigned base, srd_t ws, unsigned l16) {
  const unsigned so = base + ((28 * hd) >> 4) * 1024;
  ld16s<0>(d[0], l16, ws, so); ld16s<1024>(d[1], l16, ws, so); ld16s<0>(d[2], l16, ws, so + (((28 * hd) & 15) >= 8 ? 2048 : 0));
}
DI void features(const u32x4 (&wp)[3], u32x4 (&k)[3], bool hi, u32x4 (&out)[4]) {
  sfor<0, 3>([&](auto I) { tie(k[I.v]); });
  f32x16 f = mfma(wp[0], k[0], zero16());
  f = mfma(wp[1], k[1], f);
  f = mfma(wp[2], k[2], f);
  dual_softmax(f, hi, out);
}
DI void o_store(u32x4 (&kv)[8], const u32x4 (&ph)[4], bool hi, srd_t ws, unsigned l16, unsigned so) {
  f32x16 n = zero16();
  sfor<0, 8>([&](auto I) { tie(kv[I.v]); n = mfma(kv[I.v], ph[I.v & 3], n); });
  const float rd = __builtin_amdgcn_rcpf(swap_hi(n[12]) + 1e-6f);
#pragma unroll
  for (int i = 0; i < 16; ++i) n[i] *= rd;
  u32x4 o1 = pack8(n, 1);
  if (hi) { o1[2] = 0u; o1[3] = 0u; }
  st16s(pack8(n, 0), ws, l16, so); st16s<1024>(o1, ws, l16, so);
}
DI void kv_loads(u32x4 (&kv)[8], int hd, srd_t ws, unsigned l16) {
  sfor<0, 8>([&](auto I) { ld16s<(I.v & 3) * 1024>(kv[I.v], l16, ws, OFF_KVP + hd * 8192 + (I.v >> 2) * 4096); });
}
struct Geo {
  int r7, s7, g4, tt, s14;
  bool wtype;
  DI Geo(int xc, int r) {
    r7 = r / 7;
    s7 = r - 7 * r7;
    g4 = 4 * (xc >> 1) + r7;
    tt = 4 * xc + r7;
    s14 = 7 * (xc & 1) + s7;
    wtype = (xc & 1) == 0;
  }
};
DI unsigned p4_slot(int kq, int j) { return (j < 3 ? R2 : j < 6 ? R1 : RA) + (3 * kq + j % 3) * 4096; }
DI int p4_k32(int kq, int s) {
  const int a = 7 * kq + (s < 7 ? s : s - 7);
  return 4 * (a >> 1) + (a & 1) + (s < 7 ? 0 : 2);
}

DI void first_loads(const Params& p, const int w, const int xc, const int r) {
  const int lane = threadIdx.x & 63;
  const Geo g(xc, r);
  const int rr = lane >> 3, cp = lane & 7;
  const unsigned wsw = (unsigned)((cp ^ (((rr >> 1) & 1) << 2)) << 4);
  const srd_t sX = mk(p.x, 2048 * 896), sQ = mk(p.wqkv, 448 * 2688);
  const srd_t sA = g.wtype ? mk(p.w1, 448 * 896) : sQ;
  const unsigned pitchA = g.wtype ? 896 : 2688;
  const unsigned colA = g.wtype ? g.s7 : 7 + g.s7;
  const unsigned wvA = rr * pitchA + wsw;
  const unsigned xv0 = rr * 896 + ((cp ^ (rr >> 1)) << 4), xv1 = xv0 ^ 64u;
  const unsigned T32 = 4 * g.g4 + w, xs = T32 * 32 * 896;
  auto G1 = [&](int c) {
    sfor<0, 2>([&](auto J) { dma16(wvA, sA, (8 * c + 2 * w + J.v) * 8 * pitchA + colA * 128, RA + (8 * c + 2 * w + J.v) * 1024); });
    sfor<0, 4>([&](auto I) { dma16((I.v & 1) ? xv1 : xv0, sX, xs + I.v * 8 * 896 + c * 128, SlotP1::off(c) + w * 4096 + I.v * 1024); });
  };
  __builtin_amdgcn_s_setprio(3);
  G1(0); G1(1);
}

DI void loader_path(const Params& p, const int w, const int xc, const int r STARG) {
  const int lane = threadIdx.x & 63, t32 = lane & 31, h = lane >> 5;
  const Geo g(xc, r);
  const int rr = lane >> 3, cp = lane & 7;
  const unsigned wsw = (unsigned)((cp ^ (((rr >> 1) & 1) << 2)) << 4);
  const srd_t sWS = mk(p.ws, WS_USED), sX = mk(p.x, 2048 * 896), sQ = mk(p.wqkv, 448 * 2688);
  const srd_t sA = g.wtype ? mk(p.w1, 448 * 896) : sQ;
  const unsigned pitchA = g.wtype ? 896 : 2688;
  const unsigned colA = g.wtype ? g.s7 : 7 + g.s7, colB = g.wtype ? g.s7 : 14 + g.s7;
  const unsigned wvA = rr * pitchA + wsw;
  const unsigned xv0 = rr * 896 + ((cp ^ (rr >> 1)) << 4), xv1 = xv0 ^ 64u;
  const unsigned T32 = 4 * g.g4 + w, xs = T32 * 32 * 896;
  auto G1 = [&](int c) {
    sfor<0, 2>([&](auto J) { dma16(wvA, sA, (8 * c + 2 * w + J.v) * 8 * pitchA + colA * 128, RA + (8 * c + 2 * w + J.v) * 1024); });
    sfor<0, 4>([&](auto I) { dma16((I.v & 1) ? xv1 : xv0, sX, xs + I.v * 8 * 896 + c * 128, SlotP1::off(c) + w * 4096 + I.v * 1024); });
  };
  u32x2 ev;
  ld8s<0>(ev, 0u, sWS, OFF_FLG);
  SB();
  const unsigned wv896 = rr * 896 + wsw, wv3 = rr * 3584 + wsw, wvB = rr * 2688 + wsw;
  auto WB = [&](int k) { dma16(wvB, sQ, (4 * k + w) * 8 * 2688 + colB * 128, R1 + (4 * k + w) * 1024); };
  const int q = (lane & 15) >> 2, pp = lane & 3, blk = (lane >> 4) & 1;
  const unsigned ca = (4 * h + q) * 128 + (unsigned)((((2 * blk + (pp >> 1)) ^ ((q >> 1) << 2)) << 4) + (pp & 1) * 8);
  const u32x4 ones = {ONE2, ONE2, ONE2, ONE2};
  f32x16 s0 = zero16(), s1 = zero16();
  auto CS = [&](unsigned a) { s0 = mfma(trfrag(a, a + 1024), ones, s0); s1 = mfma(trfrag(a ^ 64u, (a ^ 64u) + 1024), ones, s1); };
  const unsigned cw = RA + ca + w * 2048;
  u32x4 gr;
  wvm<7>(); bar(); G1(2); CS(cw);
  wvm<7>(); bar(); G1(3); CS(cw + 8192);
  wvm<6>(ev); bar(); G1(4); CS(cw + 2 * 8192);
  wvm<6>(); bar(); G1(5); CS(cw + 3 * 8192);
  ld16s<0>(gr, (T32 * 32 + t32) * 16, sWS, OFF_ST0);
  wvm<7>(); bar(); G1(6); CS(cw + 4 * 8192);
  const u64 tag = make_tag(ev);
  const u32x2 tg = {(unsigned)tag, (unsigned)(tag >> 32)};
  wvm<6>(gr); bar(); sfor<0, 3>([&](auto K) { WB(K.v); });
  CS(cw + 5 * 8192);
  granule(gr, sWS, (T32 * 32 + t32) * 16, tg);
  const float ve1 = u2f(gr[2]);
  float r1 = __builtin_amdgcn_rsqf(ve1);
  const float v2 = (ve1 - 1e-5f) * r1 * r1 + 1e-5f;
  GeluK k1 = gelu_k(r1 * __builtin_amdgcn_rsqf(v2), __builtin_amdgcn_sqrtf(ve1 * v2));
  k1[3] = u2f(gr[0]);
  tie(k1); tie(r1);
  cs_write(R2 - 16384 + w * 128, s0, lane); cs_write(R2 - 16384 + 512 + w * 128, s1, lane);
  wvm<3>(); bar();
  {
    const unsigned a = R2 - 16384 + h * 512 + t32 * 4, c6 = (RA + ca + 24 * 2048) ^ ((w & 1) * 64);
    u32x4 f[4];
    sfor<0, 4>([&](auto U) { f[U.v] = trfrag(c6 + U.v * 2048, c6 + U.v * 2048 + 1024); });
    const float t = (ldsr<float>(a) + ldsr<float>(a + 128)) + (ldsr<float>(a + 256) + ldsr<float>(a + 384));
    s0 = zero16();
    sfor<0, 4>([&](auto U) { s0 = mfma(f[U.v], ones, s0); });
    sfor<3, 6>([&](auto K) { WB(K.v); });
    ldsw<f32x4>(R2 + 2048 + w * 1024 + lane * 16, k1);
    ldsw<float>(R2 + 6144 + w * 256 + lane * 4, r1);
    ldsw<float>(R2 + 1024 + lane * 4, t);
    cs_write(R2 + 1280 + (w & 1) * 128, s0, lane);
  }
  GAS u64* fl = (GAS u64*)(p.ws + OFF_FLG);
  const bool l0 = threadIdx.x == 256;
  bar();
  __builtin_amdgcn_s_setprio(0);
  sfor<6, 14>([&](auto K) { WB(K.v); });
  sfor<0, 2>([&](auto J) { dma16(rr * 896 + ((cp ^ rr) << 4), sX, (g.tt * 64 + (2 * w + J.v) * 8) * 896 + g.s7 * 128, XT + (2 * w + J.v) * 1024); });
  const srd_t sW2 = mk(p.w2, 448 * 896);
  auto W2 = [&](int k) { const int pc = 4 * k + w; dma16(wv896, sW2, pc * 8 * 896 + g.s7 * 128, RA + pc * 1024); };
  sfor<0, 4>([&](auto K) { W2(K.v); });
  wvm<4>(); bar();
  sfor<4, 14>([&](auto K) { W2(K.v); });
  const srd_t sW4 = mk(p.w4, 1792 * 896), sWo = mk(p.wo, 448 * 896), sW3 = mk(p.w3, 448 * 3584);
  u32x4 w4r[9][4];
  auto L4 = [&](auto SS) {
    constexpr int s = SS.v;
    sfor<0, 4>([&](auto I) {
      u32x4& d = w4r[s - 9][I.v];
      if constexpr (s < 14) ld16p(d, wv896, sW4, (32 * p4_k32(w, s) + 8 * I.v) * 896 + g.s7 * 128);
      else ld16p(d, wv896, sWo, (28 * (4 * w + (s - 14)) + 8 * I.v) * 896 + g.s7 * 128);
    });
  };
  bar();
  publish(sWS, tg, FL_A + g.g4 * 14 + g.s14, l0);
  const int kg = 2 * xc + (r >> 4), kh16 = r & 15;
  dma16(lane * 16, mk(p.wphi, 16 * 1568), kh16 * 1568 + (w & 1) * 1024, WPH + (w & 1) * 1024);
  if (lane < 16) ldsw<unsigned>(WPH + 2048 + lane * 4, 0u);
  sfor<9, 9 + NE>(L4);
  {
    const int th = w & 1, kh = w >> 1, tc = 2 * g.tt + th;
    const int gsel = min(max(tc + ((lane & 4) ? 2 * th - 1 : 0), 0), 63) >> 2;
    const int t3 = T32 >> 1, px = (4 * (t3 >> 3) + (t3 & 3)) * 14 + 7 * ((t3 >> 2) & 1);
    poll(fl, 18, lane < 8 ? FL_A + gsel * 14 + min(3 * kh + (lane & 3), 6) : lane < 15 ? FL_A + px + lane - 8 : lane < 17 ? FL_CS + 2 * g.s14 + (lane & 1) : FL_CS + 28 + g.s14, tag);
  asm volatile("s_mov_b32 m0, %3\n\ts_nop 0\n\tbuffer_load_dword %0, %1, %2 offen sc1 lds" ::"v"(lane * 4), "s"(sWS), "s"(OFF_CS3 + (28 + g.s14) * 256), "s"(XT) : "memory");
    const srd_t sH = mk(p.ws + OFF_H, 64 * 28 * 1024);
    const int tok = tc * 32 + t32, ta = tok - 1 - kh, tb = tok + 1 + kh;
    const unsigned oa = (unsigned)((ta >> 5) * 28 * 1024 + ((ta & 31) + 32 * h) * 16), ob = (unsigned)((tb >> 5) * 28 * 1024 + ((tb & 31) + 32 * h) * 16);
    sfor<0, 6>([&](auto U) { dma16s(oa, sH, (kh * 14 + U.v) * 1024, HREG + w * 10240 + U.v * 1024); });
    wvm<6>();
    bar();
    dma16s(oa, sH, (kh * 14 + 6) * 1024, HREG + w * 10240 + 6144);
    sfor<7, 10>([&](auto U) { dma16s(ob, sH, (kh * 14 + U.v) * 1024, HREG + w * 10240 + U.v * 1024); });
  }
  wvm<0>();
  bar();
  auto o3 = [&](int k) { return (unsigned)((4 * k + w) * 8 * 3584 + g.s14 * 256); };
  sfor<0, 14>([&](auto K) { dma16(wv3, sW3, o3(K.v), R1 + (4 * K.v + w) * 1024); });
  bar();
  publish(sWS, tg, FL_Q + g.g4 * 14 + g.s14, l0);
  sfor<0, 4>([&](auto K) { dma16(wv3, sW3, o3(K.v) + 128, RA + (4 * K.v + w) * 1024); });
  u32x4 qf[3], wp[3], ph[4];
  phi_frags(wp, kh16, WPH, WPH + 2048, lane);
  wvm<5>(); bar();
  sfor<4, 14>([&](auto K) { dma16(wv3, sW3, o3(K.v) + 128, RA + (4 * K.v + w) * 1024); });
  poll(fl, 2, FL_Q + kg * 14 + ((28 * kh16 + (lane & 1) * 27) >> 6), tag);
  head_loads(qf, kh16, OFF_QKV + (4 * kg + w) * 84 * 1024, sWS, lane * 16);
  auto W4 = [&](auto SS) {
    sfor<0, 4>([&](auto I) { dma16(wv896, sW4, (32 * p4_k32(w, SS.v) + 8 * I.v) * 896 + g.s7 * 128, p4_slot(w, SS.v) + I.v * 1024); });
  };
  sfor<0, 3>(W4);
  sfor<9 + NE, 9 + NE + NB>(L4);
  wvm<12 + 4 * NB>();
  features(wp, qf, h != 0, ph);
  sfor<0, 4>([&](auto I) { tie(ph[I.v]); });
  bar();
  sfor<3, 6>(W4);
  sfor<9 + NE + NB, 18>(L4);
  wvm<24>();
  bar();
  publish(sWS, tg, FL_3A + g.g4 * 14 + g.s14, l0);
  poll(fl, 2, FL_KV + kh16 * 2 + lane, tag);
  {
    u32x4 kv[8];
    kv_loads(kv, kh16, sWS, lane * 16);
    wvm<0>();
    o_store(kv, ph, h != 0, sWS, lane * 16, OFF_O + ((4 * kg + w) * 32 + 2 * kh16) * 1024);
  }
  const unsigned f3 = (g.tt >> 1) * 14 + (7 * w >> 1) + (lane & 3);
  poll(fl, 4, FL_3A + f3, tag);
  bar();
  publish(sWS, tg, FL_O + kg * 16 + kh16, l0);
  bar();
  publish(sWS, tg, FL_3B + g.g4 * 14 + g.s14, l0);
  sfor<6, 9>(W4);
  auto S4 = [&](auto J) { sfor<0, 4>([&](auto I) { tie(w4r[J.v][I.v]); ldsw<u32x4>(p4_slot(w, J.v) + I.v * 1024 + lane * 16, w4r[J.v][I.v]); }); };
  poll(fl, 8, lane < 4 ? FL_3B + f3 : FL_O + (g.tt >> 1) * 16 + 4 * w + (lane & 3), tag);
  bar();
  u32x4 x1[2];
  sfor<0, 2>([&](auto U) { ld16s<U.v * 1024>(x1[U.v], lane * 16, sWS, OFF_X1 + ((2 * g.tt + (w & 1)) * 28 + 4 * g.s7 + (w & 2)) * 1024); });
  sfor<0, 4>(S4);
  wvm2<0>(x1[0], x1[1]);
  bar(); sfor<4, 8>(S4);
  bar(); S4(IC<8>{});
  bar();
  __builtin_amdgcn_s_setprio(3);
  sfor<0, 4>([&](auto A) {
    f32x4 b = ldsr<f32x4>(p4_slot(w, 0) + A.v * 1024 + lane * 16);
    f32x4 o[3];
    sfor<0, 3>([&](auto P) { o[P.v] = ldsr<f32x4>(R1 + (w ^ (P.v + 1)) * 12288 + (4 * P.v + A.v) * 1024 + lane * 16); });
    const unsigned y0 = x1[A.v >> 1][2 * (A.v & 1)], y1 = x1[A.v >> 1][2 * (A.v & 1) + 1];
    const float xx[4] = {bflo(y0), bfhi(y0), bflo(y1), bfhi(y1)};
    sfor<0, 4>([&](auto I) {
      float t = b[I.v] + o[0][I.v], u = o[1][I.v] + o[2][I.v];
      tie(t); tie(u);
      t = (t + xx[I.v]) + u;
      b[I.v] = t * __builtin_amdgcn_rcpf(1.f + __builtin_amdgcn_exp2f(-LOG2E * t));
    });
    ldsw<f32x4>(p4_slot(w, 1) + A.v * 1024 + lane * 16, b);
  });
  bar();
}

DI void compute_path(const Params& p, unsigned& dummy, const int w, const int xc, const int r STARG) {
  const int tid = threadIdx.x, lane = tid & 63, t32 = lane & 31, h = lane >> 5, th = w & 1, kh = w >> 1;
  const Geo g(xc, r);
  const srd_t sWS = mk(p.ws, WS_USED);
  __builtin_amdgcn_s_setprio(2);
  unsigned dm[4];
  {
    const unsigned id = ((xc * 28 + r) * 4 + w) * 35 + lane;
    const bool on = lane < 35;
    ld4p(dm[0], on && id < 3136 ? id * 128 : OOB, mk(p.w2, 448 * 896));
    ld4p(dm[1], on && id - 3136 < 12544 ? (id - 3136) * 128 : OOB, mk(p.w3, 448 * 3584));
    ld4p(dm[2], on && id - 15680 < 12544 ? (id - 15680) * 128 : OOB, mk(p.w4, 1792 * 896));
    ld4p(dm[3], on && id - 28224 < 3136 ? (id - 28224) * 128 : OOB, mk(p.wo, 448 * 896));
  }
  u32x2 ev;
  ld8s<0>(ev, 0u, sWS, OFF_FLG);
  const unsigned T32 = 4 * g.g4 + w, tc = 2 * g.tt + th;
  const int ftok = tc * 32 + t32;
  const unsigned x1o = OFF_X1 + (tc * 28 + 4 * g.s7 + 2 * kh) * 1024, st1o = OFF_ST1 + (2 * g.s7 + kh) * 2048 * 8;
  st16s(u32x4{POISON, POISON, POISON, POISON}, sWS, lane * 16, x1o); st16s<1024>(u32x4{POISON, POISON, POISON, POISON}, sWS, lane * 16, x1o);
  st8s(u32x2{POISON, POISON}, sWS, h == 0 ? ftok * 8 : OOB, st1o);
  u32x4 xv[2], x1p[2], gr;
  f32x16 xn;
  const int q = (lane & 15) >> 2, pp = lane & 3, blk = (lane >> 4) & 1;
  unsigned aperm = (4 * h + q) * 128 + (((2 * blk + (pp >> 1)) ^ ((q >> 1) << 2)) << 4) + (pp & 1) * 8, l16 = lane * 16;
  auto tra = [&](int hs, unsigned base, unsigned x) {
    tie(aperm);
    unsigned a = base + (aperm ^ x);
    if (hs == 8) a += l16 & 512;
    tie(a);
    return a;
  };
  auto lnv = [&](unsigned base) { tie(l16); unsigned a = base + l16; tie(a); return a; };
  u32x4 xf[28];
  unsigned xr[4], a0 = tra(8, RA, 0), a1 = tra(8, RA, 64);
  sfor<0, 4>([&](auto I) { xr[I.v] = R2 - 16384 + w * 4096 + t32 * 128 + (((2 * I.v + h) ^ ((t32 >> 1) & 7)) << 4); tie(xr[I.v]); });
  f32x16 c0, c1, c2, c3;
  pass_a<512, SlotP1>(c0, c1, a0, a1, xr, xf);
  bar();
  settle(c0, c1);
  f32x4 cst[8];
  const GeluK gk1 = ldsr<f32x4>(R2 + 2048 + w * 1024 + lane * 16);
  const float mean1 = gk1[3], r1 = ldsr<float>(R2 + 6144 + w * 256 + lane * 4);
  auto cs_load = [&](unsigned a) {
    sfor<0, 8>([&](auto A) { cst[A.v] = ldsr<f32x4>(a + A.v * 32 + h * 16); });
    sfor<0, 8>([&](auto A) { sfor<0, 4>([&](auto I) { (A.v < 4 ? c2 : c3)[4 * (A.v & 3) + I.v] = cst[A.v][I.v]; }); });
  };
  cs_load(R2 + 1024);
  sfor<0, 8>([&](auto A) { const f32x4 t = ldsr<f32x4>(R2 + 1280 + A.v * 32 + h * 16); sfor<0, 4>([&](auto I) { (A.v < 4 ? c2 : c3)[4 * (A.v & 3) + I.v] += t[I.v]; }); });
  wvm<0>(ev);
  tie(dummy); tie(dm[0]); tie(dm[1]); tie(dm[2]); tie(dm[3]);
  const u64 tag = make_tag(ev);
  const u32x2 tg = {(unsigned)tag, (unsigned)(tag >> 32)};
  auto epi = [&](auto ACT, unsigned so) {
    unsigned pk[4];
    sfor<0, 16>([&](auto J) {
      constexpr int e = 2 * (J.v & 7);
      const f32x16 &c = J.v < 8 ? c0 : c1, &s = J.v < 8 ? c2 : c3;
      f32x2 z = f32x2{c[e], c[e + 1]} - f32x2{s[e], s[e + 1]} * mean1;
      if constexpr (ACT.v) z = gelu2(z, gk1); else z *= r1;
      pk[J.v & 3] = pk2(z[0], z[1]);
      if constexpr ((J.v & 3) == 3) st16s<(J.v >> 2) * 1024>(u32x4{pk[0], pk[1], pk[2], pk[3]}, sWS, l16, so);
    });
  };
  if (g.wtype) epi(IC<1>{}, OFF_H + (T32 * 28 + 4 * g.s7) * 1024);
  else epi(IC<0>{}, OFF_QKV + (T32 * 84 + 28 + 4 * g.s7) * 1024);
  ld16s<0>(gr, ftok * 16, sWS, OFF_ST0);
  bar();
  ST(3);
  {
    u32x4 wf[3][2];
    float m0, r0;
    auto rd = [&](auto UU, auto NN) {
      const unsigned ad = (NN.v ? a1 : a0) + UU.v * 2048;
      wf[UU.v % 3][NN.v] = trfrag(ad, ad + 512);
    };
    a0 = tra(8, R1, 0); a1 = tra(8, R1, 64);
    rd(IC<0>{}, IC<0>{}); rd(IC<0>{}, IC<1>{}); rd(IC<1>{}, IC<0>{}); rd(IC<1>{}, IC<1>{});
    sfor<0, 28>([&](auto UU) {
      constexpr int U = UU.v;
      mmx<U == 0>(c0, wf[U % 3][0], xf[U]);
      if constexpr (U == UH) {
        wvm<0>(gr);
        bar();
        granule(gr, sWS, ftok * 16, tg);
      }
      if constexpr (U + 2 < 28) rd(IC<U + 2>{}, IC<0>{});
      if constexpr (U >= 2 && U < 6) {
        constexpr int A = U - 2;
        const u32x2 t = ldsr<u32x2>(XT + (th * 32 + t32) * 128 + (((4 * kh + A) ^ (t32 & 7)) << 4) + h * 8);
        xv[A >> 1][2 * (A & 1)] = t.x; xv[A >> 1][2 * (A & 1) + 1] = t.y;
      }
      SB();
      mmx<U == 0>(c1, wf[U % 3][1], xf[U]);
      if constexpr (U + 2 < 28) rd(IC<U + 2>{}, IC<1>{});
      if constexpr (U == UH + 1) { r0 = __builtin_amdgcn_rsqf(u2f(gr[2])); m0 = -u2f(gr[0]) * r0; tie(r0); tie(m0); }
      if constexpr (U >= 20) {
        constexpr int I = U - 20;
        float a = fmaf(bflo(xv[I >> 2][I & 3]), r0, m0), b = fmaf(bfhi(xv[I >> 2][I & 3]), r0, m0);
        tie(a); tie(b);
        xn[2 * I] = a; xn[2 * I + 1] = b;
      }
      SB();
    });
  }
  ST(4);
  bar();
  {
    const srd_t sH = mk(p.ws + fresh(OFF_H), 64 * 28 * 1024);
    const int tb = ftok + 1 + fresh(kh);
    const unsigned ob = (unsigned)((tb >> 5) * 28 * 1024 + ((tb & 31) + 32 * h) * 16);
    sfor<10, 14>([&](auto U) { ld16s<(U.v - 10) * 1024>(xf[U.v], ob, sH, (fresh(kh) * 14 + 10) * 1024); });
  }
  cs_load(XT);
  settle(c0, c1);
  epi(IC<0>{}, OFF_QKV + (fresh(T32) * 84 + (g.wtype ? 0 : 56) + 4 * g.s7) * 1024);
  ST(5);
  c0 = xn;
  {
    a0 = tra(4, RA + fresh(kh) * 14 * 2048, fresh(kh) * 64); a1 = a0 ^ 64u; tie(a1);
    const unsigned hb = lnv(HREG + fresh(w) * 10240);
    tie(c0);
  bar();
    u32x4 wf[3][2];
    auto rd = [&](auto UU) {
      if constexpr (UU.v < 10) xf[UU.v] = ldsr<u32x4>(hb + UU.v * 1024);
      wf[UU.v % 3][0] = trfrag(a0 + UU.v * 2048, a0 + UU.v * 2048 + 1024); wf[UU.v % 3][1] = trfrag(a1 + UU.v * 2048, a1 + UU.v * 2048 + 1024);
    };
    rd(IC<0>{}); rd(IC<1>{});
    sfor<0, 14>([&](auto UU) {
      constexpr int U = UU.v;
      if constexpr (U == 10) wvm4<4>(xf[10], xf[11], xf[12], xf[13]);
      mma(c0, wf[U % 3][0], xf[U]);
      if constexpr (U + 2 < 14) rd(IC<U + 2>{});
      mmx<U == 0>(c1, wf[U % 3][1], xf[U]);
      SB();
    });
    settle(c0, c1);
    sfor<0, 4>([&](auto A) { ldsw<f32x4>(hb + A.v * 1024, quad(c1, A.v)); });
  }
  wvm<0>();
  bar();
  ST(27);
  {
    f32x4 o[4];
    const unsigned pb = lnv(HREG + (fresh(w) ^ 2) * 10240);
    sfor<0, 4>([&](auto A) { o[A.v] = ldsr<f32x4>(pb + A.v * 1024); });
    sfor<0, 4>([&](auto A) {
      const f32x4 t = quad(c0, A.v) + o[A.v];
      x1p[A.v >> 1][2 * (A.v & 1)] = pk2(t[0], t[1]);
      x1p[A.v >> 1][2 * (A.v & 1) + 1] = pk2(t[2], t[3]);
    });
    sfor<0, 8>([&](auto I) { x1p[I.v >> 2][I.v & 3] = min(x1p[I.v >> 2][I.v & 3], POISON - 1); });
    st16s(x1p[0], sWS, l16, x1o); st16s<1024>(x1p[1], sWS, l16, x1o);
    float a1 = 0.f, a2 = 0.f;
    sfor<0, 8>([&](auto I) { a1 = dot2(x1p[I.v >> 2][I.v & 3], ONE2, a1); a2 = dot2(x1p[I.v >> 2][I.v & 3], x1p[I.v >> 2][I.v & 3], a2); });
    st8s(u32x2{min(f2u(swap_add(a1)), POISON - 1), min(f2u(swap_add(a2)), POISON - 1)}, sWS, h == 0 ? ftok * 8 : OOB, st1o);
  }
  ST(7);
  bar();
  ST(8);
  GeluK gk3;
  float mean3;
  u32x2 cs3;
  {
    a0 = tra(4, R1, 0); a1 = tra(4, R1, 64);
    const unsigned spo = (T32 * 32 + t32 + h * 7 * 2048) * 8;
    ld8s<0>(cs3, lane * 8, sWS, OFF_CS3 + g.s14 * 512);
#pragma unroll 1
    for (unsigned spins = 0; spins < SPIN_CAP; ++spins) {
      unsigned v = 0;
      if (lane < 14) v = __hip_atomic_load((GAS unsigned*)(p.ws + OFF_ST1 + (lane * 2048 + T32 * 32 + 31) * 8), __ATOMIC_RELAXED, __HIP_MEMORY_SCOPE_AGENT);
      if (__all(v != POISON)) break;
      __builtin_amdgcn_s_sleep(2);
    }
    wvm<0>(cs3);
    ST(11);
    const unsigned xb = lnv(OFF_X1 + fresh(T32) * 28 * 1024);
    auto X1 = [&](auto UU) { ld16s<(UU.v & 3) * 1024>(xf[UU.v], xb, sWS, (UU.v / 4) * 4096); };
    u32x2 sp[7];
    auto SP = [&]() { sfor<0, 7>([&](auto I) { ld8s<0>(sp[I.v], spo, sWS, OFF_ST1 + I.v * 2048 * 8); }); };
    sfor<0, 12>(X1);
    u32x4 wf[3][2];
    auto rd = [&](auto UU, auto NN) { const unsigned ad = (NN.v ? a1 : a0) + UU.v * 2048; wf[UU.v % 3][NN.v] = trfrag(ad, ad + 1024); };
    auto check = [&](auto CC, auto NN) {
      constexpr int U = 4 * CC.v;
      wvm4<NN.v>(xf[U], xf[U + 1], xf[U + 2], xf[U + 3]);
#pragma unroll 1
      for (unsigned spins = 0; spins < SPIN_CAP; ++spins) {
        unsigned m = 0;
        sfor<0, 4>([&](auto I) { m = max(max(m, max(xf[U + I.v][0], xf[U + I.v][1])), max(xf[U + I.v][2], xf[U + I.v][3])); });
        if (__builtin_expect(__all(m != POISON), 1)) break;
        sfor<U, U + 4>(X1);
        wvm4<0>(xf[U], xf[U + 1], xf[U + 2], xf[U + 3]);
      }
    };
    rd(IC<0>{}, IC<0>{}); rd(IC<0>{}, IC<1>{}); rd(IC<1>{}, IC<0>{}); rd(IC<1>{}, IC<1>{});
    check(IC<0>{}, IC<8>{});
    sfor<0, 28>([&](auto UU) {
      constexpr int U = UU.v;
      mmx<U == 0>(c0, wf[U % 3][0], xf[U]);
      if constexpr (U + 2 < 28) rd(IC<U + 2>{}, IC<0>{});
      if constexpr (U + 12 < 28) X1(IC<U + 12>{});
      if constexpr (U == 16) SP();
      SB();
      mmx<U == 0>(c1, wf[U % 3][1], xf[U]);
      if constexpr (U + 2 < 28) rd(IC<U + 2>{}, IC<1>{});
      if constexpr (U % 4 == 3 && U < 27) check(IC<U / 4 + 1>{}, IC<(U < 16 ? 8 : U == 19 ? 11 : 7)>{});
      SB();
    });
    wvm<0>();
#pragma unroll 1
    for (unsigned spins = 0; spins < SPIN_CAP; ++spins) {
      unsigned m = 0;
      sfor<0, 7>([&](auto I) { tie(sp[I.v]); m = max(m, max(sp[I.v].x, sp[I.v].y)); });
      if (__builtin_expect(__all(m != POISON), 1)) break;
      SP();
      wvm<0>();
    }
    float sm = 0.f, sq = 0.f;
    sfor<0, 7>([&](auto I) { tie(sp[I.v]); sm += u2f(sp[I.v].x); sq += u2f(sp[I.v].y); });
    mean3 = swap_add(sm) * (1.f / D);
    const float ve = fmaxf(swap_add(sq) * (1.f / D) - mean3 * mean3, 0.f) + 1e-5f;
    gk3 = gelu_k(__builtin_amdgcn_rsqf(ve), __builtin_amdgcn_sqrtf(ve));
    tie(gk3); tie(mean3);
  }
  bar();
  ST(13);
  ldsw<u32x2>(R1 + 49152 + fresh(w) * 512 + lane * 8, cs3);
  unsigned csb = R1 + 49152 + fresh(w) * 512 + h * 16;
  tie(csb);
  auto CS3 = [&](int t) { sfor<0, 8>([&](auto A) { cst[A.v] = ldsr<f32x4>(csb + t * 128 + A.v * 32); }); };
  const unsigned so3 = OFF_H3 + (fresh(T32) * 112 + 8 * g.s14) * 1024;
  unsigned pk[4];
  auto G = [&](auto JJ) {
    constexpr int j = JJ.v, t = j >> 3, e = 2 * (j & 7);
    const f32x16& c = t == 0 ? c0 : t == 1 ? c1 : t == 2 ? c2 : c3;
    const f32x2 z = gelu2(f32x2{c[e], c[e + 1]} - f32x2{cst[4 * (t & 1) + (e >> 2)][e & 3], cst[4 * (t & 1) + (e >> 2)][(e & 3) + 1]} * mean3, gk3);
    pk[j & 3] = pk2(z[0], z[1]);
    if constexpr ((j & 3) == 3) st16s<(j >> 2 & 3) * 1024>(u32x4{pk[0], pk[1], pk[2], pk[3]}, sWS, l16, so3 + (j >> 4) * 4096);
  };
  CS3(0);
  settle(c0, c1);
  sfor<0, 16>([&](auto J) { G(J); });
  ST(24);
  {
    u32x4 wf[4];
    a0 = tra(4, RA, 0); a1 = tra(4, RA, 64);
    auto rd = [&](auto MM) { const unsigned ad = (MM.v < 28 ? a0 : a1) + (MM.v % 28) * 2048; wf[MM.v & 3] = trfrag(ad, ad + 1024); };
    rd(IC<0>{}); rd(IC<1>{}); rd(IC<2>{});
    sfor<0, 56>([&](auto MM) {
      constexpr int M = MM.v;
      mmx<M % 28 == 0>(M < 28 ? c2 : c3, wf[M & 3], xf[M % 28]);
      if constexpr (M + 3 < 56) rd(IC<M + 3>{});
      if constexpr (M == M3) { wvm<0>(); asm volatile("s_barrier" ::: "memory"); }
      if constexpr (M == 50) CS3(2);
      SB();
    });
  }
  ST(14);
  settle(c2, c3);
  sfor<16, 24>([&](auto J) { G(J); });
  ST(15);
  u32x4 af[4][4];
  unsigned av[2] = {lnv(OFF_H3 + fresh(tc) * 112 * 1024), lnv(OFF_H3 + (fresh(tc) ^ 1) * 112 * 1024)}, as;
  auto ACT1 = [&](auto SS, auto II) {
    constexpr int s = SS.v;
    if constexpr (s == 14 && II.v == 0) { av[0] = lnv(OFF_O + fresh(tc) * 32 * 1024); av[1] = lnv(OFF_O + (fresh(tc) ^ 1) * 32 * 1024); }
    if constexpr (II.v == 0) as = s < 14 ? p4_k32(fresh(w), s) * 2048 : (4 * fresh(w) + s - 14) * 2048;
    ld16s<(II.v & 1) * 1024>(af[s & 3][II.v], av[II.v >> 1], sWS, as);
  };
  unsigned bs[3][2];
  sfor<0, 3>([&](auto I) { bs[I.v][0] = tra(4, p4_slot(fresh(w), 3 * I.v), fresh(kh) * 64); bs[I.v][1] = bs[I.v][0] ^ 64u; tie(bs[I.v][1]); });
  u32x4 wf[2][4];
  auto RW = [&](auto SS, auto I) {
    constexpr int j = SS.v % 9;
    const unsigned aa = bs[j / 3][I.v & 1] + (j % 3) * 4096 + (I.v >> 1) * 2048;
    wf[SS.v & 1][I.v] = trfrag(aa, aa + 1024);
  };
  bar();
  sfor<0, 8>([&](auto K) { ACT1(IC<K.v / 4>{}, IC<K.v % 4>{}); });
  tie(c3);
  sfor<24, 32>([&](auto J) { G(J); });
  sfor<8, 12>([&](auto K) { ACT1(IC<K.v / 4>{}, IC<K.v % 4>{}); });
  ST(16);
  sfor<0, 4>([&](auto I) { RW(IC<0>{}, I); });
  f32x16 d0, d1, d2, d3;
  const unsigned exw = lnv(R1 + fresh(w) * 12288), own = lnv(p4_slot(fresh(w), 0));
  sfor<0, 18>([&](auto SS) {
    constexpr int s = SS.v;
    if constexpr (s == 4 || s == 8 || s == 11 || s == 15) bar();
    if constexpr (s % 3 == 0) ST(17 + s / 3);
    wvm4<(s == 0 ? 10 : s <= 15 ? 8 : 4 * (17 - s))>(af[s & 3][0], af[s & 3][1], af[s & 3][2], af[s & 3][3]);
    if constexpr (s == 1) asm volatile("s_barrier" ::: "memory");
    f32x16 &a0 = s < 14 ? c0 : d0, &a1 = s < 14 ? c1 : d1, &a2 = s < 14 ? c2 : d2, &a3 = s < 14 ? c3 : d3;
    auto side = [&](auto KK) {
      constexpr int k = 4 * s + KK.v;
      if constexpr (s + 3 <= 17) ACT1(IC<s + 3>{}, KK);
      if constexpr (k == 56) { settle(c0, c1); settle(c2, c3); }
      if constexpr (k >= 56 && k < 60) {
        sfor<0, 3>([&](auto J) { constexpr int A = 3 * (k - 56) + J.v; ldsw<f32x4>(exw + A * 1024, quad(A < 4 ? c1 : A < 8 ? c2 : c3, A & 3)); });
        ldsw<f32x4>(own + (k - 56) * 1024, quad(c0, k - 56));
      }
    };
    sfor<0, 2>([&](auto U) {
      mmx<s % 14 == 0 && U.v == 0>(a0, wf[s & 1][2 * U.v], af[s & 3][U.v]);
      if constexpr (s + 1 < 18) RW(IC<s + 1>{}, IC<2 * U.v>{});
      mmx<s % 14 == 0 && U.v == 0>(a1, wf[s & 1][2 * U.v], af[s & 3][2 + U.v]);
      side(IC<2 * U.v>{});
      SB();
      mmx<s % 14 == 0 && U.v == 0>(a2, wf[s & 1][2 * U.v + 1], af[s & 3][U.v]);
      if constexpr (s + 1 < 18) RW(IC<s + 1>{}, IC<2 * U.v + 1>{});
      mmx<s % 14 == 0 && U.v == 0>(a3, wf[s & 1][2 * U.v + 1], af[s & 3][2 + U.v]);
      side(IC<2 * U.v + 1>{});
      SB();
    });
  });
  ST(23);
  settle(d0, d1); tie(d2); tie(d3);
  const unsigned e3[3] = {lnv(RA + (fresh(w) ^ 1) * 12288), lnv(RA + (fresh(w) ^ 2) * 12288), lnv(RA + (fresh(w) ^ 3) * 12288)};
  unsigned oo = ftok * 896 + h * 16 + fresh(g.s7) * 128 + kh * 64;
  tie(oo);
  sfor<0, 12>([&](auto A) { ldsw<f32x4>(exw - R1 + A.v * 1024, quad(A.v < 4 ? d1 : A.v < 8 ? d2 : d3, A.v & 3)); });
  bar();
  sfor<0, 12>([&](auto A) {
    const f32x4 v = ldsr<f32x4>(e3[A.v / 4] + A.v * 1024);
    sfor<0, 4>([&](auto I) { d0[4 * (A.v & 3) + I.v] += v[I.v]; });
  });
  sfor<0, 4>([&](auto A) {
    const f32x4 v = ldsr<f32x4>(own + 4096 + A.v * 1024);
    sfor<0, 4>([&](auto I) { c0[4 * A.v + I.v] = v[I.v]; });
  });
  const srd_t sO = mk(p.out, 2048 * 896);
  sfor<0, 2>([&](auto U) {
    u32x4 ov;
    sfor<0, 4>([&](auto I) {
      constexpr int e = 8 * U.v + 2 * I.v;
      ov[I.v] = pk2(fmaf(d0[e], c0[e], bflo(xv[U.v][I.v])), fmaf(d0[e + 1], c0[e + 1], bfhi(xv[U.v][I.v])));
    });
    __builtin_amdgcn_raw_buffer_store_b128(cross(ov), sO, oo + U.v * 32, 0, 0);
  });
}

DI void spare_path(const Params& p, const int w, const int xc, const int r STARG) {
  const int tid = threadIdx.x, lane = tid & 63, t32 = lane & 31, h = lane >> 5, tb = w & 3, ch = w >> 2;
  constexpr unsigned WPHI = 160768, ZERO = 159744, DUMMY = 158720;
  const int rr = lane >> 3, cp = lane & 7, rs = r - 28;
  const srd_t sWS = mk(p.ws, WS_USED), sX = mk(p.x, 2048 * 896);
  GAS u64* fl = (GAS u64*)(p.ws + OFF_FLG);
  const unsigned srow = 512 * (xc >> 1) + 64 * ((xc & 1) * 4 + rs) + 8 * w + rr;
  u32x2 ev;
  ld8s<0>(ev, 0u, sWS, OFF_FLG);
  u32x4 xr7[7];
  sfor<0, 7>([&](auto K) { ld16p(xr7[K.v], srow * 896 + cp * 16, sX, K.v * 128); });
  const int hd = 4 * (xc >> 1) + rs, hf = xc & 1;
  wvm<0>(ev);
  const u64 tag = make_tag(ev);
  const u32x2 tg = {(unsigned)tag, (unsigned)(tag >> 32)};
  {
    float a1 = 0.f, a2 = 0.f;
    sfor<0, 7>([&](auto K) { tie(xr7[K.v]); sfor<0, 4>([&](auto I) { a1 = dot2(xr7[K.v][I.v], ONE2, a1); a2 = dot2(xr7[K.v][I.v], xr7[K.v][I.v], a2); }); });
    sfor<0, 3>([&](auto K) { a1 += __shfl_xor(a1, 1 << K.v); a2 += __shfl_xor(a2, 1 << K.v); });
    const float mean = a1 * (1.f / D), ve = fmaxf(a2 * (1.f / D) - mean * mean, 0.f) + 1e-5f;
    st16s(u32x4{f2u(mean), tg.x, f2u(ve), tg.y}, sWS, cp == 0 ? srow * 16 : OOB, OFF_ST0);
  }
  ST(1);
  const int bi = (xc >> 1) * 4 + rs, par = xc & 1;
  const int q = (lane & 15) >> 2, pp = lane & 3, blk = (lane >> 4) & 1;
  const unsigned acol = (unsigned)((((2 * blk + (pp >> 1)) ^ ((q >> 1) << 2)) << 4) + (pp & 1) * 8);
  const unsigned aperm = (4 * h + q) * 128 + acol;
  const srd_t sW3 = mk(p.w3, 448 * 3584);
  const unsigned wsw = (cp ^ (((rr >> 1) & 1) << 2)) << 4;
  {
    const srd_t s0 = bi < 7 ? mk(p.wqkv, 448 * 2688) : sW3, sP = mk(p.wphi, 16 * 1568);
    const unsigned pitch = bi < 7 ? 2688 : 3584, col = (14 * par + (bi < 7 ? bi : bi - 7)) * 128;
    sfor<0, 7>([&](auto K) { dma16(rr * pitch + wsw, s0, (8 * K.v + w) * 8 * pitch + col, RA + (8 * K.v + w) * 1024); });
    dma16(lane * 16, sP, hd * 1568 + (w & 1) * 1024, WPHI + (w & 1) * 1024);
  }
  if (bi >= 11) sfor<0, 7>([&](auto K) { dma16(rr * 3584 + wsw, sW3, (8 * K.v + w) * 8 * 3584 + (14 * par + bi - 2) * 128, R1 + (8 * K.v + w) * 1024); });
  const int oh = 12 + rs, og = 2 * xc + 1;
  dma16(lane * 16, mk(p.wphi, 16 * 1568), oh * 1568 + (w & 1) * 1024, WPHI - 4096 + (w & 1) * 1024);
  if (tid < 16) ldsw<unsigned>(ZERO + tid * 4, 0u);
  wvm<0>();
  bar();
  cs_write(R2 + w * 128, colsum<7>(RA + (aperm ^ (ch * 64)) + tb * 7 * 2048), lane);
  if (bi >= 11) cs_write(R2 + 1024 + w * 128, colsum<7>(R1 + (aperm ^ (ch * 64)) + tb * 7 * 2048), lane);
  bar();
  const int cb0 = bi < 7 ? 28 + 7 * par + bi : 14 * par + bi - 7, cb1 = 14 * par + bi - 2;
  if (tb == 0 || (tb == 1 && bi >= 11)) {
    const f32x16 cs = cs_read(R2 + tb * 1024 + ch * 512 + h * 16, 128);
    sfor<0, 4>([&](auto A) { st16s<A.v * 32>(__builtin_bit_cast(u32x4, quad(cs, A.v)), sWS, t32 == 0 ? h * 16 : OOB, OFF_CS3 + (tb ? cb1 : cb0) * 256 + ch * 128); });
    wvm<0>();
  }
  bar();
  publish(sWS, tg, FL_CS + (tid ? cb1 : cb0), tid == 0 || (tid == 1 && bi >= 11));
  ST(2);
  const int gq = 8 * hf + w;
  const unsigned qkvs = OFF_QKV + gq * 4 * 84 * 1024;
  const int b01 = (28 * hd + (lane & 1) * 27) >> 6;
  const unsigned img = w * 18432;
  u32x4 wp[3];
  phi_frags(wp, hd, WPHI, ZERO, lane);
  unsigned vw[6];
  sfor<0, 6>([&](auto A) {
    const int d0 = 8 * A.v + 4 * h - ((28 * hd) & 15);
    vw[A.v] = d0 >= 0 && d0 < 28 ? img + 16384 + t32 * 64 + (((d0 >> 2) ^ ((t32 >> 1) & 7)) << 3) : DUMMY + lane * 8;
  });
  u32x4 kp[4][3], vp[4][3];
  auto gather = [&](u32x4 (&d)[4][3], unsigned base) { sfor<0, 4>([&](auto T) { head_loads(d[T.v], hd, qkvs + (T.v * 84 + base) * 1024, sWS, lane * 16); }); };
  const unsigned fq = gq * 14 + b01;
  poll(fl, 2, FL_A + fq + 7, tag);
  ST(3);
  gather(kp, 28);
  if (h) ldsw<u32x2>(img + 16384 + t32 * 64 + ((7 ^ ((t32 >> 1) & 7)) << 3), u32x2{0x3f80u, 0u});
  sfor<0, 4>([&](auto T) {
    wvm<9 - 3 * T.v>();
    u32x4 ph[4];
    features(wp, kp[T.v], h != 0, ph);
    const unsigned pr = img + T.v * 4096 + t32 * 128, sw = h ^ (((t32 >> 1) & 1) << 3) ^ (t32 & 1) ^ (((t32 >> 2) & 3) << 1);
    sfor<0, 8>([&](auto A) { ldsw<u32x2>(pr + (((2 * A.v) ^ sw) << 3), u32x2{ph[A.v >> 1][2 * (A.v & 1)], ph[A.v >> 1][2 * (A.v & 1) + 1]}); });
  });
  ST(4);
  poll(fl, 4, (lane & 2) ? FL_Q + fq + 7 : FL_Q + og * 14 + ((28 * oh + (lane & 1) * 27) >> 6), tag);
  ST(5);
  gather(vp, 56);
  u32x4 qf[3];
  head_loads(qf, oh, OFF_QKV + (4 * og + tb) * 84 * 1024, sWS, lane * 16);
  f32x16 kvp = zero16(), kvm = zero16();
  unsigned aa[2], ba[2];
  sfor<0, 2>([&](auto T) {
    const int row = 8 * h + 4 * T.v + q, pc = 4 * blk + pp;
    aa[T.v] = img + row * 128 + ((pc ^ ((q >> 1) << 3) ^ (q & 1) ^ ((2 * h + T.v) << 1)) << 3);
    ba[T.v] = img + 16384 + row * 64 + ((pc ^ (row >> 1)) << 3);
  });
  sfor<0, 4>([&](auto T) {
    wvm<12 - 3 * T.v>();
    sfor<0, 3>([&](auto I) { tie(vp[T.v][I.v]); });
    sfor<0, 6>([&](auto A) { ldsw<u32x2>(vw[A.v], u32x2{vp[T.v][A.v >> 1][2 * (A.v & 1)], vp[T.v][A.v >> 1][2 * (A.v & 1) + 1]}); });
    sfor<0, 2>([&](auto U) {
      constexpr unsigned oa = T.v * 4096 + U.v * 2048;
      const u32x4 bf = trfrag(ba[0] + U.v * 1024, ba[1] + U.v * 1024);
      kvp = mfma(trfrag(aa[0] + oa, aa[1] + oa), bf, kvp);
      kvm = mfma(trfrag((aa[0] ^ 64u) + oa, (aa[1] ^ 64u) + oa), bf, kvm);
    });
  });
  sfor<0, 8>([&](auto A) { ldsw<f32x4>(img + A.v * 1024 + lane * 16, quad(A.v < 4 ? kvp : kvm, A.v & 3)); });
  bar();
  f32x4 own = {0.f, 0.f, 0.f, 0.f};
  sfor<0, 8>([&](auto S) { own += ldsr<f32x4>(S.v * 18432 + w * 1024 + lane * 16); });
  st8s(u32x2{pk2(own[0], own[1]), pk2(own[2], own[3])}, sWS, lane * 16 + (w & 1) * 8, OFF_KVP + ((hd * 2 + hf) * 4 + (w >> 1)) * 1024);
  wvm<0>();
  bar();
  publish(sWS, tg, FL_KV + hd * 2 + hf, tid == 0);
  ST(6);
  if (ch == 0) {
    u32x4 ph[4], kv[8];
    phi_frags(wp, oh, WPHI - 4096, ZERO, lane);
    features(wp, qf, h != 0, ph);
    poll(fl, 2, FL_KV + oh * 2 + (lane & 1), tag);
    kv_loads(kv, oh, sWS, lane * 16);
    wvm<0>();
    o_store(kv, ph, h != 0, sWS, lane * 16, OFF_O + ((4 * og + tb) * 32 + 2 * oh) * 1024);
    wvm<0>();
  }
  bar();
  publish(sWS, tg, FL_O + og * 16 + oh, tid == 0);
  ST(8);
  if (xc == 0 && rs == 0 && w == 0) {
    sfor<0, 4>([&](auto I) { poll(fl, 56, FL_Q + 56 * I.v + lane, tag); });
    publish(sWS, tg, FL_E, lane == 0);
  }
}

__global__ __launch_bounds__(512, 2) void fused(Params p) {
  u64 pc;
  asm volatile("s_getpc_b64 %0" : "=s"(pc));
  ST(0);
  int w = __builtin_amdgcn_readfirstlane(threadIdx.x >> 6);
  asm volatile("s_nop 4" : "+s"(w));
  const int xc = blockIdx.x & 7, r = blockIdx.x >> 3;
  unsigned dummy = 0;
  if (r < 28 && w >= 4) first_loads(p, w - 4, xc, r);
  if (r < 28 && w < 4) code_prefetch(dummy, pc, (((r & 1) * 4 + w) * 64 + (threadIdx.x & 63)) * 128);
  SB();
  if (r < 28) {
    if (w < 4) compute_path(p, dummy, w, xc, r STPASS);
    else loader_path(p, w - 4, xc, r STPASS);
  }
  else spare_path(p, w, xc, r STPASS);
}

extern "C" void kernel_launch(void* const* d_in, const int* in_sizes, int n_in, void* d_out, int out_size, void* d_ws, size_t ws_size,
                              hipStream_t stream) {
  Params p;
  p.x = (const uint16_t*)d_in[0]; p.wqkv = (const uint16_t*)d_in[1]; p.wphi = (const uint16_t*)d_in[2]; p.wo = (const uint16_t*)d_in[3];
  p.w1 = (const uint16_t*)d_in[5]; p.w2 = (const uint16_t*)d_in[6]; p.w3 = (const uint16_t*)d_in[7]; p.w4 = (const uint16_t*)d_in[8];
  p.out = (uint16_t*)d_out; p.ws = (char*)d_ws + ((ws_size - WS_USED) & ~(size_t)0xFFFFF);
  (void)hipFuncSetAttribute((const void*)fused, hipFuncAttributeMaxDynamicSharedMemorySize, LDS_BYTES);
  fused<<<256, 512, LDS_BYTES, stream>>>(p);
}
```

```cpp
#include <hip/hip_runtime.h>
#include <stdint.h>

#define LAS __attribute__((address_space(3)))
#define GAS __attribute__((address_space(1)))
#define DI __device__ __forceinline__
typedef short s16x4 __attribute__((ext_vector_type(4)));
typedef __bf16 bf16x8 __attribute__((ext_vector_type(8)));
typedef __bf16 bf16x2 __attribute__((ext_vector_type(2)));
typedef float f32x16 __attribute__((ext_vector_type(16)));
typedef float f32x4 __attribute__((ext_vector_type(4)));
typedef float f32x2 __attribute__((ext_vector_type(2)));
typedef unsigned u32x4 __attribute__((ext_vector_type(4)));
typedef unsigned u32x2 __attribute__((ext_vector_type(2)));
typedef unsigned long long u64;
typedef __amdgpu_buffer_rsrc_t srd_t;

constexpr int D = 448;
constexpr float LOG2E = 1.4426950408889634f;
constexpr unsigned OFF_FLG = 0;
constexpr unsigned OFF_H = 262144;
constexpr unsigned OFF_X1 = OFF_H + 64 * 28 * 1024;
constexpr unsigned OFF_QKV = OFF_X1 + 64 * 28 * 1024;
constexpr unsigned OFF_H3 = OFF_QKV + 64 * 84 * 1024;
constexpr unsigned OFF_O = OFF_H3 + 64 * 112 * 1024;
constexpr unsigned OFF_KVP = OFF_O + 64 * 32 * 1024;
constexpr unsigned OFF_ST0 = OFF_KVP + 32 * 4096;
constexpr unsigned OFF_ST1 = OFF_ST0 + 2048 * 16;
constexpr unsigned OFF_CS3 = OFF_ST1 + 14 * 2048 * 8;
constexpr unsigned WS_USED = OFF_CS3 + 42 * 256;
constexpr size_t OFF_STAMP = 64u << 20;
constexpr int FL_E = 0, FL_A = 1, FL_Q = FL_A + 16 * 14, FL_3A = FL_Q + 16 * 14, FL_3B = FL_3A + 16 * 14,
              FL_O = FL_3B + 16 * 14, FL_KV = FL_O + 256, FL_CS = FL_KV + 32;
static_assert((FL_CS + 42) * 128 <= 262144, "flag block");
constexpr unsigned RA = 0, R1 = 57344, R2 = 114688, LDS_BYTES = 163840;
constexpr unsigned OOB = 0x80000000u;
constexpr unsigned CODE_BYTES = 46080;
constexpr unsigned SPIN_CAP = 1u << 18;

struct Params {
  const uint16_t *x, *wqkv, *wphi, *wo, *w1, *w2, *w3, *w4;
  uint16_t* out;
  char* ws;
};

template <int I> struct IC { static constexpr int v = I; };
template <int B, int E, class F> DI void sfor(F&& f) {
  if constexpr (B < E) { f(IC<B>{}); sfor<B + 1, E>(f); }
}
DI f32x16 mfma(u32x4 a, u32x4 b, f32x16 c) {
  return __builtin_amdgcn_mfma_f32_32x32x16_bf16(__builtin_bit_cast(bf16x8, a), __builtin_bit_cast(bf16x8, b), c, 0, 0, 0);
}
DI void mma(f32x16& c, u32x4 a, u32x4 b) { asm volatile("v_mfma_f32_32x32x16_bf16 %0, %1, %2, %0" : "+v"(c) : "v"(a), "v"(b) : "memory"); }
DI void mma0(f32x16& c, u32x4 a, u32x4 b) { asm volatile("v_mfma_f32_32x32x16_bf16 %0, %1, %2, 0" : "=&v"(c) : "v"(a), "v"(b) : "memory"); }
template <bool FIRST> DI void mmx(f32x16& c, u32x4 a, u32x4 b) { if constexpr (FIRST) mma0(c, a, b); else mma(c, a, b); }
DI void settle(f32x16& a, f32x16& b) { asm volatile("s_nop 11" : "+v"(a), "+v"(b)); }
DI unsigned pk2(float a, float b) {
  f32x2 v = {a, b};
  return __builtin_bit_cast(unsigned, __builtin_convertvector(v, bf16x2));
}
DI float u2f(unsigned u) { return __builtin_bit_cast(float, u); }
DI unsigned f2u(float f) { return __builtin_bit_cast(unsigned, f); }
DI float bflo(unsigned u) { return __builtin_bit_cast(float, u << 16); }
DI float bfhi(unsigned u) { return __builtin_bit_cast(float, u & 0xffff0000u); }
DI u32x4 trfrag(unsigned a0, unsigned a1) {
  u32x2 lo = __builtin_bit_cast(u32x2, __builtin_amdgcn_ds_read_tr16_b64_v4i16((LAS s16x4*)(uintptr_t)a0));
  u32x2 hi = __builtin_bit_cast(u32x2, __builtin_amdgcn_ds_read_tr16_b64_v4i16((LAS s16x4*)(uintptr_t)a1));
  return u32x4{lo.x, lo.y, hi.x, hi.y};
}
template <class T> DI T ldsr(unsigned a) { return *(LAS T*)(uintptr_t)a; }
template <class T> DI void ldsw(unsigned a, T v) { *(LAS T*)(uintptr_t)a = v; }
DI srd_t mk(const void* p, unsigned bytes) { return __builtin_amdgcn_make_buffer_rsrc((void*)p, 0, (int)bytes, 0x00020000); }
DI void dma16(unsigned voff, srd_t r, unsigned soff, unsigned lds) {
  asm volatile("s_mov_b32 m0, %3\n\ts_nop 0\n\tbuffer_load_dwordx4 %0, %1, %2 offen lds" ::"v"(voff), "s"(r), "s"(soff), "s"(lds) : "memory");
}
DI void dma16s(unsigned voff, srd_t r, unsigned soff, unsigned lds) {
  asm volatile("s_mov_b32 m0, %3\n\ts_nop 0\n\tbuffer_load_dwordx4 %0, %1, %2 offen sc1 lds" ::"v"(voff), "s"(r), "s"(soff), "s"(lds) : "memory");
}
DI void ld4p(unsigned& d, unsigned voff, srd_t r) {
  asm volatile("buffer_load_dword %0, %1, %2, 0 offen" : "=v"(d) : "v"(voff), "s"(r) : "memory");
}
DI void ld16p(u32x4& d, unsigned voff, srd_t r, unsigned soff) {
  asm volatile("buffer_load_dwordx4 %0, %1, %2, %3 offen" : "=v"(d) : "v"(voff), "s"(r), "s"(soff) : "memory");
}
template <int IMM> DI void ld16s(u32x4& d, unsigned voff, srd_t r, unsigned soff) {
  asm volatile("buffer_load_dwordx4 %0, %1, %2, %3 offen offset:%c4 sc1" : "=v"(d) : "v"(voff), "s"(r), "s"(soff), "i"(IMM) : "memory");
}
template <int IMM> DI void ld8s(u32x2& d, unsigned voff, srd_t r, unsigned soff) {
  asm volatile("buffer_load_dwordx2 %0, %1, %2, %3 offen offset:%c4 sc1" : "=v"(d) : "v"(voff), "s"(r), "s"(soff), "i"(IMM) : "memory");
}
template <int IMM = 0> DI void st16s(u32x4 v, srd_t r, unsigned voff, unsigned soff) {
  asm volatile("buffer_store_dwordx4 %0, %1, %2, %3 offen offset:%c4 sc1\n\ts_nop 1" ::"v"(v), "v"(voff), "s"(r), "s"(soff), "i"(IMM) : "memory");
}
DI void st8s(u32x2 v, srd_t r, unsigned voff, unsigned soff) {
  asm volatile("buffer_store_dwordx2 %0, %1, %2, %3 offen sc1" ::"v"(v), "v"(voff), "s"(r), "s"(soff) : "memory");
}
template <int N> DI void wvm() { asm volatile("s_waitcnt vmcnt(%c0)" ::"i"(N) : "memory"); }
template <class T> DI void tie(T& a) { asm volatile("" : "+v"(a)); }
template <int N, class T> DI void wvm(T& a) { asm volatile("s_waitcnt vmcnt(%c1)" : "+v"(a) : "i"(N) : "memory"); }
DI void wlg4(u32x4& a, u32x4& b, u32x4& c, u32x4& d) { asm volatile("s_waitcnt lgkmcnt(0)" : "+v"(a), "+v"(b), "+v"(c), "+v"(d)::"memory"); }
template <int N> DI void wvm4(u32x4& a, u32x4& b, u32x4& c, u32x4& d) {
  asm volatile("s_waitcnt vmcnt(%c4)" : "+v"(a), "+v"(b), "+v"(c), "+v"(d) : "i"(N) : "memory");
}
template <int N> DI void wvm2(u32x4& a, u32x4& b) { asm volatile("s_waitcnt vmcnt(%c2)" : "+v"(a), "+v"(b) : "i"(N) : "memory"); }
DI int fresh(int v) { asm volatile("" : "+s"(v)); return v; }
DI void bar() { asm volatile("s_waitcnt lgkmcnt(0)\n\ts_barrier" ::: "memory"); }
#define SB() __builtin_amdgcn_sched_barrier(0)
DI void halves(float v, float& lo, float& hi) {
  auto r = __builtin_amdgcn_permlane32_swap(f2u(v), f2u(v), false, false);
  lo = u2f(r[0]); hi = u2f(r[1]);
}
DI float swap_add(float v) { float a, b; halves(v, a, b); return a + b; }
DI float swap_max(float v) { float a, b; halves(v, a, b); return fmaxf(a, b); }
DI float swap_hi(float v) { float a, b; halves(v, a, b); return b; }
typedef f32x4 GeluK;
DI GeluK gelu_k(float r, float ir) {
  const float c0 = -2.f * 0.7978845608028654f * LOG2E, c1 = c0 * 0.044715f;
  return GeluK{c0 * r, c1 * r * r * r, ir, 0.f};
}
DI f32x2 gelu2(f32x2 z, GeluK k) {
  const f32x2 u = z * (z * z * k[1] + k[0]);
  const f32x2 e = {__builtin_amdgcn_exp2f(u[0]), __builtin_amdgcn_exp2f(u[1])};
  const f32x2 d = e * k[2] + k[2];
  return z * f32x2{__builtin_amdgcn_rcpf(d[0]), __builtin_amdgcn_rcpf(d[1])};
}
DI float dot2(unsigned a, unsigned b, float c) {
  return __builtin_amdgcn_fdot2_f32_bf16(__builtin_bit_cast(bf16x2, a), __builtin_bit_cast(bf16x2, b), c, false);
}
constexpr unsigned ONE2 = 0x3f803f80u;
DI f32x16 zero16() { f32x16 z; for (int i = 0; i < 16; ++i) z[i] = 0.f; return z; }
DI u32x4 pack8(const f32x16& a, int u) {
  return u32x4{pk2(a[8 * u], a[8 * u + 1]), pk2(a[8 * u + 2], a[8 * u + 3]), pk2(a[8 * u + 4], a[8 * u + 5]), pk2(a[8 * u + 6], a[8 * u + 7])};
}
DI void poll(GAS u64* fl, int n, int idx, u64 tag) {
  const bool on = (int)(threadIdx.x & 63) < n;
#pragma unroll 1
  for (unsigned spins = 0; spins < SPIN_CAP; ++spins) {
    u64 v = tag;
    if (on) v = __hip_atomic_load(fl + idx * 16, __ATOMIC_RELAXED, __HIP_MEMORY_SCOPE_AGENT);
    if (__all(v == tag)) break;
    __builtin_amdgcn_s_sleep(1);
  }
  wvm<0>();
}
DI void publish(srd_t ws, u32x2 tg, unsigned idx, bool on) { st8s(tg, ws, on ? idx * 128 : OOB, OFF_FLG); }
DI bool all_set(u32x2 f, u32x2 tg) { return __all(f.x == tg.x && f.y == tg.y); }

#define ST(i) do {} while (0)

template <int NU> DI f32x16 colsum(unsigned a) {
  const u32x4 ones = {ONE2, ONE2, ONE2, ONE2};
  f32x16 c = zero16();
  sfor<0, NU>([&](auto UU) { c = mfma(trfrag(a + UU.v * 2048, a + UU.v * 2048 + 1024), ones, c); });
  return c;
}
DI void cs_write(unsigned dst, const f32x16& c, int lane) {
  if ((lane & 31) == 0) {
    const unsigned d = dst + (lane >> 5) * 16;
    sfor<0, 4>([&](auto A) { ldsw<f32x4>(d + A.v * 32, f32x4{c[4 * A.v], c[4 * A.v + 1], c[4 * A.v + 2], c[4 * A.v + 3]}); });
  }
}
DI float max3(float a, float b, float c) { float r; asm("v_max3_f32 %0, %1, %2, %3" : "=v"(r) : "v"(a), "v"(b), "v"(c)); return r; }
DI float min3(float a, float b, float c) { float r; asm("v_min3_f32 %0, %1, %2, %3" : "=v"(r) : "v"(a), "v"(b), "v"(c)); return r; }
DI void dual_softmax(f32x16 p, bool hi, u32x4 (&o)[4]) {
  p[0] += 0.f;
#pragma unroll
  for (int i = 12; i < 16; ++i) p[i] = hi ? p[0] : p[i];
  float mx = max3(p[0], p[1], p[2]), mn = min3(p[0], p[1], p[2]);
#pragma unroll
  for (int i = 3; i < 15; i += 2) { mx = max3(mx, p[i], p[i + 1]); mn = min3(mn, p[i], p[i + 1]); }
  float a, b;
  halves(max3(mx, p[15], p[15]), a, b);
  const float cp = -LOG2E * max3(a, b, b);
  halves(min3(mn, p[15], p[15]), a, b);
  const float cm = LOG2E * min3(a, b, b);
  f32x2 ep[8], em[8];
#pragma unroll
  for (int i = 0; i < 8; ++i) {
    const f32x2 v = {p[2 * i], p[2 * i + 1]}, x = v * LOG2E + cp, y = v * -LOG2E + cm;
    ep[i] = f32x2{__builtin_amdgcn_exp2f(x[0]), __builtin_amdgcn_exp2f(x[1])};
    em[i] = f32x2{__builtin_amdgcn_exp2f(y[0]), __builtin_amdgcn_exp2f(y[1])};
  }
  const float keep = hi ? 0.f : 1.f;
  const f32x2 sp = ((ep[0] + ep[1]) + (ep[2] + ep[3])) + ((ep[4] + ep[5]) + (ep[6] + ep[7]) * keep);
  const f32x2 sn = ((em[0] + em[1]) + (em[2] + em[3])) + ((em[4] + em[5]) + (em[6] + em[7]) * keep);
  const float rp = __builtin_amdgcn_rcpf(swap_add(sp[0] + sp[1])), rm = __builtin_amdgcn_rcpf(swap_add(sn[0] + sn[1]));
#pragma unroll
  for (int i = 0; i < 8; ++i) {
    const f32x2 x = ep[i] * (i < 6 ? rp : rp * keep), y = em[i] * (i < 6 ? rm : rm * keep);
    o[i >> 2][i & 3] = pk2(x[0], x[1]);
    o[2 + (i >> 2)][i & 3] = pk2(y[0], y[1]);
  }
}
DI void granule(u32x4& g, srd_t ws, unsigned voff, u32x2 tg) {
#pragma unroll 1
  for (unsigned spins = 0; spins < SPIN_CAP && !__all(g[1] == tg.x && g[3] == tg.y); ++spins) {
    ld16s<0>(g, voff, ws, OFF_ST0);
    wvm<0>(g);
  }
}
DI f32x16 cs_read(unsigned a, unsigned str) {
  f32x4 v[4][4];
  sfor<0, 4>([&](auto P) { sfor<0, 4>([&](auto A) { v[P.v][A.v] = ldsr<f32x4>(a + P.v * str + A.v * 32); }); });
  f32x16 r;
  sfor<0, 4>([&](auto A) {
    const f32x4 t = (v[0][A.v] + v[1][A.v]) + (v[2][A.v] + v[3][A.v]);
    sfor<0, 4>([&](auto I) { r[4 * A.v + I.v] = t[I.v]; });
  });
  return r;
}
DI f32x4 quad(const f32x16& c, int a) { return f32x4{c[4 * a], c[4 * a + 1], c[4 * a + 2], c[4 * a + 3]}; }
DI u32x4 cross(u32x4 v) {
  auto a = __builtin_amdgcn_permlane32_swap(v[0], v[2], false, false);
  auto b = __builtin_amdgcn_permlane32_swap(v[1], v[3], false, false);
  return u32x4{a[0], b[0], a[1], b[1]};
}

#define STARG
#define STPASS
DI void code_prefetch(unsigned& d, u64 pc, unsigned voff) {
  asm volatile("buffer_load_dword %0, %1, %2, 0 offen" : "=v"(d) : "v"(voff), "s"(mk((const void*)(pc & ~127ull), CODE_BYTES)) : "memory");
}
DI u64 make_tag(u32x2 ev) {
  return (((u64)(unsigned)__builtin_amdgcn_readfirstlane(ev.y) << 32) | (unsigned)__builtin_amdgcn_readfirstlane(ev.x)) + 0x9E3779B97F4A7C15ull;
}

struct SlotP1 {
  static constexpr unsigned off(int c) { return R2 - 16384 + ((c + 1) % 4) * 16384; }
  static DI unsigned addr(const unsigned (&xr)[4], int U) { return xr[U & 3] + ((U / 4 + 1) % 4) * 16384; }
};
constexpr unsigned HREG = R2;
constexpr unsigned XT = R2 + 40960;
constexpr unsigned WPH = XT + 1024;
constexpr int NE = 2;
constexpr int UH = 12;
constexpr int NB = 1;
constexpr unsigned POISON = 0xFFFFFFFFu;
constexpr int M3 = 20;
template <int TS, class Slot>
DI void pass_a(f32x16& c0, f32x16& c1, const unsigned a0, const unsigned a1, const unsigned (&xr)[4], u32x4* xf) {
  auto rx = [&](auto UU) { xf[UU.v] = ldsr<u32x4>(Slot::addr(xr, UU.v)); };
  u32x4 wf[3][2];
  auto rd = [&](auto UU, auto NN) {
    const unsigned ad = (NN.v ? a1 : a0) + UU.v * 2048;
    wf[UU.v % 3][NN.v] = trfrag(ad, ad + TS);
  };
  bar();
  rx(IC<0>{}); rd(IC<0>{}, IC<0>{}); rd(IC<0>{}, IC<1>{}); rx(IC<1>{}); rd(IC<1>{}, IC<0>{}); rd(IC<1>{}, IC<1>{});
  sfor<0, 28>([&](auto UU) {
    constexpr int U = UU.v;
    if constexpr (U % 4 == 2 && U < 26) asm volatile("s_barrier" ::: "memory");
    mmx<U == 0>(c0, wf[U % 3][0], xf[U]);
    if constexpr (U + 2 < 28) { rx(IC<U + 2>{}); rd(IC<U + 2>{}, IC<0>{}); }
    SB();
    mmx<U == 0>(c1, wf[U % 3][1], xf[U]);
    if constexpr (U + 2 < 28) rd(IC<U + 2>{}, IC<1>{});
    SB();
  });
}
DI void phi_frags(u32x4 (&wp)[3], int hd, unsigned wphi, unsigned zero, int lane) {
  const int o = (28 * hd) & 15, h = lane >> 5, q = (lane & 15) >> 2;
  const unsigned c = ((lane >> 4) & 1) * 32 + (lane & 3) * 8;
  sfor<0, 3>([&](auto I) {
    unsigned a[2];
    sfor<0, 2>([&](auto T) { const int d0 = 16 * I.v + 8 * T.v + 4 * h - o; a[T.v] = (d0 >= 0 && d0 < 28 ? wphi + (d0 + q) * 56 : zero) + c; });
    wp[I.v] = trfrag(a[0], a[1]);
  });
}
DI void head_loads(u32x4 (&d)[3], int hd, unsigned base, srd_t ws, unsigned l16) {
  const unsigned so = base + ((28 * hd) >> 4) * 1024;
  ld16s<0>(d[0], l16, ws, so); ld16s<1024>(d[1], l16, ws, so); ld16s<0>(d[2], l16, ws, so + (((28 * hd) & 15) >= 8 ? 2048 : 0));
}
DI void features(const u32x4 (&wp)[3], u32x4 (&k)[3], bool hi, u32x4 (&out)[4]) {
  sfor<0, 3>([&](auto I) { tie(k[I.v]); });
  f32x16 f = mfma(wp[0], k[0], zero16());
  f = mfma(wp[1], k[1], f);
  f = mfma(wp[2], k[2], f);
  dual_softmax(f, hi, out);
}
DI void o_store(u32x4 (&kv)[8], const u32x4 (&ph)[4], bool hi, srd_t ws, unsigned l16, unsigned so) {
  f32x16 n = zero16();
  sfor<0, 8>([&](auto I) { tie(kv[I.v]); n = mfma(kv[I.v], ph[I.v & 3], n); });
  const float rd = __builtin_amdgcn_rcpf(swap_hi(n[12]) + 1e-6f);
#pragma unroll
  for (int i = 0; i < 16; ++i) n[i] *= rd;
  u32x4 o1 = pack8(n, 1);
  if (hi) { o1[2] = 0u; o1[3] = 0u; }
  st16s(pack8(n, 0), ws, l16, so); st16s<1024>(o1, ws, l16, so);
}
DI void kv_loads(u32x4 (&kv)[8], int hd, srd_t ws, unsigned l16) {
  sfor<0, 8>([&](auto I) { ld16s<(I.v & 3) * 1024>(kv[I.v], l16, ws, OFF_KVP + hd * 8192 + (I.v >> 2) * 4096); });
}
struct Geo {
  int r7, s7, g4, tt, s14;
  bool wtype;
  DI Geo(int xc, int r) {
    r7 = r / 7;
    s7 = r - 7 * r7;
    g4 = 4 * (xc >> 1) + r7;
    tt = 4 * xc + r7;
    s14 = 7 * (xc & 1) + s7;
    wtype = (xc & 1) == 0;
  }
};
DI unsigned p4_slot(int kq, int j) { return (j < 3 ? R2 : j < 6 ? R1 : RA) + (3 * kq + j % 3) * 4096; }
DI int p4_k32(int kq, int s) {
  const int a = 7 * kq + (s < 7 ? s : s - 7);
  return 4 * (a >> 1) + (a & 1) + (s < 7 ? 0 : 2);
}

DI void first_loads(const Params& p, const int w, const int xc, const int r) {
  const int lane = threadIdx.x & 63;
  const Geo g(xc, r);
  const int rr = lane >> 3, cp = lane & 7;
  const unsigned wsw = (unsigned)((cp ^ (((rr >> 1) & 1) << 2)) << 4);
  const srd_t sX = mk(p.x, 2048 * 896), sQ = mk(p.wqkv, 448 * 2688);
  const srd_t sA = g.wtype ? mk(p.w1, 448 * 896) : sQ;
  const unsigned pitchA = g.wtype ? 896 : 2688;
  const unsigned colA = g.wtype ? g.s7 : 7 + g.s7;
  const unsigned wvA = rr * pitchA + wsw;
  const unsigned xv0 = rr * 896 + ((cp ^ (rr >> 1)) << 4), xv1 = xv0 ^ 64u;
  const unsigned T32 = 4 * g.g4 + w, xs = T32 * 32 * 896;
  auto G1 = [&](int c) {
    sfor<0, 2>([&](auto J) { dma16(wvA, sA, (8 * c + 2 * w + J.v) * 8 * pitchA + colA * 128, RA + (8 * c + 2 * w + J.v) * 1024); });
    sfor<0, 4>([&](auto I) { dma16((I.v & 1) ? xv1 : xv0, sX, xs + I.v * 8 * 896 + c * 128, SlotP1::off(c) + w * 4096 + I.v * 1024); });
  };
  __builtin_amdgcn_s_setprio(3);
  G1(0); G1(1);
}

DI void loader_path(const Params& p, const int w, const int xc, const int r STARG) {
  const int lane = threadIdx.x & 63, t32 = lane & 31, h = lane >> 5;
  const Geo g(xc, r);
  const int rr = lane >> 3, cp = lane & 7;
  const unsigned wsw = (unsigned)((cp ^ (((rr >> 1) & 1) << 2)) << 4);
  const srd_t sWS = mk(p.ws, WS_USED), sX = mk(p.x, 2048 * 896), sQ = mk(p.wqkv, 448 * 2688);
  const srd_t sA = g.wtype ? mk(p.w1, 448 * 896) : sQ;
  const unsigned pitchA = g.wtype ? 896 : 2688;
  const unsigned colA = g.wtype ? g.s7 : 7 + g.s7, colB = g.wtype ? g.s7 : 14 + g.s7;
  const unsigned wvA = rr * pitchA + wsw;
  const unsigned xv0 = rr * 896 + ((cp ^ (rr >> 1)) << 4), xv1 = xv0 ^ 64u;
  const unsigned T32 = 4 * g.g4 + w, xs = T32 * 32 * 896;
  auto G1 = [&](int c) {
    sfor<0, 2>([&](auto J) { dma16(wvA, sA, (8 * c + 2 * w + J.v) * 8 * pitchA + colA * 128, RA + (8 * c + 2 * w + J.v) * 1024); });
    sfor<0, 4>([&](auto I) { dma16((I.v & 1) ? xv1 : xv0, sX, xs + I.v * 8 * 896 + c * 128, SlotP1::off(c) + w * 4096 + I.v * 1024); });
  };
  u32x2 ev;
  ld8s<0>(ev, 0u, sWS, OFF_FLG);
  SB();
  const unsigned wv896 = rr * 896 + wsw, wv3 = rr * 3584 + wsw, wvB = rr * 2688 + wsw;
  auto WB = [&](int k) { dma16(wvB, sQ, (4 * k + w) * 8 * 2688 + colB * 128, R1 + (4 * k + w) * 1024); };
  const int q = (lane & 15) >> 2, pp = lane & 3, blk = (lane >> 4) & 1;
  const unsigned ca = (4 * h + q) * 128 + (unsigned)((((2 * blk + (pp >> 1)) ^ ((q >> 1) << 2)) << 4) + (pp & 1) * 8);
  const u32x4 ones = {ONE2, ONE2, ONE2, ONE2};
  f32x16 s0 = zero16(), s1 = zero16();
  auto CS = [&](unsigned a) { s0 = mfma(trfrag(a, a + 1024), ones, s0); s1 = mfma(trfrag(a ^ 64u, (a ^ 64u) + 1024), ones, s1); };
  const unsigned cw = RA + ca + w * 2048;
  u32x4 gr;
  wvm<7>(); bar(); G1(2); CS(cw);
  wvm<7>(); bar(); G1(3); CS(cw + 8192);
  wvm<6>(ev); bar(); G1(4); CS(cw + 2 * 8192);
  wvm<6>(); bar(); G1(5); CS(cw + 3 * 8192);
  ld16s<0>(gr, (T32 * 32 + t32) * 16, sWS, OFF_ST0);
  wvm<7>(); bar(); G1(6); CS(cw + 4 * 8192);
  const u64 tag = make_tag(ev);
  const u32x2 tg = {(unsigned)tag, (unsigned)(tag >> 32)};
  wvm<6>(gr); bar(); sfor<0, 3>([&](auto K) { WB(K.v); });
  CS(cw + 5 * 8192);
  granule(gr, sWS, (T32 * 32 + t32) * 16, tg);
  const float ve1 = u2f(gr[2]);
  float r1 = __builtin_amdgcn_rsqf(ve1);
  const float v2 = (ve1 - 1e-5f) * r1 * r1 + 1e-5f;
  GeluK k1 = gelu_k(r1 * __builtin_amdgcn_rsqf(v2), __builtin_amdgcn_sqrtf(ve1 * v2));
  k1[3] = u2f(gr[0]);
  tie(k1); tie(r1);
  cs_write(R2 - 16384 + w * 128, s0, lane); cs_write(R2 - 16384 + 512 + w * 128, s1, lane);
  wvm<3>(); bar();
  {
    const unsigned a = R2 - 16384 + h * 512 + t32 * 4, c6 = (RA + ca + 24 * 2048) ^ ((w & 1) * 64);
    u32x4 f[4];
    sfor<0, 4>([&](auto U) { f[U.v] = trfrag(c6 + U.v * 2048, c6 + U.v * 2048 + 1024); });
    const float t = (ldsr<float>(a) + ldsr<float>(a + 128)) + (ldsr<float>(a + 256) + ldsr<float>(a + 384));
    s0 = zero16();
    sfor<0, 4>([&](auto U) { s0 = mfma(f[U.v], ones, s0); });
    sfor<3, 6>([&](auto K) { WB(K.v); });
    ldsw<f32x4>(R2 + 2048 + w * 1024 + lane * 16, k1);
    ldsw<float>(R2 + 6144 + w * 256 + lane * 4, r1);
    ldsw<float>(R2 + 1024 + lane * 4, t);
    cs_write(R2 + 1280 + (w & 1) * 128, s0, lane);
  }
  GAS u64* fl = (GAS u64*)(p.ws + OFF_FLG);
  const bool l0 = threadIdx.x == 256;
  bar();
  __builtin_amdgcn_s_setprio(0);
  sfor<6, 14>([&](auto K) { WB(K.v); });
  sfor<0, 2>([&](auto J) { dma16(rr * 896 + ((cp ^ rr) << 4), sX, (g.tt * 64 + (2 * w + J.v) * 8) * 896 + g.s7 * 128, XT + (2 * w + J.v) * 1024); });
  const srd_t sW2 = mk(p.w2, 448 * 896);
  auto W2 = [&](int k) { const int pc = 4 * k + w; dma16(wv896, sW2, pc * 8 * 896 + g.s7 * 128, RA + pc * 1024); };
  sfor<0, 4>([&](auto K) { W2(K.v); });
  wvm<4>(); bar();
  sfor<4, 14>([&](auto K) { W2(K.v); });
  const srd_t sW4 = mk(p.w4, 1792 * 896), sWo = mk(p.wo, 448 * 896), sW3 = mk(p.w3, 448 * 3584);
  u32x4 w4r[9][4];
  auto L4 = [&](auto SS) {
    constexpr int s = SS.v;
    sfor<0, 4>([&](auto I) {
      u32x4& d = w4r[s - 9][I.v];
      if constexpr (s < 14) ld16p(d, wv896, sW4, (32 * p4_k32(w, s) + 8 * I.v) * 896 + g.s7 * 128);
      else ld16p(d, wv896, sWo, (28 * (4 * w + (s - 14)) + 8 * I.v) * 896 + g.s7 * 128);
    });
  };
  bar();
  publish(sWS, tg, FL_A + g.g4 * 14 + g.s14, l0);
  const int kg = 2 * xc + (r >> 4), kh16 = r & 15;
  dma16(lane * 16, mk(p.wphi, 16 * 1568), kh16 * 1568 + (w & 1) * 1024, WPH + (w & 1) * 1024);
  if (lane < 16) ldsw<unsigned>(WPH + 2048 + lane * 4, 0u);
  sfor<9, 9 + NE>(L4);
  {
    const int th = w & 1, kh = w >> 1, tc = 2 * g.tt + th;
    const int gsel = min(max(tc + ((lane & 4) ? 2 * th - 1 : 0), 0), 63) >> 2;
    const int t3 = T32 >> 1, px = (4 * (t3 >> 3) + (t3 & 3)) * 14 + 7 * ((t3 >> 2) & 1);
    poll(fl, 18, lane < 8 ? FL_A + gsel * 14 + min(3 * kh + (lane & 3), 6) : lane < 15 ? FL_A + px + lane - 8 : lane < 17 ? FL_CS + 2 * g.s14 + (lane & 1) : FL_CS + 28 + g.s14, tag);
  asm volatile("s_mov_b32 m0, %3\n\ts_nop 0\n\tbuffer_load_dword %0, %1, %2 offen sc1 lds" ::"v"(lane * 4), "s"(sWS), "s"(OFF_CS3 + (28 + g.s14) * 256), "s"(XT) : "memory");
    const srd_t sH = mk(p.ws + OFF_H, 64 * 28 * 1024);
    const int tok = tc * 32 + t32, ta = tok - 1 - kh, tb = tok + 1 + kh;
    const unsigned oa = (unsigned)((ta >> 5) * 28 * 1024 + ((ta & 31) + 32 * h) * 16), ob = (unsigned)((tb >> 5) * 28 * 1024 + ((tb & 31) + 32 * h) * 16);
    sfor<0, 6>([&](auto U) { dma16s(oa, sH, (kh * 14 + U.v) * 1024, HREG + w * 10240 + U.v * 1024); });
    wvm<6>();
    bar();
    dma16s(oa, sH, (kh * 14 + 6) * 1024, HREG + w * 10240 + 6144);
    sfor<7, 10>([&](auto U) { dma16s(ob, sH, (kh * 14 + U.v) * 1024, HREG + w * 10240 + U.v * 1024); });
  }
  wvm<0>();
  bar();
  auto o3 = [&](int k) { return (unsigned)((4 * k + w) * 8 * 3584 + g.s14 * 256); };
  sfor<0, 14>([&](auto K) { dma16(wv3, sW3, o3(K.v), R1 + (4 * K.v + w) * 1024); });
  bar();
  publish(sWS, tg, FL_Q + g.g4 * 14 + g.s14, l0);
  sfor<0, 4>([&](auto K) { dma16(wv3, sW3, o3(K.v) + 128, RA + (4 * K.v + w) * 1024); });
  u32x4 qf[3], wp[3], ph[4];
  phi_frags(wp, kh16, WPH, WPH + 2048, lane);
  wvm<5>(); bar();
  sfor<4, 14>([&](auto K) { dma16(wv3, sW3, o3(K.v) + 128, RA + (4 * K.v + w) * 1024); });
  poll(fl, 2, FL_Q + kg * 14 + ((28 * kh16 + (lane & 1) * 27) >> 6), tag);
  head_loads(qf, kh16, OFF_QKV + (4 * kg + w) * 84 * 1024, sWS, lane * 16);
  auto W4 = [&](auto SS) {
    sfor<0, 4>([&](auto I) { dma16(wv896, sW4, (32 * p4_k32(w, SS.v) + 8 * I.v) * 896 + g.s7 * 128, p4_slot(w, SS.v) + I.v * 1024); });
  };
  sfor<0, 3>(W4);
  sfor<9 + NE, 9 + NE + NB>(L4);
  wvm<12 + 4 * NB>();
  features(wp, qf, h != 0, ph);
  sfor<0, 4>([&](auto I) { tie(ph[I.v]); });
  bar();
  sfor<3, 6>(W4);
  sfor<9 + NE + NB, 18>(L4);
  wvm<24>();
  bar();
  publish(sWS, tg, FL_3A + g.g4 * 14 + g.s14, l0);
  poll(fl, 2, FL_KV + kh16 * 2 + lane, tag);
  {
    u32x4 kv[8];
    kv_loads(kv, kh16, sWS, lane * 16);
    wvm<0>();
    o_store(kv, ph, h != 0, sWS, lane * 16, OFF_O + ((4 * kg + w) * 32 + 2 * kh16) * 1024);
  }
  const unsigned f3 = (g.tt >> 1) * 14 + (7 * w >> 1) + (lane & 3);
  poll(fl, 4, FL_3A + f3, tag);
  bar();
  publish(sWS, tg, FL_O + kg * 16 + kh16, l0);
  bar();
  publish(sWS, tg, FL_3B + g.g4 * 14 + g.s14, l0);
  sfor<6, 9>(W4);
  auto S4 = [&](auto J) { sfor<0, 4>([&](auto I) { tie(w4r[J.v][I.v]); ldsw<u32x4>(p4_slot(w, J.v) + I.v * 1024 + lane * 16, w4r[J.v][I.v]); }); };
  poll(fl, 8, lane < 4 ? FL_3B + f3 : FL_O + (g.tt >> 1) * 16 + 4 * w + (lane & 3), tag);
  bar();
  u32x4 x1[2];
  sfor<0, 2>([&](auto U) { ld16s<U.v * 1024>(x1[U.v], lane * 16, sWS, OFF_X1 + ((2 * g.tt + (w & 1)) * 28 + 4 * g.s7 + (w & 2)) * 1024); });
  sfor<0, 4>(S4);
  wvm2<0>(x1[0], x1[1]);
  bar(); sfor<4, 8>(S4);
  bar(); S4(IC<8>{});
  bar();
  __builtin_amdgcn_s_setprio(3);
  sfor<0, 4>([&](auto A) {
    f32x4 b = ldsr<f32x4>(p4_slot(w, 0) + A.v * 1024 + lane * 16);
    f32x4 o[3];
    sfor<0, 3>([&](auto P) { o[P.v] = ldsr<f32x4>(R1 + (w ^ (P.v + 1)) * 12288 + (4 * P.v + A.v) * 1024 + lane * 16); });
    const unsigned y0 = x1[A.v >> 1][2 * (A.v & 1)], y1 = x1[A.v >> 1][2 * (A.v & 1) + 1];
    const float xx[4] = {bflo(y0), bfhi(y0), bflo(y1), bfhi(y1)};
    sfor<0, 4>([&](auto I) {
      float t = b[I.v] + o[0][I.v], u = o[1][I.v] + o[2][I.v];
      tie(t); tie(u);
      t = (t + xx[I.v]) + u;
      b[I.v] = t * __builtin_amdgcn_rcpf(1.f + __builtin_amdgcn_exp2f(-LOG2E * t));
    });
    ldsw<f32x4>(p4_slot(w, 1) + A.v * 1024 + lane * 16, b);
  });
  bar();
}

DI void compute_path(const Params& p, unsigned& dummy, const int w, const int xc, const int r STARG) {
  const int tid = threadIdx.x, lane = tid & 63, t32 = lane & 31, h = lane >> 5, th = w & 1, kh = w >> 1;
  const Geo g(xc, r);
  const srd_t sWS = mk(p.ws, WS_USED);
  __builtin_amdgcn_s_setprio(2);
  unsigned dm[7] = {0, 0, 0, 0, 0, 0, 0};
  {
    const unsigned i1 = ((xc * 28 + r) * 4 + w) * 30 + lane;
    const bool o1 = lane < 30;
    ld4p(dm[4], o1 && i1 < 14336 ? i1 * 128 : OOB, mk(p.x, 2048 * 896));
    ld4p(dm[5], o1 && i1 - 14336 < 3136 ? (i1 - 14336) * 128 : OOB, mk(p.w1, 448 * 896));
    ld4p(dm[6], o1 && i1 - 17472 < 9408 ? (i1 - 17472) * 128 : OOB, mk(p.wqkv, 448 * 2688));
  }
  auto later = [&]() {
    const unsigned id = ((xc * 28 + r) * 4 + w) * 35 + lane;
    const bool on = lane < 35;
    ld4p(dm[0], on && id < 3136 ? id * 128 : OOB, mk(p.w2, 448 * 896));
    ld4p(dm[1], on && id - 3136 < 12544 ? (id - 3136) * 128 : OOB, mk(p.w3, 448 * 3584));
    ld4p(dm[2], on && id - 15680 < 12544 ? (id - 15680) * 128 : OOB, mk(p.w4, 1792 * 896));
    ld4p(dm[3], on && id - 28224 < 3136 ? (id - 28224) * 128 : OOB, mk(p.wo, 448 * 896));
  };
  later();
  u32x2 ev;
  ld8s<0>(ev, 0u, sWS, OFF_FLG);
  const unsigned T32 = 4 * g.g4 + w, tc = 2 * g.tt + th;
  const int ftok = tc * 32 + t32;
  const unsigned x1o = OFF_X1 + (tc * 28 + 4 * g.s7 + 2 * kh) * 1024, st1o = OFF_ST1 + (2 * g.s7 + kh) * 2048 * 8;
  st16s(u32x4{POISON, POISON, POISON, POISON}, sWS, lane * 16, x1o); st16s<1024>(u32x4{POISON, POISON, POISON, POISON}, sWS, lane * 16, x1o);
  st8s(u32x2{POISON, POISON}, sWS, h == 0 ? ftok * 8 : OOB, st1o);
  u32x4 xv[2], x1p[2], gr;
  f32x16 xn;
  const int q = (lane & 15) >> 2, pp = lane & 3, blk = (lane >> 4) & 1;
  unsigned aperm = (4 * h + q) * 128 + (((2 * blk + (pp >> 1)) ^ ((q >> 1) << 2)) << 4) + (pp & 1) * 8, l16 = lane * 16;
  auto tra = [&](int hs, unsigned base, unsigned x) {
    tie(aperm);
    unsigned a = base + (aperm ^ x);
    if (hs == 8) a += l16 & 512;
    tie(a);
    return a;
  };
  auto lnv = [&](unsigned base) { tie(l16); unsigned a = base + l16; tie(a); return a; };
  u32x4 xf[28];
  unsigned xr[4], a0 = tra(8, RA, 0), a1 = tra(8, RA, 64);
  sfor<0, 4>([&](auto I) { xr[I.v] = R2 - 16384 + w * 4096 + t32 * 128 + (((2 * I.v + h) ^ ((t32 >> 1) & 7)) << 4); tie(xr[I.v]); });
  f32x16 c0, c1, c2, c3;
  pass_a<512, SlotP1>(c0, c1, a0, a1, xr, xf);
  bar();
  settle(c0, c1);
  f32x4 cst[8];
  const GeluK gk1 = ldsr<f32x4>(R2 + 2048 + w * 1024 + lane * 16);
  const float mean1 = gk1[3], r1 = ldsr<float>(R2 + 6144 + w * 256 + lane * 4);
  auto cs_load = [&](unsigned a) {
    sfor<0, 8>([&](auto A) { cst[A.v] = ldsr<f32x4>(a + A.v * 32 + h * 16); });
    sfor<0, 8>([&](auto A) { sfor<0, 4>([&](auto I) { (A.v < 4 ? c2 : c3)[4 * (A.v & 3) + I.v] = cst[A.v][I.v]; }); });
  };
  cs_load(R2 + 1024);
  sfor<0, 8>([&](auto A) { const f32x4 t = ldsr<f32x4>(R2 + 1280 + A.v * 32 + h * 16); sfor<0, 4>([&](auto I) { (A.v < 4 ? c2 : c3)[4 * (A.v & 3) + I.v] += t[I.v]; }); });
  wvm<0>(ev);
  tie(dummy); tie(dm[0]); tie(dm[1]); tie(dm[2]); tie(dm[3]); tie(dm[4]); tie(dm[5]); tie(dm[6]);
  const u64 tag = make_tag(ev);
  const u32x2 tg = {(unsigned)tag, (unsigned)(tag >> 32)};
  auto epi = [&](auto ACT, unsigned so) {
    unsigned pk[4];
    sfor<0, 16>([&](auto J) {
      constexpr int e = 2 * (J.v & 7);
      const f32x16 &c = J.v < 8 ? c0 : c1, &s = J.v < 8 ? c2 : c3;
      f32x2 z = f32x2{c[e], c[e + 1]} - f32x2{s[e], s[e + 1]} * mean1;
      if constexpr (ACT.v) z = gelu2(z, gk1); else z *= r1;
      pk[J.v & 3] = pk2(z[0], z[1]);
      if constexpr ((J.v & 3) == 3) st16s<(J.v >> 2) * 1024>(u32x4{pk[0], pk[1], pk[2], pk[3]}, sWS, l16, so);
    });
  };
  if (g.wtype) epi(IC<1>{}, OFF_H + (T32 * 28 + 4 * g.s7) * 1024);
  else epi(IC<0>{}, OFF_QKV + (T32 * 84 + 28 + 4 * g.s7) * 1024);
  ld16s<0>(gr, ftok * 16, sWS, OFF_ST0);
  bar();
  ST(3);
  {
    u32x4 wf[3][2];
    float m0, r0;
    auto rd = [&](auto UU, auto NN) {
      const unsigned ad = (NN.v ? a1 : a0) + UU.v * 2048;
      wf[UU.v % 3][NN.v] = trfrag(ad, ad + 512);
    };
    a0 = tra(8, R1, 0); a1 = tra(8, R1, 64);
    rd(IC<0>{}, IC<0>{}); rd(IC<0>{}, IC<1>{}); rd(IC<1>{}, IC<0>{}); rd(IC<1>{}, IC<1>{});
    sfor<0, 28>([&](auto UU) {
      constexpr int U = UU.v;
      mmx<U == 0>(c0, wf[U % 3][0], xf[U]);
      if constexpr (U == UH) {
        wvm<0>(gr);
        bar();
        granule(gr, sWS, ftok * 16, tg);
      }
      if constexpr (U + 2 < 28) rd(IC<U + 2>{}, IC<0>{});
      if constexpr (U >= 2 && U < 6) {
        constexpr int A = U - 2;
        const u32x2 t = ldsr<u32x2>(XT + (th * 32 + t32) * 128 + (((4 * kh + A) ^ (t32 & 7)) << 4) + h * 8);
        xv[A >> 1][2 * (A & 1)] = t.x; xv[A >> 1][2 * (A & 1) + 1] = t.y;
      }
      SB();
      mmx<U == 0>(c1, wf[U % 3][1], xf[U]);
      if constexpr (U + 2 < 28) rd(IC<U + 2>{}, IC<1>{});
      if constexpr (U == UH + 1) { r0 = __builtin_amdgcn_rsqf(u2f(gr[2])); m0 = -u2f(gr[0]) * r0; tie(r0); tie(m0); }
      if constexpr (U >= 20) {
        constexpr int I = U - 20;
        float a = fmaf(bflo(xv[I >> 2][I & 3]), r0, m0), b = fmaf(bfhi(xv[I >> 2][I & 3]), r0, m0);
        tie(a); tie(b);
        xn[2 * I] = a; xn[2 * I + 1] = b;
      }
      SB();
    });
  }
  ST(4);
  bar();
  {
    const srd_t sH = mk(p.ws + fresh(OFF_H), 64 * 28 * 1024);
    const int tb = ftok + 1 + fresh(kh);
    const unsigned ob = (unsigned)((tb >> 5) * 28 * 1024 + ((tb & 31) + 32 * h) * 16);
    sfor<10, 14>([&](auto U) { ld16s<(U.v - 10) * 1024>(xf[U.v], ob, sH, (fresh(kh) * 14 + 10) * 1024); });
  }
  cs_load(XT);
  settle(c0, c1);
  epi(IC<0>{}, OFF_QKV + (fresh(T32) * 84 + (g.wtype ? 0 : 56) + 4 * g.s7) * 1024);
  ST(5);
  c0 = xn;
  {
    a0 = tra(4, RA + fresh(kh) * 14 * 2048, fresh(kh) * 64); a1 = a0 ^ 64u; tie(a1);
    const unsigned hb = lnv(HREG + fresh(w) * 10240);
    tie(c0);
  bar();
    u32x4 wf[3][2];
    auto rd = [&](auto UU) {
      if constexpr (UU.v < 10) xf[UU.v] = ldsr<u32x4>(hb + UU.v * 1024);
      wf[UU.v % 3][0] = trfrag(a0 + UU.v * 2048, a0 + UU.v * 2048 + 1024); wf[UU.v % 3][1] = trfrag(a1 + UU.v * 2048, a1 + UU.v * 2048 + 1024);
    };
    rd(IC<0>{}); rd(IC<1>{});
    sfor<0, 14>([&](auto UU) {
      constexpr int U = UU.v;
      if constexpr (U == 10) wvm4<4>(xf[10], xf[11], xf[12], xf[13]);
      mma(c0, wf[U % 3][0], xf[U]);
      if constexpr (U + 2 < 14) rd(IC<U + 2>{});
      mmx<U == 0>(c1, wf[U % 3][1], xf[U]);
      SB();
    });
    settle(c0, c1);
    sfor<0, 4>([&](auto A) { ldsw<f32x4>(hb + A.v * 1024, quad(c1, A.v)); });
  }
  wvm<0>();
  bar();
  ST(27);
  {
    f32x4 o[4];
    const unsigned pb = lnv(HREG + (fresh(w) ^ 2) * 10240);
    sfor<0, 4>([&](auto A) { o[A.v] = ldsr<f32x4>(pb + A.v * 1024); });
    sfor<0, 4>([&](auto A) {
      const f32x4 t = quad(c0, A.v) + o[A.v];
      x1p[A.v >> 1][2 * (A.v & 1)] = pk2(t[0], t[1]);
      x1p[A.v >> 1][2 * (A.v & 1) + 1] = pk2(t[2], t[3]);
    });
    sfor<0, 8>([&](auto I) { x1p[I.v >> 2][I.v & 3] = min(x1p[I.v >> 2][I.v & 3], POISON - 1); });
    st16s(x1p[0], sWS, l16, x1o); st16s<1024>(x1p[1], sWS, l16, x1o);
    float a1 = 0.f, a2 = 0.f;
    sfor<0, 8>([&](auto I) { a1 = dot2(x1p[I.v >> 2][I.v & 3], ONE2, a1); a2 = dot2(x1p[I.v >> 2][I.v & 3], x1p[I.v >> 2][I.v & 3], a2); });
    st8s(u32x2{min(f2u(swap_add(a1)), POISON - 1), min(f2u(swap_add(a2)), POISON - 1)}, sWS, h == 0 ? ftok * 8 : OOB, st1o);
  }
  ST(7);
  bar();
  ST(8);
  GeluK gk3;
  float mean3;
  u32x2 cs3;
  {
    a0 = tra(4, R1, 0); a1 = tra(4, R1, 64);
    const unsigned spo = (T32 * 32 + t32 + h * 7 * 2048) * 8;
    ld8s<0>(cs3, lane * 8, sWS, OFF_CS3 + g.s14 * 512);
#pragma unroll 1
    for (unsigned spins = 0; spins < SPIN_CAP; ++spins) {
      unsigned v = 0;
      if (lane < 14) v = __hip_atomic_load((GAS unsigned*)(p.ws + OFF_ST1 + (lane * 2048 + T32 * 32 + 31) * 8), __ATOMIC_RELAXED, __HIP_MEMORY_SCOPE_AGENT);
      if (__all(v != POISON)) break;
      __builtin_amdgcn_s_sleep(2);
    }
    wvm<0>(cs3);
    ST(11);
    const unsigned xb = lnv(OFF_X1 + fresh(T32) * 28 * 1024);
    auto X1 = [&](auto UU) { ld16s<(UU.v & 3) * 1024>(xf[UU.v], xb, sWS, (UU.v / 4) * 4096); };
    u32x2 sp[7];
    auto SP = [&]() { sfor<0, 7>([&](auto I) { ld8s<0>(sp[I.v], spo, sWS, OFF_ST1 + I.v * 2048 * 8); }); };
    sfor<0, 12>(X1);
    u32x4 wf[3][2];
    auto rd = [&](auto UU, auto NN) { const unsigned ad = (NN.v ? a1 : a0) + UU.v * 2048; wf[UU.v % 3][NN.v] = trfrag(ad, ad + 1024); };
    auto check = [&](auto CC, auto NN) {
      constexpr int U = 4 * CC.v;
      wvm4<NN.v>(xf[U], xf[U + 1], xf[U + 2], xf[U + 3]);
#pragma unroll 1
      for (unsigned spins = 0; spins < SPIN_CAP; ++spins) {
        unsigned m = 0;
        sfor<0, 4>([&](auto I) { m = max(max(m, max(xf[U + I.v][0], xf[U + I.v][1])), max(xf[U + I.v][2], xf[U + I.v][3])); });
        if (__builtin_expect(__all(m != POISON), 1)) break;
        sfor<U, U + 4>(X1);
        wvm4<0>(xf[U], xf[U + 1], xf[U + 2], xf[U + 3]);
      }
    };
    rd(IC<0>{}, IC<0>{}); rd(IC<0>{}, IC<1>{}); rd(IC<1>{}, IC<0>{}); rd(IC<1>{}, IC<1>{});
    check(IC<0>{}, IC<8>{});
    sfor<0, 28>([&](auto UU) {
      constexpr int U = UU.v;
      mmx<U == 0>(c0, wf[U % 3][0], xf[U]);
      if constexpr (U + 2 < 28) rd(IC<U + 2>{}, IC<0>{});
      if constexpr (U + 12 < 28) X1(IC<U + 12>{});
      if constexpr (U == 16) SP();
      SB();
      mmx<U == 0>(c1, wf[U % 3][1], xf[U]);
      if constexpr (U + 2 < 28) rd(IC<U + 2>{}, IC<1>{});
      if constexpr (U % 4 == 3 && U < 27) check(IC<U / 4 + 1>{}, IC<(U < 16 ? 8 : U == 19 ? 11 : 7)>{});
      SB();
    });
    wvm<0>();
#pragma unroll 1
    for (unsigned spins = 0; spins < SPIN_CAP; ++spins) {
      unsigned m = 0;
      sfor<0, 7>([&](auto I) { tie(sp[I.v]); m = max(m, max(sp[I.v].x, sp[I.v].y)); });
      if (__builtin_expect(__all(m != POISON), 1)) break;
      SP();
      wvm<0>();
    }
    float sm = 0.f, sq = 0.f;
    sfor<0, 7>([&](auto I) { tie(sp[I.v]); sm += u2f(sp[I.v].x); sq += u2f(sp[I.v].y); });
    mean3 = swap_add(sm) * (1.f / D);
    const float ve = fmaxf(swap_add(sq) * (1.f / D) - mean3 * mean3, 0.f) + 1e-5f;
    gk3 = gelu_k(__builtin_amdgcn_rsqf(ve), __builtin_amdgcn_sqrtf(ve));
    tie(gk3); tie(mean3);
  }
  bar();
  ST(13);
  ldsw<u32x2>(R1 + 49152 + fresh(w) * 512 + lane * 8, cs3);
  unsigned csb = R1 + 49152 + fresh(w) * 512 + h * 16;
  tie(csb);
  auto CS3 = [&](int t) { sfor<0, 8>([&](auto A) { cst[A.v] = ldsr<f32x4>(csb + t * 128 + A.v * 32); }); };
  const unsigned so3 = OFF_H3 + (fresh(T32) * 112 + 8 * g.s14) * 1024;
  unsigned pk[4];
  auto G = [&](auto JJ) {
    constexpr int j = JJ.v, t = j >> 3, e = 2 * (j & 7);
    const f32x16& c = t == 0 ? c0 : t == 1 ? c1 : t == 2 ? c2 : c3;
    const f32x2 z = gelu2(f32x2{c[e], c[e + 1]} - f32x2{cst[4 * (t & 1) + (e >> 2)][e & 3], cst[4 * (t & 1) + (e >> 2)][(e & 3) + 1]} * mean3, gk3);
    pk[j & 3] = pk2(z[0], z[1]);
    if constexpr ((j & 3) == 3) st16s<(j >> 2 & 3) * 1024>(u32x4{pk[0], pk[1], pk[2], pk[3]}, sWS, l16, so3 + (j >> 4) * 4096);
  };
  CS3(0);
  settle(c0, c1);
  sfor<0, 16>([&](auto J) { G(J); });
  ST(24);
  {
    u32x4 wf[4];
    a0 = tra(4, RA, 0); a1 = tra(4, RA, 64);
    auto rd = [&](auto MM) { const unsigned ad = (MM.v < 28 ? a0 : a1) + (MM.v % 28) * 2048; wf[MM.v & 3] = trfrag(ad, ad + 1024); };
    rd(IC<0>{}); rd(IC<1>{}); rd(IC<2>{});
    sfor<0, 56>([&](auto MM) {
      constexpr int M = MM.v;
      mmx<M % 28 == 0>(M < 28 ? c2 : c3, wf[M & 3], xf[M % 28]);
      if constexpr (M + 3 < 56) rd(IC<M + 3>{});
      if constexpr (M == M3) { wvm<0>(); asm volatile("s_barrier" ::: "memory"); }
      if constexpr (M == 50) CS3(2);
      SB();
    });
  }
  ST(14);
  settle(c2, c3);
  sfor<16, 24>([&](auto J) { G(J); });
  ST(15);
  u32x4 af[4][4];
  unsigned av[2] = {lnv(OFF_H3 + fresh(tc) * 112 * 1024), lnv(OFF_H3 + (fresh(tc) ^ 1) * 112 * 1024)}, as;
  auto ACT1 = [&](auto SS, auto II) {
    constexpr int s = SS.v;
    if constexpr (s == 14 && II.v == 0) { av[0] = lnv(OFF_O + fresh(tc) * 32 * 1024); av[1] = lnv(OFF_O + (fresh(tc) ^ 1) * 32 * 1024); }
    if constexpr (II.v == 0) as = s < 14 ? p4_k32(fresh(w), s) * 2048 : (4 * fresh(w) + s - 14) * 2048;
    ld16s<(II.v & 1) * 1024>(af[s & 3][II.v], av[II.v >> 1], sWS, as);
  };
  unsigned bs[3][2];
  sfor<0, 3>([&](auto I) { bs[I.v][0] = tra(4, p4_slot(fresh(w), 3 * I.v), fresh(kh) * 64); bs[I.v][1] = bs[I.v][0] ^ 64u; tie(bs[I.v][1]); });
  u32x4 wf[2][4];
  auto RW = [&](auto SS, auto I) {
    constexpr int j = SS.v % 9;
    const unsigned aa = bs[j / 3][I.v & 1] + (j % 3) * 4096 + (I.v >> 1) * 2048;
    wf[SS.v & 1][I.v] = trfrag(aa, aa + 1024);
  };
  bar();
  sfor<0, 8>([&](auto K) { ACT1(IC<K.v / 4>{}, IC<K.v % 4>{}); });
  tie(c3);
  sfor<24, 32>([&](auto J) { G(J); });
  sfor<8, 12>([&](auto K) { ACT1(IC<K.v / 4>{}, IC<K.v % 4>{}); });
  ST(16);
  sfor<0, 4>([&](auto I) { RW(IC<0>{}, I); });
  f32x16 d0, d1, d2, d3;
  const unsigned exw = lnv(R1 + fresh(w) * 12288), own = lnv(p4_slot(fresh(w), 0));
  sfor<0, 18>([&](auto SS) {
    constexpr int s = SS.v;
    if constexpr (s == 4 || s == 8 || s == 11 || s == 15) bar();
    if constexpr (s % 3 == 0) ST(17 + s / 3);
    wvm4<(s == 0 ? 10 : s <= 15 ? 8 : 4 * (17 - s))>(af[s & 3][0], af[s & 3][1], af[s & 3][2], af[s & 3][3]);
    if constexpr (s == 1) asm volatile("s_barrier" ::: "memory");
    f32x16 &a0 = s < 14 ? c0 : d0, &a1 = s < 14 ? c1 : d1, &a2 = s < 14 ? c2 : d2, &a3 = s < 14 ? c3 : d3;
    auto side = [&](auto KK) {
      constexpr int k = 4 * s + KK.v;
      if constexpr (s + 3 <= 17) ACT1(IC<s + 3>{}, KK);
      if constexpr (k == 56) { settle(c0, c1); settle(c2, c3); }
      if constexpr (k >= 56 && k < 60) {
        sfor<0, 3>([&](auto J) { constexpr int A = 3 * (k - 56) + J.v; ldsw<f32x4>(exw + A * 1024, quad(A < 4 ? c1 : A < 8 ? c2 : c3, A & 3)); });
        ldsw<f32x4>(own + (k - 56) * 1024, quad(c0, k - 56));
      }
    };
    sfor<0, 2>([&](auto U) {
      mmx<s % 14 == 0 && U.v == 0>(a0, wf[s & 1][2 * U.v], af[s & 3][U.v]);
      if constexpr (s + 1 < 18) RW(IC<s + 1>{}, IC<2 * U.v>{});
      mmx<s % 14 == 0 && U.v == 0>(a1, wf[s & 1][2 * U.v], af[s & 3][2 + U.v]);
      side(IC<2 * U.v>{});
      SB();
      mmx<s % 14 == 0 && U.v == 0>(a2, wf[s & 1][2 * U.v + 1], af[s & 3][U.v]);
      if constexpr (s + 1 < 18) RW(IC<s + 1>{}, IC<2 * U.v + 1>{});
      mmx<s % 14 == 0 && U.v == 0>(a3, wf[s & 1][2 * U.v + 1], af[s & 3][2 + U.v]);
      side(IC<2 * U.v + 1>{});
      SB();
    });
  });
  ST(23);
  settle(d0, d1); tie(d2); tie(d3);
  const unsigned e3[3] = {lnv(RA + (fresh(w) ^ 1) * 12288), lnv(RA + (fresh(w) ^ 2) * 12288), lnv(RA + (fresh(w) ^ 3) * 12288)};
  unsigned oo = ftok * 896 + h * 16 + fresh(g.s7) * 128 + kh * 64;
  tie(oo);
  sfor<0, 12>([&](auto A) { ldsw<f32x4>(exw - R1 + A.v * 1024, quad(A.v < 4 ? d1 : A.v < 8 ? d2 : d3, A.v & 3)); });
  bar();
  sfor<0, 12>([&](auto A) {
    const f32x4 v = ldsr<f32x4>(e3[A.v / 4] + A.v * 1024);
    sfor<0, 4>([&](auto I) { d0[4 * (A.v & 3) + I.v] += v[I.v]; });
  });
  sfor<0, 4>([&](auto A) {
    const f32x4 v = ldsr<f32x4>(own + 4096 + A.v * 1024);
    sfor<0, 4>([&](auto I) { c0[4 * A.v + I.v] = v[I.v]; });
  });
  const srd_t sO = mk(p.out, 2048 * 896);
  sfor<0, 2>([&](auto U) {
    u32x4 ov;
    sfor<0, 4>([&](auto I) {
      constexpr int e = 8 * U.v + 2 * I.v;
      ov[I.v] = pk2(fmaf(d0[e], c0[e], bflo(xv[U.v][I.v])), fmaf(d0[e + 1], c0[e + 1], bfhi(xv[U.v][I.v])));
    });
    __builtin_amdgcn_raw_buffer_store_b128(cross(ov), sO, oo + U.v * 32, 0, 0);
  });
}

DI void spare_path(const Params& p, const int w, const int xc, const int r STARG) {
  const int tid = threadIdx.x, lane = tid & 63, t32 = lane & 31, h = lane >> 5, tb = w & 3, ch = w >> 2;
  constexpr unsigned WPHI = 160768, ZERO = 159744, DUMMY = 158720;
  const int rr = lane >> 3, cp = lane & 7, rs = r - 28;
  const srd_t sWS = mk(p.ws, WS_USED), sX = mk(p.x, 2048 * 896);
  GAS u64* fl = (GAS u64*)(p.ws + OFF_FLG);
  const unsigned srow = 512 * (xc >> 1) + 64 * ((xc & 1) * 4 + rs) + 8 * w + rr;
  u32x2 ev;
  ld8s<0>(ev, 0u, sWS, OFF_FLG);
  u32x4 xr7[7];
  sfor<0, 7>([&](auto K) { ld16p(xr7[K.v], srow * 896 + cp * 16, sX, K.v * 128); });
  const int hd = 4 * (xc >> 1) + rs, hf = xc & 1;
  wvm<0>(ev);
  const u64 tag = make_tag(ev);
  const u32x2 tg = {(unsigned)tag, (unsigned)(tag >> 32)};
  {
    float a1 = 0.f, a2 = 0.f;
    sfor<0, 7>([&](auto K) { tie(xr7[K.v]); sfor<0, 4>([&](auto I) { a1 = dot2(xr7[K.v][I.v], ONE2, a1); a2 = dot2(xr7[K.v][I.v], xr7[K.v][I.v], a2); }); });
    sfor<0, 3>([&](auto K) { a1 += __shfl_xor(a1, 1 << K.v); a2 += __shfl_xor(a2, 1 << K.v); });
    const float mean = a1 * (1.f / D), ve = fmaxf(a2 * (1.f / D) - mean * mean, 0.f) + 1e-5f;
    st16s(u32x4{f2u(mean), tg.x, f2u(ve), tg.y}, sWS, cp == 0 ? srow * 16 : OOB, OFF_ST0);
  }
  ST(1);
  const int bi = (xc >> 1) * 4 + rs, par = xc & 1;
  const int q = (lane & 15) >> 2, pp = lane & 3, blk = (lane >> 4) & 1;
  const unsigned acol = (unsigned)((((2 * blk + (pp >> 1)) ^ ((q >> 1) << 2)) << 4) + (pp & 1) * 8);
  const unsigned aperm = (4 * h + q) * 128 + acol;
  const srd_t sW3 = mk(p.w3, 448 * 3584);
  const unsigned wsw = (cp ^ (((rr >> 1) & 1) << 2)) << 4;
  {
    const srd_t s0 = bi < 7 ? mk(p.wqkv, 448 * 2688) : sW3, sP = mk(p.wphi, 16 * 1568);
    const unsigned pitch = bi < 7 ? 2688 : 3584, col = (14 * par + (bi < 7 ? bi : bi - 7)) * 128;
    sfor<0, 7>([&](auto K) { dma16(rr * pitch + wsw, s0, (8 * K.v + w) * 8 * pitch + col, RA + (8 * K.v + w) * 1024); });
    dma16(lane * 16, sP, hd * 1568 + (w & 1) * 1024, WPHI + (w & 1) * 1024);
  }
  if (bi >= 11) sfor<0, 7>([&](auto K) { dma16(rr * 3584 + wsw, sW3, (8 * K.v + w) * 8 * 3584 + (14 * par + bi - 2) * 128, R1 + (8 * K.v + w) * 1024); });
  const int oh = 12 + rs, og = 2 * xc + 1;
  dma16(lane * 16, mk(p.wphi, 16 * 1568), oh * 1568 + (w & 1) * 1024, WPHI - 4096 + (w & 1) * 1024);
  if (tid < 16) ldsw<unsigned>(ZERO + tid * 4, 0u);
  wvm<0>();
  bar();
  cs_write(R2 + w * 128, colsum<7>(RA + (aperm ^ (ch * 64)) + tb * 7 * 2048), lane);
  if (bi >= 11) cs_write(R2 + 1024 + w * 128, colsum<7>(R1 + (aperm ^ (ch * 64)) + tb * 7 * 2048), lane);
  bar();
  const int cb0 = bi < 7 ? 28 + 7 * par + bi : 14 * par + bi - 7, cb1 = 14 * par + bi - 2;
  if (tb == 0 || (tb == 1 && bi >= 11)) {
    const f32x16 cs = cs_read(R2 + tb * 1024 + ch * 512 + h * 16, 128);
    sfor<0, 4>([&](auto A) { st16s<A.v * 32>(__builtin_bit_cast(u32x4, quad(cs, A.v)), sWS, t32 == 0 ? h * 16 : OOB, OFF_CS3 + (tb ? cb1 : cb0) * 256 + ch * 128); });
    wvm<0>();
  }
  bar();
  publish(sWS, tg, FL_CS + (tid ? cb1 : cb0), tid == 0 || (tid == 1 && bi >= 11));
  ST(2);
  const int gq = 8 * hf + w;
  const unsigned qkvs = OFF_QKV + gq * 4 * 84 * 1024;
  const int b01 = (28 * hd + (lane & 1) * 27) >> 6;
  const unsigned img = w * 18432;
  u32x4 wp[3];
  phi_frags(wp, hd, WPHI, ZERO, lane);
  unsigned vw[6];
  sfor<0, 6>([&](auto A) {
    const int d0 = 8 * A.v + 4 * h - ((28 * hd) & 15);
    vw[A.v] = d0 >= 0 && d0 < 28 ? img + 16384 + t32 * 64 + (((d0 >> 2) ^ ((t32 >> 1) & 7)) << 3) : DUMMY + lane * 8;
  });
  u32x4 kp[4][3], vp[4][3];
  auto gather = [&](u32x4 (&d)[4][3], unsigned base) { sfor<0, 4>([&](auto T) { head_loads(d[T.v], hd, qkvs + (T.v * 84 + base) * 1024, sWS, lane * 16); }); };
  const unsigned fq = gq * 14 + b01;
  poll(fl, 2, FL_A + fq + 7, tag);
  ST(3);
  gather(kp, 28);
  if (h) ldsw<u32x2>(img + 16384 + t32 * 64 + ((7 ^ ((t32 >> 1) & 7)) << 3), u32x2{0x3f80u, 0u});
  sfor<0, 4>([&](auto T) {
    wvm<9 - 3 * T.v>();
    u32x4 ph[4];
    features(wp, kp[T.v], h != 0, ph);
    const unsigned pr = img + T.v * 4096 + t32 * 128, sw = h ^ (((t32 >> 1) & 1) << 3) ^ (t32 & 1) ^ (((t32 >> 2) & 3) << 1);
    sfor<0, 8>([&](auto A) { ldsw<u32x2>(pr + (((2 * A.v) ^ sw) << 3), u32x2{ph[A.v >> 1][2 * (A.v & 1)], ph[A.v >> 1][2 * (A.v & 1) + 1]}); });
  });
  ST(4);
  poll(fl, 4, (lane & 2) ? FL_Q + fq + 7 : FL_Q + og * 14 + ((28 * oh + (lane & 1) * 27) >> 6), tag);
  ST(5);
  gather(vp, 56);
  u32x4 qf[3];
  head_loads(qf, oh, OFF_QKV + (4 * og + tb) * 84 * 1024, sWS, lane * 16);
  f32x16 kvp = zero16(), kvm = zero16();
  unsigned aa[2], ba[2];
  sfor<0, 2>([&](auto T) {
    const int row = 8 * h + 4 * T.v + q, pc = 4 * blk + pp;
    aa[T.v] = img + row * 128 + ((pc ^ ((q >> 1) << 3) ^ (q & 1) ^ ((2 * h + T.v) << 1)) << 3);
    ba[T.v] = img + 16384 + row * 64 + ((pc ^ (row >> 1)) << 3);
  });
  sfor<0, 4>([&](auto T) {
    wvm<12 - 3 * T.v>();
    sfor<0, 3>([&](auto I) { tie(vp[T.v][I.v]); });
    sfor<0, 6>([&](auto A) { ldsw<u32x2>(vw[A.v], u32x2{vp[T.v][A.v >> 1][2 * (A.v & 1)], vp[T.v][A.v >> 1][2 * (A.v & 1) + 1]}); });
    sfor<0, 2>([&](auto U) {
      constexpr unsigned oa = T.v * 4096 + U.v * 2048;
      const u32x4 bf = trfrag(ba[0] + U.v * 1024, ba[1] + U.v * 1024);
      kvp = mfma(trfrag(aa[0] + oa, aa[1] + oa), bf, kvp);
      kvm = mfma(trfrag((aa[0] ^ 64u) + oa, (aa[1] ^ 64u) + oa), bf, kvm);
    });
  });
  sfor<0, 8>([&](auto A) { ldsw<f32x4>(img + A.v * 1024 + lane * 16, quad(A.v < 4 ? kvp : kvm, A.v & 3)); });
  bar();
  f32x4 own = {0.f, 0.f, 0.f, 0.f};
  sfor<0, 8>([&](auto S) { own += ldsr<f32x4>(S.v * 18432 + w * 1024 + lane * 16); });
  st8s(u32x2{pk2(own[0], own[1]), pk2(own[2], own[3])}, sWS, lane * 16 + (w & 1) * 8, OFF_KVP + ((hd * 2 + hf) * 4 + (w >> 1)) * 1024);
  wvm<0>();
  bar();
  publish(sWS, tg, FL_KV + hd * 2 + hf, tid == 0);
  ST(6);
  if (ch == 0) {
    u32x4 ph[4], kv[8];
    phi_frags(wp, oh, WPHI - 4096, ZERO, lane);
    features(wp, qf, h != 0, ph);
    poll(fl, 2, FL_KV + oh * 2 + (lane & 1), tag);
    kv_loads(kv, oh, sWS, lane * 16);
    wvm<0>();
    o_store(kv, ph, h != 0, sWS, lane * 16, OFF_O + ((4 * og + tb) * 32 + 2 * oh) * 1024);
    wvm<0>();
  }
  bar();
  publish(sWS, tg, FL_O + og * 16 + oh, tid == 0);
  ST(8);
  if (xc == 0 && rs == 0 && w == 0) {
    sfor<0, 4>([&](auto I) { poll(fl, 56, FL_Q + 56 * I.v + lane, tag); });
    publish(sWS, tg, FL_E, lane == 0);
  }
}

__global__ __launch_bounds__(512, 2) void fused(Params p) {
  u64 pc;
  asm volatile("s_getpc_b64 %0" : "=s"(pc));
  ST(0);
  int w = __builtin_amdgcn_readfirstlane(threadIdx.x >> 6);
  asm volatile("s_nop 4" : "+s"(w));
  const int xc = blockIdx.x & 7, r = blockIdx.x >> 3;
  unsigned dummy = 0;
  if (r < 28 && w >= 4) first_loads(p, w - 4, xc, r);
  if (r < 28 && w < 4) code_prefetch(dummy, pc, (((r & 1) * 4 + w) * 64 + (threadIdx.x & 63)) * 128);
  SB();
  if (r < 28) {
    if (w < 4) compute_path(p, dummy, w, xc, r STPASS);
    else loader_path(p, w - 4, xc, r STPASS);
  }
  else spare_path(p, w, xc, r STPASS);
}

extern "C" void kernel_launch(void* const* d_in, const int* in_sizes, int n_in, void* d_out, int out_size, void* d_ws, size_t ws_size,
                              hipStream_t stream) {
  Params p;
  p.x = (const uint16_t*)d_in[0]; p.wqkv = (const uint16_t*)d_in[1]; p.wphi = (const uint16_t*)d_in[2]; p.wo = (const uint16_t*)d_in[3];
  p.w1 = (const uint16_t*)d_in[5]; p.w2 = (const uint16_t*)d_in[6]; p.w3 = (const uint16_t*)d_in[7]; p.w4 = (const uint16_t*)d_in[8];
  p.out = (uint16_t*)d_out; p.ws = (char*)d_ws + ((ws_size - WS_USED) & ~(size_t)0xFFFFF);
  (void)hipFuncSetAttribute((const void*)fused, hipFuncAttributeMaxDynamicSharedMemorySize, LDS_BYTES);
  fused<<<256, 512, LDS_BYTES, stream>>>(p);
}
```

```cpp
#include <hip/hip_runtime.h>
#include <stdint.h>

#define LAS __attribute__((address_space(3)))
#define GAS __attribute__((address_space(1)))
#define DI __device__ __forceinline__
typedef short s16x4 __attribute__((ext_vector_type(4)));
typedef __bf16 bf16x8 __attribute__((ext_vector_type(8)));
typedef __bf16 bf16x2 __attribute__((ext_vector_type(2)));
typedef float f32x16 __attribute__((ext_vector_type(16)));
typedef float f32x4 __attribute__((ext_vector_type(4)));
typedef float f32x2 __attribute__((ext_vector_type(2)));
typedef unsigned u32x4 __attribute__((ext_vector_type(4)));
typedef unsigned u32x2 __attribute__((ext_vector_type(2)));
typedef unsigned long long u64;
typedef __amdgpu_buffer_rsrc_t srd_t;

constexpr int D = 448;
constexpr float LOG2E = 1.4426950408889634f;
constexpr unsigned OFF_FLG = 0;
constexpr unsigned OFF_H = 262144;
constexpr unsigned OFF_X1 = OFF_H + 64 * 28 * 1024;
constexpr unsigned OFF_QKV = OFF_X1 + 64 * 28 * 1024;
constexpr unsigned OFF_H3 = OFF_QKV + 64 * 84 * 1024;
constexpr unsigned OFF_O = OFF_H3 + 64 * 112 * 1024;
constexpr unsigned OFF_KVP = OFF_O + 64 * 32 * 1024;
constexpr unsigned OFF_ST0 = OFF_KVP + 32 * 8192;
constexpr unsigned OFF_ST1 = OFF_ST0 + 2048 * 16;
constexpr unsigned OFF_CS3 = OFF_ST1 + 14 * 2048 * 8;
constexpr unsigned WS_USED = OFF_CS3 + 42 * 256;
constexpr size_t OFF_STAMP = 64u << 20;
constexpr int FL_E = 0, FL_A = 1, FL_Q = FL_A + 16 * 14, FL_3A = FL_Q + 16 * 14, FL_3B = FL_3A + 16 * 14,
              FL_O = FL_3B + 16 * 14, FL_KV = FL_O + 256, FL_CS = FL_KV + 32;
static_assert((FL_CS + 42) * 128 <= 262144, "flag block");
constexpr unsigned RA = 0, R1 = 57344, R2 = 114688, LDS_BYTES = 163840;
constexpr unsigned OOB = 0x80000000u;
constexpr unsigned CODE_BYTES = 48128;
constexpr unsigned SPIN_CAP = 1u << 18;

struct Params {
  const uint16_t *x, *wqkv, *wphi, *wo, *w1, *w2, *w3, *w4;
  uint16_t* out;
  char* ws;
};

template <int I> struct IC { static constexpr int v = I; };
template <int B, int E, class F> DI void sfor(F&& f) {
  if constexpr (B < E) { f(IC<B>{}); sfor<B + 1, E>(f); }
}
DI f32x16 mfma(u32x4 a, u32x4 b, f32x16 c) {
  return __builtin_amdgcn_mfma_f32_32x32x16_bf16(__builtin_bit_cast(bf16x8, a), __builtin_bit_cast(bf16x8, b), c, 0, 0, 0);
}
DI void mma(f32x16& c, u32x4 a, u32x4 b) { asm volatile("v_mfma_f32_32x32x16_bf16 %0, %1, %2, %0" : "+v"(c) : "v"(a), "v"(b) : "memory"); }
DI void mma0(f32x16& c, u32x4 a, u32x4 b) { asm volatile("v_mfma_f32_32x32x16_bf16 %0, %1, %2, 0" : "=&v"(c) : "v"(a), "v"(b) : "memory"); }
template <bool FIRST> DI void mmx(f32x16& c, u32x4 a, u32x4 b) { if constexpr (FIRST) mma0(c, a, b); else mma(c, a, b); }
DI void settle(f32x16& a, f32x16& b) { asm volatile("s_nop 11" : "+v"(a), "+v"(b)); }
DI unsigned pk2(float a, float b) {
  f32x2 v = {a, b};
  return __builtin_bit_cast(unsigned, __builtin_convertvector(v, bf16x2));
}
DI float u2f(unsigned u) { return __builtin_bit_cast(float, u); }
DI unsigned f2u(float f) { return __builtin_bit_cast(unsigned, f); }
DI float bflo(unsigned u) { return __builtin_bit_cast(float, u << 16); }
DI float bfhi(unsigned u) { return __builtin_bit_cast(float, u & 0xffff0000u); }
DI u32x4 trfrag(unsigned a0, unsigned a1) {
  u32x2 lo = __builtin_bit_cast(u32x2, __builtin_amdgcn_ds_read_tr16_b64_v4i16((LAS s16x4*)(uintptr_t)a0));
  u32x2 hi = __builtin_bit_cast(u32x2, __builtin_amdgcn_ds_read_tr16_b64_v4i16((LAS s16x4*)(uintptr_t)a1));
  return u32x4{lo.x, lo.y, hi.x, hi.y};
}
template <class T> DI T ldsr(unsigned a) { return *(LAS T*)(uintptr_t)a; }
template <class T> DI void ldsw(unsigned a, T v) { *(LAS T*)(uintptr_t)a = v; }
DI srd_t mk(const void* p, unsigned bytes) { return __builtin_amdgcn_make_buffer_rsrc((void*)p, 0, (int)bytes, 0x00020000); }
DI void dma16(unsigned voff, srd_t r, unsigned soff, unsigned lds) {
  asm volatile("s_mov_b32 m0, %3\n\ts_nop 0\n\tbuffer_load_dwordx4 %0, %1, %2 offen lds" ::"v"(voff), "s"(r), "s"(soff), "s"(lds) : "memory");
}
DI void dma16s(unsigned voff, srd_t r, unsigned soff, unsigned lds) {
  asm volatile("s_mov_b32 m0, %3\n\ts_nop 0\n\tbuffer_load_dwordx4 %0, %1, %2 offen sc1 lds" ::"v"(voff), "s"(r), "s"(soff), "s"(lds) : "memory");
}
DI void ld4p(unsigned& d, unsigned voff, srd_t r) {
  asm volatile("buffer_load_dword %0, %1, %2, 0 offen" : "=v"(d) : "v"(voff), "s"(r) : "memory");
}
DI void ld16p(u32x4& d, unsigned voff, srd_t r, unsigned soff) {
  asm volatile("buffer_load_dwordx4 %0, %1, %2, %3 offen" : "=v"(d) : "v"(voff), "s"(r), "s"(soff) : "memory");
}
template <int IMM> DI void ld16s(u32x4& d, unsigned voff, srd_t r, unsigned soff) {
  asm volatile("buffer_load_dwordx4 %0, %1, %2, %3 offen offset:%c4 sc1" : "=v"(d) : "v"(voff), "s"(r), "s"(soff), "i"(IMM) : "memory");
}
template <int IMM> DI void ld8s(u32x2& d, unsigned voff, srd_t r, unsigned soff) {
  asm volatile("buffer_load_dwordx2 %0, %1, %2, %3 offen offset:%c4 sc1" : "=v"(d) : "v"(voff), "s"(r), "s"(soff), "i"(IMM) : "memory");
}
template <int IMM = 0> DI void st16s(u32x4 v, srd_t r, unsigned voff, unsigned soff) {
  asm volatile("buffer_store_dwordx4 %0, %1, %2, %3 offen offset:%c4 sc1\n\ts_nop 1" ::"v"(v), "v"(voff), "s"(r), "s"(soff), "i"(IMM) : "memory");
}
DI void st8s(u32x2 v, srd_t r, unsigned voff, unsigned soff) {
  asm volatile("buffer_store_dwordx2 %0, %1, %2, %3 offen sc1" ::"v"(v), "v"(voff), "s"(r), "s"(soff) : "memory");
}
template <int N> DI void wvm() { asm volatile("s_waitcnt vmcnt(%c0)" ::"i"(N) : "memory"); }
template <class T> DI void tie(T& a) { asm volatile("" : "+v"(a)); }
template <int N, class T> DI void wvm(T& a) { asm volatile("s_waitcnt vmcnt(%c1)" : "+v"(a) : "i"(N) : "memory"); }
DI void wlg4(u32x4& a, u32x4& b, u32x4& c, u32x4& d) { asm volatile("s_waitcnt lgkmcnt(0)" : "+v"(a), "+v"(b), "+v"(c), "+v"(d)::"memory"); }
template <int N> DI void wvm4(u32x4& a, u32x4& b, u32x4& c, u32x4& d) {
  asm volatile("s_waitcnt vmcnt(%c4)" : "+v"(a), "+v"(b), "+v"(c), "+v"(d) : "i"(N) : "memory");
}
template <int N> DI void wvm2(u32x4& a, u32x4& b) { asm volatile("s_waitcnt vmcnt(%c2)" : "+v"(a), "+v"(b) : "i"(N) : "memory"); }
DI int fresh(int v) { asm volatile("" : "+s"(v)); return v; }
DI void bar() { asm volatile("s_waitcnt lgkmcnt(0)\n\ts_barrier" ::: "memory"); }
#define SB() __builtin_amdgcn_sched_barrier(0)
DI void halves(float v, float& lo, float& hi) {
  auto r = __builtin_amdgcn_permlane32_swap(f2u(v), f2u(v), false, false);
  lo = u2f(r[0]); hi = u2f(r[1]);
}
DI float swap_add(float v) { float a, b; halves(v, a, b); return a + b; }
DI float swap_max(float v) { float a, b; halves(v, a, b); return fmaxf(a, b); }
DI float swap_hi(float v) { float a, b; halves(v, a, b); return b; }
typedef f32x4 GeluK;
DI GeluK gelu_k(float r, float ir) {
  const float c0 = -2.f * 0.7978845608028654f * LOG2E, c1 = c0 * 0.044715f;
  return GeluK{c0 * r, c1 * r * r * r, ir, 0.f};
}
DI f32x2 gelu2(f32x2 z, GeluK k) {
  const f32x2 u = z * (z * z * k[1] + k[0]);
  const f32x2 e = {__builtin_amdgcn_exp2f(u[0]), __builtin_amdgcn_exp2f(u[1])};
  const f32x2 d = e * k[2] + k[2];
  return z * f32x2{__builtin_amdgcn_rcpf(d[0]), __builtin_amdgcn_rcpf(d[1])};
}
DI float dot2(unsigned a, unsigned b, float c) {
  return __builtin_amdgcn_fdot2_f32_bf16(__builtin_bit_cast(bf16x2, a), __builtin_bit_cast(bf16x2, b), c, false);
}
constexpr unsigned ONE2 = 0x3f803f80u;
DI f32x16 zero16() { f32x16 z; for (int i = 0; i < 16; ++i) z[i] = 0.f; return z; }
DI u32x4 pack8(const f32x16& a, int u) {
  return u32x4{pk2(a[8 * u], a[8 * u + 1]), pk2(a[8 * u + 2], a[8 * u + 3]), pk2(a[8 * u + 4], a[8 * u + 5]), pk2(a[8 * u + 6], a[8 * u + 7])};
}
DI void poll(GAS u64* fl, int n, int idx, u64 tag) {
  const bool on = (int)(threadIdx.x & 63) < n;
#pragma unroll 1
  for (unsigned spins = 0; spins < SPIN_CAP; ++spins) {
    u64 v = tag;
    if (on) v = __hip_atomic_load(fl + idx * 16, __ATOMIC_RELAXED, __HIP_MEMORY_SCOPE_AGENT);
    if (__all(v == tag)) break;
    __builtin_amdgcn_s_sleep(1);
  }
  wvm<0>();
}
DI void publish(srd_t ws, u32x2 tg, unsigned idx, bool on) { st8s(tg, ws, on ? idx * 128 : OOB, OFF_FLG); }
DI bool all_set(u32x2 f, u32x2 tg) { return __all(f.x == tg.x && f.y == tg.y); }

#define ST(i) do {} while (0)

template <int NU> DI f32x16 colsum(unsigned a) {
  const u32x4 ones = {ONE2, ONE2, ONE2, ONE2};
  f32x16 c = zero16();
  sfor<0, NU>([&](auto UU) { c = mfma(trfrag(a + UU.v * 2048, a + UU.v * 2048 + 1024), ones, c); });
  return c;
}
DI void cs_write(unsigned dst, const f32x16& c, int lane) {
  if ((lane & 31) == 0) {
    const unsigned d = dst + (lane >> 5) * 16;
    sfor<0, 4>([&](auto A) { ldsw<f32x4>(d + A.v * 32, f32x4{c[4 * A.v], c[4 * A.v + 1], c[4 * A.v + 2], c[4 * A.v + 3]}); });
  }
}
DI float max3(float a, float b, float c) { float r; asm("v_max3_f32 %0, %1, %2, %3" : "=v"(r) : "v"(a), "v"(b), "v"(c)); return r; }
DI float min3(float a, float b, float c) { float r; asm("v_min3_f32 %0, %1, %2, %3" : "=v"(r) : "v"(a), "v"(b), "v"(c)); return r; }
DI void dual_softmax(f32x16 p, bool hi, u32x4 (&o)[4]) {
  p[0] += 0.f;
#pragma unroll
  for (int i = 12; i < 16; ++i) p[i] = hi ? p[0] : p[i];
  float mx = max3(p[0], p[1], p[2]), mn = min3(p[0], p[1], p[2]);
#pragma unroll
  for (int i = 3; i < 15; i += 2) { mx = max3(mx, p[i], p[i + 1]); mn = min3(mn, p[i], p[i + 1]); }
  float a, b;
  halves(max3(mx, p[15], p[15]), a, b);
  const float cp = -LOG2E * max3(a, b, b);
  halves(min3(mn, p[15], p[15]), a, b);
  const float cm = LOG2E * min3(a, b, b);
  f32x2 ep[8], em[8];
#pragma unroll
  for (int i = 0; i < 8; ++i) {
    const f32x2 v = {p[2 * i], p[2 * i + 1]}, x = v * LOG2E + cp, y = v * -LOG2E + cm;
    ep[i] = f32x2{__builtin_amdgcn_exp2f(x[0]), __builtin_amdgcn_exp2f(x[1])};
    em[i] = f32x2{__builtin_amdgcn_exp2f(y[0]), __builtin_amdgcn_exp2f(y[1])};
  }
  const float keep = hi ? 0.f : 1.f;
  const f32x2 sp = ((ep[0] + ep[1]) + (ep[2] + ep[3])) + ((ep[4] + ep[5]) + (ep[6] + ep[7]) * keep);
  const f32x2 sn = ((em[0] + em[1]) + (em[2] + em[3])) + ((em[4] + em[5]) + (em[6] + em[7]) * keep);
  const float rp = __builtin_amdgcn_rcpf(swap_add(sp[0] + sp[1])), rm = __builtin_amdgcn_rcpf(swap_add(sn[0] + sn[1]));
#pragma unroll
  for (int i = 0; i < 8; ++i) {
    const f32x2 x = ep[i] * (i < 6 ? rp : rp * keep), y = em[i] * (i < 6 ? rm : rm * keep);
    o[i >> 2][i & 3] = pk2(x[0], x[1]);
    o[2 + (i >> 2)][i & 3] = pk2(y[0], y[1]);
  }
}
DI void granule(u32x4& g, srd_t ws, unsigned voff, u32x2 tg) {
#pragma unroll 1
  for (unsigned spins = 0; spins < SPIN_CAP && !__all(g[1] == tg.x && g[3] == tg.y); ++spins) {
    ld16s<0>(g, voff, ws, OFF_ST0);
    wvm<0>(g);
  }
}
DI f32x16 cs_read(unsigned a, unsigned str) {
  f32x4 v[4][4];
  sfor<0, 4>([&](auto P) { sfor<0, 4>([&](auto A) { v[P.v][A.v] = ldsr<f32x4>(a + P.v * str + A.v * 32); }); });
  f32x16 r;
  sfor<0, 4>([&](auto A) {
    const f32x4 t = (v[0][A.v] + v[1][A.v]) + (v[2][A.v] + v[3][A.v]);
    sfor<0, 4>([&](auto I) { r[4 * A.v + I.v] = t[I.v]; });
  });
  return r;
}
DI f32x4 quad(const f32x16& c, int a) { return f32x4{c[4 * a], c[4 * a + 1], c[4 * a + 2], c[4 * a + 3]}; }
DI u32x4 cross(u32x4 v) {
  auto a = __builtin_amdgcn_permlane32_swap(v[0], v[2], false, false);
  auto b = __builtin_amdgcn_permlane32_swap(v[1], v[3], false, false);
  return u32x4{a[0], b[0], a[1], b[1]};
}

#define STARG
#define STPASS
DI void code_prefetch(unsigned& d, u64 pc, unsigned voff) {
  asm volatile("buffer_load_dword %0, %1, %2, 0 offen" : "=v"(d) : "v"(voff), "s"(mk((const void*)(pc & ~127ull), CODE_BYTES)) : "memory");
}
DI u64 make_tag(u32x2 ev) {
  return (((u64)(unsigned)__builtin_amdgcn_readfirstlane(ev.y) << 32) | (unsigned)__builtin_amdgcn_readfirstlane(ev.x)) + 0x9E3779B97F4A7C15ull;
}

struct SlotP1 {
  static constexpr unsigned off(int c) { return R2 - 16384 + ((c + 1) % 4) * 16384; }
  static DI unsigned addr(const unsigned (&xr)[4], int U) { return xr[U & 3] + ((U / 4 + 1) % 4) * 16384; }
};
constexpr unsigned HREG = R2;
constexpr unsigned XT = R2 + 40960;
constexpr int UH = 12;
constexpr int NB = 5;
constexpr unsigned POISON = 0xFFFFFFFFu;
constexpr int M3 = 20;
template <int TS, class Slot>
DI void pass_a(f32x16& c0, f32x16& c1, const unsigned a0, const unsigned a1, const unsigned (&xr)[4], u32x4* xf) {
  auto rx = [&](auto UU) { xf[UU.v] = ldsr<u32x4>(Slot::addr(xr, UU.v)); };
  u32x4 wf[3][2];
  auto rd = [&](auto UU, auto NN) {
    const unsigned ad = (NN.v ? a1 : a0) + UU.v * 2048;
    wf[UU.v % 3][NN.v] = trfrag(ad, ad + TS);
  };
  bar();
  rx(IC<0>{}); rd(IC<0>{}, IC<0>{}); rd(IC<0>{}, IC<1>{}); rx(IC<1>{}); rd(IC<1>{}, IC<0>{}); rd(IC<1>{}, IC<1>{});
  sfor<0, 28>([&](auto UU) {
    constexpr int U = UU.v;
    if constexpr (U % 4 == 2 && U < 26) asm volatile("s_barrier" ::: "memory");
    mmx<U == 0>(c0, wf[U % 3][0], xf[U]);
    if constexpr (U + 2 < 28) { rx(IC<U + 2>{}); rd(IC<U + 2>{}, IC<0>{}); }
    SB();
    mmx<U == 0>(c1, wf[U % 3][1], xf[U]);
    if constexpr (U + 2 < 28) rd(IC<U + 2>{}, IC<1>{});
    SB();
  });
}
DI int rot7(int c, int rot) { const int v = c + rot; return v >= 7 ? v - 7 : v; }
struct Geo {
  int r7, s7, g4, tt, s14, rot;
  bool wtype;
  DI Geo(int xc, int r) {
    r7 = r / 7;
    s7 = r - 7 * r7;
    g4 = 4 * (xc >> 1) + r7;
    tt = 4 * xc + r7;
    s14 = 7 * (xc & 1) + s7;
    rot = 0;
    wtype = (xc & 1) == 0;
  }
};
DI unsigned p4_slot(int kq, int j) { return (j < 3 ? R2 : j < 6 ? R1 : RA) + (3 * kq + j % 3) * 4096; }
DI int p4_k32(int kq, int s, int rot) {
  const int a = 7 * kq + rot7(s < 7 ? s : s - 7, rot);
  return 4 * (a >> 1) + (a & 1) + (s < 7 ? 0 : 2);
}

DI void first_loads(const Params& p, const int w, const int xc, const int r) {
  const int lane = threadIdx.x & 63;
  const Geo g(xc, r);
  const int rr = lane >> 3, cp = lane & 7;
  const unsigned wsw = (unsigned)((cp ^ (((rr >> 1) & 1) << 2)) << 4);
  const srd_t sX = mk(p.x, 2048 * 896), sQ = mk(p.wqkv, 448 * 2688);
  const srd_t sA = g.wtype ? mk(p.w1, 448 * 896) : sQ;
  const unsigned pitchA = g.wtype ? 896 : 2688;
  const unsigned colA = g.wtype ? g.s7 : 7 + g.s7;
  const unsigned wvA = rr * pitchA + wsw;
  const unsigned xv0 = rr * 896 + ((cp ^ (rr >> 1)) << 4), xv1 = xv0 ^ 64u;
  const unsigned T32 = 4 * g.g4 + w, xs = T32 * 32 * 896;
  auto G1 = [&](int c) {
    const int cg = rot7(c, g.rot);
    sfor<0, 2>([&](auto J) { dma16(wvA, sA, (8 * cg + 2 * w + J.v) * 8 * pitchA + colA * 128, RA + (8 * c + 2 * w + J.v) * 1024); });
    sfor<0, 4>([&](auto I) { dma16((I.v & 1) ? xv1 : xv0, sX, xs + I.v * 8 * 896 + cg * 128, SlotP1::off(c) + w * 4096 + I.v * 1024); });
  };
  __builtin_amdgcn_s_setprio(3);
  G1(0); G1(1);
}

DI void loader_path(const Params& p, const int w, const int xc, const int r STARG) {
  const int lane = threadIdx.x & 63, t32 = lane & 31, h = lane >> 5;
  const Geo g(xc, r);
  const int rr = lane >> 3, cp = lane & 7;
  const unsigned wsw = (unsigned)((cp ^ (((rr >> 1) & 1) << 2)) << 4);
  const srd_t sWS = mk(p.ws, WS_USED), sX = mk(p.x, 2048 * 896), sQ = mk(p.wqkv, 448 * 2688);
  const srd_t sA = g.wtype ? mk(p.w1, 448 * 896) : sQ;
  const unsigned pitchA = g.wtype ? 896 : 2688;
  const unsigned colA = g.wtype ? g.s7 : 7 + g.s7, colB = g.wtype ? g.s7 : 14 + g.s7;
  const unsigned wvA = rr * pitchA + wsw;
  const unsigned xv0 = rr * 896 + ((cp ^ (rr >> 1)) << 4), xv1 = xv0 ^ 64u;
  const unsigned T32 = 4 * g.g4 + w, xs = T32 * 32 * 896;
  auto G1 = [&](int c) {
    const int cg = rot7(c, g.rot);
    sfor<0, 2>([&](auto J) { dma16(wvA, sA, (8 * cg + 2 * w + J.v) * 8 * pitchA + colA * 128, RA + (8 * c + 2 * w + J.v) * 1024); });
    sfor<0, 4>([&](auto I) { dma16((I.v & 1) ? xv1 : xv0, sX, xs + I.v * 8 * 896 + cg * 128, SlotP1::off(c) + w * 4096 + I.v * 1024); });
  };
  u32x2 ev;
  ld8s<0>(ev, 0u, sWS, OFF_FLG);
  SB();
  const unsigned wv896 = rr * 896 + wsw, wv3 = rr * 3584 + wsw, wvB = rr * 2688 + wsw;
  auto WB = [&](int k) { dma16(wvB, sQ, (8 * rot7(k >> 1, g.rot) + 4 * (k & 1) + w) * 8 * 2688 + colB * 128, R1 + (4 * k + w) * 1024); };
  const int q = (lane & 15) >> 2, pp = lane & 3, blk = (lane >> 4) & 1;
  const unsigned ca = (4 * h + q) * 128 + (unsigned)((((2 * blk + (pp >> 1)) ^ ((q >> 1) << 2)) << 4) + (pp & 1) * 8);
  const u32x4 ones = {ONE2, ONE2, ONE2, ONE2};
  f32x16 s0 = zero16(), s1 = zero16();
  auto CS = [&](unsigned a) { s0 = mfma(trfrag(a, a + 1024), ones, s0); s1 = mfma(trfrag(a ^ 64u, (a ^ 64u) + 1024), ones, s1); };
  const unsigned cw = RA + ca + w * 2048;
  u32x4 gr;
  wvm<7>(); bar(); G1(2); CS(cw);
  wvm<7>(); bar(); G1(3); CS(cw + 8192);
  wvm<6>(ev); bar(); G1(4); CS(cw + 2 * 8192);
  wvm<6>(); bar(); G1(5); CS(cw + 3 * 8192);
  ld16s<0>(gr, (T32 * 32 + t32) * 16, sWS, OFF_ST0);
  wvm<7>(); bar(); G1(6); CS(cw + 4 * 8192);
  const u64 tag = make_tag(ev);
  const u32x2 tg = {(unsigned)tag, (unsigned)(tag >> 32)};
  wvm<6>(gr); bar(); sfor<0, 3>([&](auto K) { WB(K.v); });
  CS(cw + 5 * 8192);
  granule(gr, sWS, (T32 * 32 + t32) * 16, tg);
  const float ve1 = u2f(gr[2]);
  float r1 = __builtin_amdgcn_rsqf(ve1);
  const float v2 = (ve1 - 1e-5f) * r1 * r1 + 1e-5f;
  GeluK k1 = gelu_k(r1 * __builtin_amdgcn_rsqf(v2), __builtin_amdgcn_sqrtf(ve1 * v2));
  k1[3] = u2f(gr[0]);
  tie(k1); tie(r1);
  cs_write(R2 - 16384 + w * 128, s0, lane); cs_write(R2 - 16384 + 512 + w * 128, s1, lane);
  wvm<3>(); bar();
  {
    const unsigned a = R2 - 16384 + h * 512 + t32 * 4, c6 = (RA + ca + 24 * 2048) ^ ((w & 1) * 64);
    u32x4 f[4];
    sfor<0, 4>([&](auto U) { f[U.v] = trfrag(c6 + U.v * 2048, c6 + U.v * 2048 + 1024); });
    const float t = (ldsr<float>(a) + ldsr<float>(a + 128)) + (ldsr<float>(a + 256) + ldsr<float>(a + 384));
    s0 = zero16();
    sfor<0, 4>([&](auto U) { s0 = mfma(f[U.v], ones, s0); });
    sfor<3, 6>([&](auto K) { WB(K.v); });
    ldsw<f32x4>(R2 + 2048 + w * 1024 + lane * 16, k1);
    ldsw<float>(R2 + 6144 + w * 256 + lane * 4, r1);
    ldsw<float>(R2 + 1024 + lane * 4, t);
    cs_write(R2 + 1280 + (w & 1) * 128, s0, lane);
  }
  GAS u64* fl = (GAS u64*)(p.ws + OFF_FLG);
  const bool l0 = threadIdx.x == 256;
  bar();
  __builtin_amdgcn_s_setprio(0);
  sfor<6, 14>([&](auto K) { WB(K.v); });
  sfor<0, 2>([&](auto J) { dma16(rr * 896 + ((cp ^ rr) << 4), sX, (g.tt * 64 + (2 * w + J.v) * 8) * 896 + g.s7 * 128, XT + (2 * w + J.v) * 1024); });
  const srd_t sW2 = mk(p.w2, 448 * 896);
  auto W2 = [&](int k) { const int pc = 8 * rot7(k >> 1, g.rot) + 4 * (k & 1) + w; dma16(wv896, sW2, pc * 8 * 896 + g.s7 * 128, RA + pc * 1024); };
  sfor<0, 4>([&](auto K) { W2(K.v); });
  wvm<4>(); bar();
  sfor<4, 14>([&](auto K) { W2(K.v); });
  const srd_t sW4 = mk(p.w4, 1792 * 896), sWo = mk(p.wo, 448 * 896), sW3 = mk(p.w3, 448 * 3584);
  u32x4 w4r[9][4], w6r[12];
  auto L4 = [&](auto SS) {
    constexpr int s = SS.v;
    sfor<0, 4>([&](auto I) {
      u32x4& d = s < 9 ? w6r[4 * (s - 6) + I.v] : w4r[s < 9 ? 0 : s - 9][I.v];
      if constexpr (s < 14) ld16p(d, wv896, sW4, (32 * p4_k32(w, s, g.rot) + 8 * I.v) * 896 + g.s7 * 128);
      else ld16p(d, wv896, sWo, (28 * (4 * w + ((s + g.rot) & 3)) + 8 * I.v) * 896 + g.s7 * 128);
    });
  };
  bar();
  publish(sWS, tg, FL_A + g.g4 * 14 + g.s14, l0);
  poll(fl, 1, FL_CS + 28 + g.s14, tag);
  asm volatile("s_mov_b32 m0, %3\n\ts_nop 0\n\tbuffer_load_dword %0, %1, %2 offen sc1 lds" ::"v"(lane * 4), "s"(sWS), "s"(OFF_CS3 + (28 + g.s14) * 256), "s"(XT) : "memory");
  {
    const int th = w & 1, kh = w >> 1, tc = 2 * g.tt + th;
    const int gsel = min(max(tc + ((lane & 4) ? 2 * th - 1 : 0), 0), 63) >> 2;
    const int t3 = T32 >> 1, px = (4 * (t3 >> 3) + (t3 & 3)) * 14 + 7 * ((t3 >> 2) & 1);
    poll(fl, 17, lane < 8 ? FL_A + gsel * 14 + min(3 * kh + (lane & 3), 6) : lane < 15 ? FL_A + px + lane - 8 : FL_CS + 2 * g.s14 + (lane & 1), tag);
    const srd_t sH = mk(p.ws + OFF_H, 64 * 28 * 1024);
    const int tok = tc * 32 + t32, ta = tok - 1 - kh, tb = tok + 1 + kh;
    const unsigned oa = (unsigned)((ta >> 5) * 28 * 1024 + ((ta & 31) + 32 * h) * 16), ob = (unsigned)((tb >> 5) * 28 * 1024 + ((tb & 31) + 32 * h) * 16);
    sfor<0, 6>([&](auto U) { dma16s(oa, sH, (kh * 14 + U.v) * 1024, HREG + w * 10240 + U.v * 1024); });
    bar();
    dma16s(oa, sH, (kh * 14 + 6) * 1024, HREG + w * 10240 + 6144);
    sfor<7, 10>([&](auto U) { dma16s(ob, sH, (kh * 14 + U.v) * 1024, HREG + w * 10240 + U.v * 1024); });
  }
  wvm<0>();
  bar();
  auto o3 = [&](int k) { return (unsigned)((8 * rot7(k >> 1, g.rot) + 4 * (k & 1) + w) * 8 * 3584 + g.s14 * 256); };
  sfor<0, 14>([&](auto K) { dma16(wv3, sW3, o3(K.v), R1 + (4 * K.v + w) * 1024); });
  bar();
  publish(sWS, tg, FL_Q + g.g4 * 14 + g.s14, l0);
  sfor<0, 4>([&](auto K) { dma16(wv3, sW3, o3(K.v) + 128, RA + (4 * K.v + w) * 1024); });
  wvm<5>(); bar();
  sfor<4, 14>([&](auto K) { dma16(wv3, sW3, o3(K.v) + 128, RA + (4 * K.v + w) * 1024); });
  auto W4 = [&](auto SS) {
    sfor<0, 4>([&](auto I) { dma16(wv896, sW4, (32 * p4_k32(w, SS.v, g.rot) + 8 * I.v) * 896 + g.s7 * 128, p4_slot(w, SS.v) + I.v * 1024); });
  };
  sfor<0, 3>(W4);
  sfor<9, 9 + NB>(L4);
  wvm<12 + 4 * NB>();
  bar();
  sfor<3, 6>(W4);
  sfor<6, 9>(L4);
  wvm<24>();
  bar();
  publish(sWS, tg, FL_3A + g.g4 * 14 + g.s14, l0);
  sfor<9 + NB, 18>(L4);
  bar();
  const unsigned f3 = (g.tt >> 1) * 14 + (7 * w >> 1) + (lane & 3);
  poll(fl, 4, FL_3A + f3, tag);
  bar();
  sfor<0, 12>([&](auto K) { tie(w6r[K.v]); ldsw<u32x4>(p4_slot(w, 6 + K.v / 4) + (K.v & 3) * 1024 + lane * 16, w6r[K.v]); });
  bar();
  publish(sWS, tg, FL_3B + g.g4 * 14 + g.s14, l0);
  auto S4 = [&](auto J) { sfor<0, 4>([&](auto I) { tie(w4r[J.v][I.v]); ldsw<u32x4>(p4_slot(w, J.v) + I.v * 1024 + lane * 16, w4r[J.v][I.v]); }); };
  poll(fl, 4, FL_3B + f3, tag);
  bar();
  u32x4 x1[2];
  sfor<0, 2>([&](auto U) { ld16s<U.v * 1024>(x1[U.v], lane * 16, sWS, OFF_X1 + ((2 * g.tt + (w & 1)) * 28 + 4 * g.s7 + (w & 2)) * 1024); });
  sfor<0, 4>(S4);
  wvm2<0>(x1[0], x1[1]);
  bar(); sfor<4, 8>(S4);
  poll(fl, 4, FL_O + (g.tt >> 1) * 16 + 4 * w + (lane & 3), tag);
  bar(); S4(IC<8>{});
  bar();
  __builtin_amdgcn_s_setprio(3);
  sfor<0, 4>([&](auto A) {
    f32x4 b = ldsr<f32x4>(p4_slot(w, 0) + A.v * 1024 + lane * 16);
    f32x4 o[3];
    sfor<0, 3>([&](auto P) { o[P.v] = ldsr<f32x4>(R1 + (w ^ (P.v + 1)) * 12288 + (4 * P.v + A.v) * 1024 + lane * 16); });
    const unsigned y0 = x1[A.v >> 1][2 * (A.v & 1)], y1 = x1[A.v >> 1][2 * (A.v & 1) + 1];
    const float xx[4] = {bflo(y0), bfhi(y0), bflo(y1), bfhi(y1)};
    sfor<0, 4>([&](auto I) {
      float t = b[I.v] + o[0][I.v], u = o[1][I.v] + o[2][I.v];
      tie(t); tie(u);
      t = (t + xx[I.v]) + u;
      b[I.v] = t * __builtin_amdgcn_rcpf(1.f + __builtin_amdgcn_exp2f(-LOG2E * t));
    });
    ldsw<f32x4>(p4_slot(w, 1) + A.v * 1024 + lane * 16, b);
  });
  bar();
}

DI void compute_path(const Params& p, unsigned& dummy, const int w, const int xc, const int r STARG) {
  const int tid = threadIdx.x, lane = tid & 63, t32 = lane & 31, h = lane >> 5, th = w & 1, kh = w >> 1;
  const Geo g(xc, r);
  const srd_t sWS = mk(p.ws, WS_USED);
  __builtin_amdgcn_s_setprio(2);
  unsigned dm[4];
  {
    const unsigned id = ((xc * 28 + r) * 4 + w) * 35 + lane;
    const bool on = lane < 35;
    ld4p(dm[0], on && id < 3136 ? id * 128 : OOB, mk(p.w2, 448 * 896));
    ld4p(dm[1], on && id - 3136 < 12544 ? (id - 3136) * 128 : OOB, mk(p.w3, 448 * 3584));
    ld4p(dm[2], on && id - 15680 < 12544 ? (id - 15680) * 128 : OOB, mk(p.w4, 1792 * 896));
    ld4p(dm[3], on && id - 28224 < 3136 ? (id - 28224) * 128 : OOB, mk(p.wo, 448 * 896));
  }
  u32x2 ev;
  ld8s<0>(ev, 0u, sWS, OFF_FLG);
  const unsigned T32 = 4 * g.g4 + w, tc = 2 * g.tt + th;
  const int ftok = tc * 32 + t32;
  const unsigned x1o = OFF_X1 + (tc * 28 + 4 * g.s7 + 2 * kh) * 1024, st1o = OFF_ST1 + (2 * g.s7 + kh) * 2048 * 8;
  st16s(u32x4{POISON, POISON, POISON, POISON}, sWS, lane * 16, x1o); st16s<1024>(u32x4{POISON, POISON, POISON, POISON}, sWS, lane * 16, x1o);
  st8s(u32x2{POISON, POISON}, sWS, h == 0 ? ftok * 8 : OOB, st1o);
  u32x4 xv[2], x1p[2], gr;
  f32x16 xn;
  const int q = (lane & 15) >> 2, pp = lane & 3, blk = (lane >> 4) & 1;
  unsigned aperm = (4 * h + q) * 128 + (((2 * blk + (pp >> 1)) ^ ((q >> 1) << 2)) << 4) + (pp & 1) * 8, l16 = lane * 16;
  auto tra = [&](int hs, unsigned base, unsigned x) {
    tie(aperm);
    unsigned a = base + (aperm ^ x);
    if (hs == 8) a += l16 & 512;
    tie(a);
    return a;
  };
  auto lnv = [&](unsigned base) { tie(l16); unsigned a = base + l16; tie(a); return a; };
  u32x4 xf[28];
  unsigned xr[4], a0 = tra(8, RA, 0), a1 = tra(8, RA, 64);
  sfor<0, 4>([&](auto I) { xr[I.v] = R2 - 16384 + w * 4096 + t32 * 128 + (((2 * I.v + h) ^ ((t32 >> 1) & 7)) << 4); tie(xr[I.v]); });
  f32x16 c0, c1, c2, c3;
  pass_a<512, SlotP1>(c0, c1, a0, a1, xr, xf);
  bar();
  settle(c0, c1);
  f32x4 cst[8];
  const GeluK gk1 = ldsr<f32x4>(R2 + 2048 + w * 1024 + lane * 16);
  const float mean1 = gk1[3], r1 = ldsr<float>(R2 + 6144 + w * 256 + lane * 4);
  auto cs_load = [&](unsigned a) {
    sfor<0, 8>([&](auto A) { cst[A.v] = ldsr<f32x4>(a + A.v * 32 + h * 16); });
    sfor<0, 8>([&](auto A) { sfor<0, 4>([&](auto I) { (A.v < 4 ? c2 : c3)[4 * (A.v & 3) + I.v] = cst[A.v][I.v]; }); });
  };
  cs_load(R2 + 1024);
  sfor<0, 8>([&](auto A) { const f32x4 t = ldsr<f32x4>(R2 + 1280 + A.v * 32 + h * 16); sfor<0, 4>([&](auto I) { (A.v < 4 ? c2 : c3)[4 * (A.v & 3) + I.v] += t[I.v]; }); });
  wvm<0>(ev);
  tie(dummy); tie(dm[0]); tie(dm[1]); tie(dm[2]); tie(dm[3]);
  const u64 tag = make_tag(ev);
  const u32x2 tg = {(unsigned)tag, (unsigned)(tag >> 32)};
  auto epi = [&](auto ACT, unsigned so) {
    unsigned pk[4];
    sfor<0, 16>([&](auto J) {
      constexpr int e = 2 * (J.v & 7);
      const f32x16 &c = J.v < 8 ? c0 : c1, &s = J.v < 8 ? c2 : c3;
      f32x2 z = f32x2{c[e], c[e + 1]} - f32x2{s[e], s[e + 1]} * mean1;
      if constexpr (ACT.v) z = gelu2(z, gk1); else z *= r1;
      pk[J.v & 3] = pk2(z[0], z[1]);
      if constexpr ((J.v & 3) == 3) st16s<(J.v >> 2) * 1024>(u32x4{pk[0], pk[1], pk[2], pk[3]}, sWS, l16, so);
    });
  };
  if (g.wtype) epi(IC<1>{}, OFF_H + (T32 * 28 + 4 * g.s7) * 1024);
  else epi(IC<0>{}, OFF_QKV + (T32 * 84 + 28 + 4 * g.s7) * 1024);
  ld16s<0>(gr, ftok * 16, sWS, OFF_ST0);
  bar();
  ST(3);
  {
    u32x4 wf[3][2];
    float m0, r0;
    auto rd = [&](auto UU, auto NN) {
      const unsigned ad = (NN.v ? a1 : a0) + UU.v * 2048;
      wf[UU.v % 3][NN.v] = trfrag(ad, ad + 512);
    };
    a0 = tra(8, R1, 0); a1 = tra(8, R1, 64);
    rd(IC<0>{}, IC<0>{}); rd(IC<0>{}, IC<1>{}); rd(IC<1>{}, IC<0>{}); rd(IC<1>{}, IC<1>{});
    sfor<0, 28>([&](auto UU) {
      constexpr int U = UU.v;
      mmx<U == 0>(c0, wf[U % 3][0], xf[U]);
      if constexpr (U == UH) {
        wvm<0>(gr);
        bar();
        granule(gr, sWS, ftok * 16, tg);
      }
      if constexpr (U + 2 < 28) rd(IC<U + 2>{}, IC<0>{});
      if constexpr (U >= 2 && U < 6) {
        constexpr int A = U - 2;
        const u32x2 t = ldsr<u32x2>(XT + (th * 32 + t32) * 128 + (((4 * kh + A) ^ (t32 & 7)) << 4) + h * 8);
        xv[A >> 1][2 * (A & 1)] = t.x; xv[A >> 1][2 * (A & 1) + 1] = t.y;
      }
      SB();
      mmx<U == 0>(c1, wf[U % 3][1], xf[U]);
      if constexpr (U + 2 < 28) rd(IC<U + 2>{}, IC<1>{});
      if constexpr (U == UH + 1) { r0 = __builtin_amdgcn_rsqf(u2f(gr[2])); m0 = -u2f(gr[0]) * r0; tie(r0); tie(m0); }
      if constexpr (U >= 20) {
        constexpr int I = U - 20;
        float a = fmaf(bflo(xv[I >> 2][I & 3]), r0, m0), b = fmaf(bfhi(xv[I >> 2][I & 3]), r0, m0);
        tie(a); tie(b);
        xn[2 * I] = a; xn[2 * I + 1] = b;
      }
      SB();
    });
  }
  ST(4);
  bar();
  {
    const srd_t sH = mk(p.ws + fresh(OFF_H), 64 * 28 * 1024);
    const int tb = ftok + 1 + fresh(kh);
    const unsigned ob = (unsigned)((tb >> 5) * 28 * 1024 + ((tb & 31) + 32 * h) * 16);
    sfor<10, 14>([&](auto U) { ld16s<(U.v - 10) * 1024>(xf[U.v], ob, sH, (fresh(kh) * 14 + 10) * 1024); });
  }
  cs_load(XT);
  settle(c0, c1);
  epi(IC<0>{}, OFF_QKV + (fresh(T32) * 84 + (g.wtype ? 0 : 56) + 4 * g.s7) * 1024);
  ST(5);
  c0 = xn;
  {
    a0 = tra(4, RA + fresh(kh) * 14 * 2048, fresh(kh) * 64); a1 = a0 ^ 64u; tie(a1);
    const unsigned hb = lnv(HREG + fresh(w) * 10240);
    tie(c0);
  bar();
    u32x4 wf[3][2];
    auto rd = [&](auto UU) {
      if constexpr (UU.v < 10) xf[UU.v] = ldsr<u32x4>(hb + UU.v * 1024);
      wf[UU.v % 3][0] = trfrag(a0 + UU.v * 2048, a0 + UU.v * 2048 + 1024); wf[UU.v % 3][1] = trfrag(a1 + UU.v * 2048, a1 + UU.v * 2048 + 1024);
    };
    rd(IC<0>{}); rd(IC<1>{});
    sfor<0, 14>([&](auto UU) {
      constexpr int U = UU.v;
      if constexpr (U == 10) wvm4<4>(xf[10], xf[11], xf[12], xf[13]);
      mma(c0, wf[U % 3][0], xf[U]);
      if constexpr (U + 2 < 14) rd(IC<U + 2>{});
      mmx<U == 0>(c1, wf[U % 3][1], xf[U]);
      SB();
    });
    settle(c0, c1);
    sfor<0, 4>([&](auto A) { ldsw<f32x4>(hb + A.v * 1024, quad(c1, A.v)); });
  }
  wvm<0>();
  bar();
  ST(27);
  {
    f32x4 o[4];
    const unsigned pb = lnv(HREG + (fresh(w) ^ 2) * 10240);
    sfor<0, 4>([&](auto A) { o[A.v] = ldsr<f32x4>(pb + A.v * 1024); });
    sfor<0, 4>([&](auto A) {
      const f32x4 t = quad(c0, A.v) + o[A.v];
      x1p[A.v >> 1][2 * (A.v & 1)] = pk2(t[0], t[1]);
      x1p[A.v >> 1][2 * (A.v & 1) + 1] = pk2(t[2], t[3]);
    });
    sfor<0, 8>([&](auto I) { x1p[I.v >> 2][I.v & 3] = min(x1p[I.v >> 2][I.v & 3], POISON - 1); });
    st16s(x1p[0], sWS, l16, x1o); st16s<1024>(x1p[1], sWS, l16, x1o);
    float a1 = 0.f, a2 = 0.f;
    sfor<0, 8>([&](auto I) { a1 = dot2(x1p[I.v >> 2][I.v & 3], ONE2, a1); a2 = dot2(x1p[I.v >> 2][I.v & 3], x1p[I.v >> 2][I.v & 3], a2); });
    st8s(u32x2{min(f2u(swap_add(a1)), POISON - 1), min(f2u(swap_add(a2)), POISON - 1)}, sWS, h == 0 ? ftok * 8 : OOB, st1o);
  }
  ST(7);
  bar();
  ST(8);
  GeluK gk3;
  float mean3;
  u32x2 cs3;
  {
    a0 = tra(4, R1, 0); a1 = tra(4, R1, 64);
    const unsigned spo = (T32 * 32 + t32 + h * 7 * 2048) * 8;
    ld8s<0>(cs3, lane * 8, sWS, OFF_CS3 + g.s14 * 512);
#pragma unroll 1
    for (unsigned spins = 0; spins < SPIN_CAP; ++spins) {
      unsigned v = 0;
      if (lane < 14) v = __hip_atomic_load((GAS unsigned*)(p.ws + OFF_ST1 + (lane * 2048 + T32 * 32 + 31) * 8), __ATOMIC_RELAXED, __HIP_MEMORY_SCOPE_AGENT);
      if (__all(v != POISON)) break;
      __builtin_amdgcn_s_sleep(2);
    }
    wvm<0>(cs3);
    ST(11);
    const unsigned xb = lnv(OFF_X1 + fresh(T32) * 28 * 1024);
    unsigned xo;
    auto X1 = [&](auto UU) {
      if constexpr ((UU.v & 3) == 0) xo = rot7(UU.v / 4, fresh(g.rot)) * 4096;
      ld16s<(UU.v & 3) * 1024>(xf[UU.v], xb, sWS, xo);
    };
    u32x2 sp[7];
    auto SP = [&]() { sfor<0, 7>([&](auto I) { ld8s<0>(sp[I.v], spo, sWS, OFF_ST1 + I.v * 2048 * 8); }); };
    sfor<0, 12>(X1);
    u32x4 wf[3][2];
    auto rd = [&](auto UU, auto NN) { const unsigned ad = (NN.v ? a1 : a0) + UU.v * 2048; wf[UU.v % 3][NN.v] = trfrag(ad, ad + 1024); };
    auto check = [&](auto CC, auto NN) {
      constexpr int U = 4 * CC.v;
      wvm4<NN.v>(xf[U], xf[U + 1], xf[U + 2], xf[U + 3]);
#pragma unroll 1
      for (unsigned spins = 0; spins < SPIN_CAP; ++spins) {
        unsigned m = 0;
        sfor<0, 4>([&](auto I) { m = max(max(m, max(xf[U + I.v][0], xf[U + I.v][1])), max(xf[U + I.v][2], xf[U + I.v][3])); });
        if (__builtin_expect(__all(m != POISON), 1)) break;
        sfor<U, U + 4>(X1);
        wvm4<0>(xf[U], xf[U + 1], xf[U + 2], xf[U + 3]);
      }
    };
    rd(IC<0>{}, IC<0>{}); rd(IC<0>{}, IC<1>{}); rd(IC<1>{}, IC<0>{}); rd(IC<1>{}, IC<1>{});
    check(IC<0>{}, IC<8>{});
    sfor<0, 28>([&](auto UU) {
      constexpr int U = UU.v;
      mmx<U == 0>(c0, wf[U % 3][0], xf[U]);
      if constexpr (U + 2 < 28) rd(IC<U + 2>{}, IC<0>{});
      if constexpr (U + 12 < 28) X1(IC<U + 12>{});
      if constexpr (U == 16) SP();
      SB();
      mmx<U == 0>(c1, wf[U % 3][1], xf[U]);
      if constexpr (U + 2 < 28) rd(IC<U + 2>{}, IC<1>{});
      if constexpr (U % 4 == 3 && U < 27) check(IC<U / 4 + 1>{}, IC<(U < 16 ? 8 : U == 19 ? 11 : 7)>{});
      SB();
    });
    wvm<0>();
#pragma unroll 1
    for (unsigned spins = 0; spins < SPIN_CAP; ++spins) {
      unsigned m = 0;
      sfor<0, 7>([&](auto I) { tie(sp[I.v]); m = max(m, max(sp[I.v].x, sp[I.v].y)); });
      if (__builtin_expect(__all(m != POISON), 1)) break;
      SP();
      wvm<0>();
    }
    float sm = 0.f, sq = 0.f;
    sfor<0, 7>([&](auto I) { tie(sp[I.v]); sm += u2f(sp[I.v].x); sq += u2f(sp[I.v].y); });
    mean3 = swap_add(sm) * (1.f / D);
    const float ve = fmaxf(swap_add(sq) * (1.f / D) - mean3 * mean3, 0.f) + 1e-5f;
    gk3 = gelu_k(__builtin_amdgcn_rsqf(ve), __builtin_amdgcn_sqrtf(ve));
    tie(gk3); tie(mean3);
  }
  bar();
  ST(13);
  ldsw<u32x2>(R1 + 49152 + fresh(w) * 512 + lane * 8, cs3);
  unsigned csb = R1 + 49152 + fresh(w) * 512 + h * 16;
  tie(csb);
  auto CS3 = [&](int t) { sfor<0, 8>([&](auto A) { cst[A.v] = ldsr<f32x4>(csb + t * 128 + A.v * 32); }); };
  const unsigned so3 = OFF_H3 + (fresh(T32) * 112 + 8 * g.s14) * 1024;
  unsigned pk[4];
  auto G = [&](auto JJ) {
    constexpr int j = JJ.v, t = j >> 3, e = 2 * (j & 7);
    const f32x16& c = t == 0 ? c0 : t == 1 ? c1 : t == 2 ? c2 : c3;
    const f32x2 z = gelu2(f32x2{c[e], c[e + 1]} - f32x2{cst[4 * (t & 1) + (e >> 2)][e & 3], cst[4 * (t & 1) + (e >> 2)][(e & 3) + 1]} * mean3, gk3);
    pk[j & 3] = pk2(z[0], z[1]);
    if constexpr ((j & 3) == 3) st16s<(j >> 2 & 3) * 1024>(u32x4{pk[0], pk[1], pk[2], pk[3]}, sWS, l16, so3 + (j >> 4) * 4096);
  };
  CS3(0);
  settle(c0, c1);
  sfor<0, 16>([&](auto J) { G(J); });
  ST(24);
  {
    u32x4 wf[4];
    a0 = tra(4, RA, 0); a1 = tra(4, RA, 64);
    auto rd = [&](auto MM) { const unsigned ad = (MM.v < 28 ? a0 : a1) + (MM.v % 28) * 2048; wf[MM.v & 3] = trfrag(ad, ad + 1024); };
    rd(IC<0>{}); rd(IC<1>{}); rd(IC<2>{});
    sfor<0, 56>([&](auto MM) {
      constexpr int M = MM.v;
      mmx<M % 28 == 0>(M < 28 ? c2 : c3, wf[M & 3], xf[M % 28]);
      if constexpr (M + 3 < 56) rd(IC<M + 3>{});
      if constexpr (M == M3) { wvm<0>(); asm volatile("s_barrier" ::: "memory"); }
      if constexpr (M == 50) CS3(2);
      SB();
    });
  }
  bar();
  ST(14);
  settle(c2, c3);
  sfor<16, 24>([&](auto J) { G(J); });
  ST(15);
  u32x4 af[4][4];
  unsigned av[2] = {lnv(OFF_H3 + fresh(tc) * 112 * 1024), lnv(OFF_H3 + (fresh(tc) ^ 1) * 112 * 1024)}, as;
  auto ACT1 = [&](auto SS, auto II) {
    constexpr int s = SS.v;
    if constexpr (s == 14 && II.v == 0) { av[0] = lnv(OFF_O + fresh(tc) * 32 * 1024); av[1] = lnv(OFF_O + (fresh(tc) ^ 1) * 32 * 1024); }
    if constexpr (II.v == 0) as = s < 14 ? p4_k32(w, s, fresh(g.rot)) * 2048 : (4 * w + ((s + fresh(g.rot)) & 3)) * 2048;
    ld16s<(II.v & 1) * 1024>(af[s & 3][II.v], av[II.v >> 1], sWS, as);
  };
  unsigned bs[3][2];
  sfor<0, 3>([&](auto I) { bs[I.v][0] = tra(4, p4_slot(fresh(w), 3 * I.v), fresh(kh) * 64); bs[I.v][1] = bs[I.v][0] ^ 64u; tie(bs[I.v][1]); });
  u32x4 wf[2][4];
  auto RW = [&](auto SS, auto I) {
    constexpr int j = SS.v % 9;
    const unsigned aa = bs[j / 3][I.v & 1] + (j % 3) * 4096 + (I.v >> 1) * 2048;
    wf[SS.v & 1][I.v] = trfrag(aa, aa + 1024);
  };
  bar();
  sfor<0, 12>([&](auto K) { ACT1(IC<K.v / 4>{}, IC<K.v % 4>{}); });
  tie(c3);
  sfor<24, 32>([&](auto J) { G(J); });
  ST(16);
  sfor<0, 4>([&](auto I) { RW(IC<0>{}, I); });
  f32x16 d0, d1, d2, d3;
  const unsigned exw = lnv(R1 + fresh(w) * 12288), own = lnv(p4_slot(fresh(w), 0));
  sfor<0, 18>([&](auto SS) {
    constexpr int s = SS.v;
    if constexpr (s == 4 || s == 8 || s == 11 || s == 15) bar();
    if constexpr (s % 3 == 0) ST(17 + s / 3);
    wvm4<(s == 0 ? 10 : s == 1 ? 4 : s <= 15 ? 8 : 4 * (17 - s))>(af[s & 3][0], af[s & 3][1], af[s & 3][2], af[s & 3][3]);
    if constexpr (s == 1) asm volatile("s_barrier" ::: "memory");
    f32x16 &a0 = s < 14 ? c0 : d0, &a1 = s < 14 ? c1 : d1, &a2 = s < 14 ? c2 : d2, &a3 = s < 14 ? c3 : d3;
    auto side = [&](auto KK) {
      constexpr int k = 4 * s + KK.v;
      if constexpr (s + 3 <= 17) ACT1(IC<s + 3>{}, KK);
      if constexpr (k == 56) { settle(c0, c1); settle(c2, c3); }
      if constexpr (k >= 56 && k < 60) {
        sfor<0, 3>([&](auto J) { constexpr int A = 3 * (k - 56) + J.v; ldsw<f32x4>(exw + A * 1024, quad(A < 4 ? c1 : A < 8 ? c2 : c3, A & 3)); });
        ldsw<f32x4>(own + (k - 56) * 1024, quad(c0, k - 56));
      }
    };
    sfor<0, 2>([&](auto U) {
      mmx<s % 14 == 0 && U.v == 0>(a0, wf[s & 1][2 * U.v], af[s & 3][U.v]);
      if constexpr (s + 1 < 18) RW(IC<s + 1>{}, IC<2 * U.v>{});
      mmx<s % 14 == 0 && U.v == 0>(a1, wf[s & 1][2 * U.v], af[s & 3][2 + U.v]);
      side(IC<2 * U.v>{});
      SB();
      mmx<s % 14 == 0 && U.v == 0>(a2, wf[s & 1][2 * U.v + 1], af[s & 3][U.v]);
      if constexpr (s + 1 < 18) RW(IC<s + 1>{}, IC<2 * U.v + 1>{});
      mmx<s % 14 == 0 && U.v == 0>(a3, wf[s & 1][2 * U.v + 1], af[s & 3][2 + U.v]);
      side(IC<2 * U.v + 1>{});
      SB();
    });
  });
  ST(23);
  settle(d0, d1); tie(d2); tie(d3);
  const unsigned e3[3] = {lnv(RA + (fresh(w) ^ 1) * 12288), lnv(RA + (fresh(w) ^ 2) * 12288), lnv(RA + (fresh(w) ^ 3) * 12288)};
  unsigned oo = ftok * 896 + h * 16 + fresh(g.s7) * 128 + kh * 64;
  tie(oo);
  sfor<0, 12>([&](auto A) { ldsw<f32x4>(exw - R1 + A.v * 1024, quad(A.v < 4 ? d1 : A.v < 8 ? d2 : d3, A.v & 3)); });
  bar();
  sfor<0, 12>([&](auto A) {
    const f32x4 v = ldsr<f32x4>(e3[A.v / 4] + A.v * 1024);
    sfor<0, 4>([&](auto I) { d0[4 * (A.v & 3) + I.v] += v[I.v]; });
  });
  sfor<0, 4>([&](auto A) {
    const f32x4 v = ldsr<f32x4>(own + 4096 + A.v * 1024);
    sfor<0, 4>([&](auto I) { c0[4 * A.v + I.v] = v[I.v]; });
  });
  const srd_t sO = mk(p.out, 2048 * 896);
  sfor<0, 2>([&](auto U) {
    u32x4 ov;
    sfor<0, 4>([&](auto I) {
      constexpr int e = 8 * U.v + 2 * I.v;
      ov[I.v] = pk2(fmaf(d0[e], c0[e], bflo(xv[U.v][I.v])), fmaf(d0[e + 1], c0[e + 1], bfhi(xv[U.v][I.v])));
    });
    __builtin_amdgcn_raw_buffer_store_b128(cross(ov), sO, oo + U.v * 32, 0, 0);
  });
}

DI void spare_path(const Params& p, const int w, const int xc, const int r STARG) {
  const int tid = threadIdx.x, lane = tid & 63, t32 = lane & 31, h = lane >> 5, tb = w & 3, ch = w >> 2;
  constexpr unsigned WPHI = 160768, ZERO = 159744, DUMMY = 158720;
  const int rr = lane >> 3, cp = lane & 7, rs = r - 28;
  const srd_t sWS = mk(p.ws, WS_USED), sX = mk(p.x, 2048 * 896);
  GAS u64* fl = (GAS u64*)(p.ws + OFF_FLG);
  const unsigned srow = 512 * (xc >> 1) + 64 * ((xc & 1) * 4 + rs) + 8 * w + rr;
  u32x2 ev;
  ld8s<0>(ev, 0u, sWS, OFF_FLG);
  u32x4 xr7[7];
  sfor<0, 7>([&](auto K) { ld16p(xr7[K.v], srow * 896 + cp * 16, sX, K.v * 128); });
  const int hd = 4 * (xc >> 1) + rs, hf = xc & 1;
  wvm<0>(ev);
  const u64 tag = make_tag(ev);
  const u32x2 tg = {(unsigned)tag, (unsigned)(tag >> 32)};
  {
    float a1 = 0.f, a2 = 0.f;
    sfor<0, 7>([&](auto K) { tie(xr7[K.v]); sfor<0, 4>([&](auto I) { a1 = dot2(xr7[K.v][I.v], ONE2, a1); a2 = dot2(xr7[K.v][I.v], xr7[K.v][I.v], a2); }); });
    sfor<0, 3>([&](auto K) { a1 += __shfl_xor(a1, 1 << K.v); a2 += __shfl_xor(a2, 1 << K.v); });
    const float mean = a1 * (1.f / D), ve = fmaxf(a2 * (1.f / D) - mean * mean, 0.f) + 1e-5f;
    st16s(u32x4{f2u(mean), tg.x, f2u(ve), tg.y}, sWS, cp == 0 ? srow * 16 : OOB, OFF_ST0);
  }
  ST(1);
  const int bi = (xc >> 1) * 4 + rs, par = xc & 1;
  const int q = (lane & 15) >> 2, pp = lane & 3, blk = (lane >> 4) & 1;
  const unsigned acol = (unsigned)((((2 * blk + (pp >> 1)) ^ ((q >> 1) << 2)) << 4) + (pp & 1) * 8);
  const unsigned aperm = (4 * h + q) * 128 + acol;
  const srd_t sW3 = mk(p.w3, 448 * 3584);
  const unsigned wsw = (cp ^ (((rr >> 1) & 1) << 2)) << 4;
  {
    const srd_t s0 = bi < 7 ? mk(p.wqkv, 448 * 2688) : sW3, sP = mk(p.wphi, 16 * 1568);
    const unsigned pitch = bi < 7 ? 2688 : 3584, col = (14 * par + (bi < 7 ? bi : bi - 7)) * 128;
    sfor<0, 7>([&](auto K) { dma16(rr * pitch + wsw, s0, (8 * K.v + w) * 8 * pitch + col, RA + (8 * K.v + w) * 1024); });
    dma16(lane * 16, sP, hd * 1568 + (w & 1) * 1024, WPHI + (w & 1) * 1024);
  }
  if (bi >= 11) sfor<0, 7>([&](auto K) { dma16(rr * 3584 + wsw, sW3, (8 * K.v + w) * 8 * 3584 + (14 * par + bi - 2) * 128, R1 + (8 * K.v + w) * 1024); });
  if (tid < 16) ldsw<unsigned>(ZERO + tid * 4, 0u);
  wvm<0>();
  bar();
  cs_write(R2 + w * 128, colsum<7>(RA + (aperm ^ (ch * 64)) + tb * 7 * 2048), lane);
  if (bi >= 11) cs_write(R2 + 1024 + w * 128, colsum<7>(R1 + (aperm ^ (ch * 64)) + tb * 7 * 2048), lane);
  bar();
  const int cb0 = bi < 7 ? 28 + 7 * par + bi : 14 * par + bi - 7, cb1 = 14 * par + bi - 2;
  if (tb == 0 || (tb == 1 && bi >= 11)) {
    const f32x16 cs = cs_read(R2 + tb * 1024 + ch * 512 + h * 16, 128);
    sfor<0, 4>([&](auto A) { st16s<A.v * 32>(__builtin_bit_cast(u32x4, quad(cs, A.v)), sWS, t32 == 0 ? h * 16 : OOB, OFF_CS3 + (tb ? cb1 : cb0) * 256 + ch * 128); });
    wvm<0>();
  }
  bar();
  publish(sWS, tg, FL_CS + (tid ? cb1 : cb0), tid == 0 || (tid == 1 && bi >= 11));
  ST(2);
  const int gq = 8 * hf + w;
  const unsigned qkvs = OFF_QKV + gq * 4 * 84 * 1024;
  const int b01 = (28 * hd + (lane & 1) * 27) >> 6;
  const int U0 = (28 * hd) >> 4, o = (28 * hd) & 15;
  const unsigned f2 = (o >= 8 ? 2 : 0) * 1024;
  const unsigned img = w * 18432;
  u32x4 wp[3];
  unsigned vw[6];
  sfor<0, 3>([&](auto I) {
    unsigned a[2];
    sfor<0, 2>([&](auto T) {
      const int d0 = 16 * I.v + 8 * T.v + 4 * h - o;
      const bool ok = d0 >= 0 && d0 < 28;
      a[T.v] = (ok ? WPHI + (d0 + q) * 56 : ZERO) + (16 * blk + 4 * pp) * 2;
      vw[2 * I.v + T.v] = ok ? img + 16384 + t32 * 64 + (((d0 >> 2) ^ ((t32 >> 1) & 7)) << 3) : DUMMY + lane * 8;
    });
    wp[I.v] = trfrag(a[0], a[1]);
  });
  u32x4 kp[4][3], vp[4][3], qp[4][3];
  auto gather = [&](u32x4 (&d)[4][3], unsigned base) {
    sfor<0, 4>([&](auto T) {
      const unsigned so = qkvs + (T.v * 84 + base + U0) * 1024;
      ld16s<0>(d[T.v][0], lane * 16, sWS, so); ld16s<1024>(d[T.v][1], lane * 16, sWS, so); ld16s<0>(d[T.v][2], lane * 16, sWS, so + f2);
    });
  };
  auto feat = [&](u32x4 (&k)[3], u32x4 (&out)[4]) {
    sfor<0, 3>([&](auto I) { tie(k[I.v]); });
    f32x16 f = mfma(wp[0], k[0], zero16());
    f = mfma(wp[1], k[1], f);
    f = mfma(wp[2], k[2], f);
    dual_softmax(f, h != 0, out);
  };
  const unsigned fq = gq * 14 + b01;
  poll(fl, 2, FL_A + fq + 7, tag);
  ST(3);
  gather(kp, 28);
  if (h) ldsw<u32x2>(img + 16384 + t32 * 64 + ((7 ^ ((t32 >> 1) & 7)) << 3), u32x2{0x3f80u, 0u});
  sfor<0, 4>([&](auto T) {
    wvm<9 - 3 * T.v>();
    u32x4 ph[4];
    feat(kp[T.v], ph);
    const unsigned pr = img + T.v * 4096 + t32 * 128, sw = h ^ (((t32 >> 1) & 1) << 3) ^ (t32 & 1) ^ (((t32 >> 2) & 3) << 1);
    sfor<0, 8>([&](auto A) { ldsw<u32x2>(pr + (((2 * A.v) ^ sw) << 3), u32x2{ph[A.v >> 1][2 * (A.v & 1)], ph[A.v >> 1][2 * (A.v & 1) + 1]}); });
  });
  ST(4);
  poll(fl, 4, FL_Q + fq + ((lane & 2) ? 7 : 0), tag);
  ST(5);
  gather(qp, 0);
  gather(vp, 56);
  u32x4 phq[4][4];
  wvm<21>();
  feat(qp[0], phq[0]);
  f32x16 kvp = zero16(), kvm = zero16();
  unsigned aa[2], ba[2];
  sfor<0, 2>([&](auto T) {
    const int row = 8 * h + 4 * T.v + q, pc = 4 * blk + pp;
    aa[T.v] = img + row * 128 + ((pc ^ ((q >> 1) << 3) ^ (q & 1) ^ ((2 * h + T.v) << 1)) << 3);
    ba[T.v] = img + 16384 + row * 64 + ((pc ^ (row >> 1)) << 3);
  });
  sfor<0, 4>([&](auto T) {
    wvm<9 - 3 * T.v>();
    sfor<0, 3>([&](auto I) { tie(vp[T.v][I.v]); });
    sfor<0, 6>([&](auto A) { ldsw<u32x2>(vw[A.v], u32x2{vp[T.v][A.v >> 1][2 * (A.v & 1)], vp[T.v][A.v >> 1][2 * (A.v & 1) + 1]}); });
    sfor<0, 2>([&](auto U) {
      constexpr unsigned oa = T.v * 4096 + U.v * 2048;
      const u32x4 bf = trfrag(ba[0] + U.v * 1024, ba[1] + U.v * 1024);
      kvp = mfma(trfrag(aa[0] + oa, aa[1] + oa), bf, kvp);
      kvm = mfma(trfrag((aa[0] ^ 64u) + oa, (aa[1] ^ 64u) + oa), bf, kvm);
    });
  });
  sfor<0, 8>([&](auto A) { ldsw<f32x4>(img + A.v * 1024 + lane * 16, quad(A.v < 4 ? kvp : kvm, A.v & 3)); });
  bar();
  f32x4 own = {0.f, 0.f, 0.f, 0.f};
  sfor<0, 8>([&](auto S) { own += ldsr<f32x4>(S.v * 18432 + w * 1024 + lane * 16); });
  st16s(__builtin_bit_cast(u32x4, own), sWS, lane * 16, OFF_KVP + ((hd * 2 + hf) * 8 + w) * 1024);
  feat(qp[1], phq[1]);
  wvm<0>();
  bar();
  publish(sWS, tg, FL_KV + hd * 2 + hf, tid == 0);
  ST(6);
  feat(qp[2], phq[2]);
  poll(fl, 1, FL_KV + hd * 2 + (hf ^ 1), tag);
  u32x4 oth;
  ld16s<0>(oth, lane * 16, sWS, OFF_KVP + ((hd * 2 + (hf ^ 1)) * 8 + w) * 1024);
  feat(qp[3], phq[3]);
  wvm<0>(oth);
  own += __builtin_bit_cast(f32x4, oth);
  ldsw<u32x2>(147456 + (w >> 1) * 1024 + lane * 16 + (w & 1) * 8, u32x2{pk2(own[0], own[1]), pk2(own[2], own[3])});
  bar();
  ST(7);
  u32x4 kvf[4];
  sfor<0, 4>([&](auto I) { kvf[I.v] = ldsr<u32x4>(147456 + I.v * 1024 + lane * 16); });
  sfor<0, 4>([&](auto T) {
    f32x16 n = mfma(kvf[0], phq[T.v][0], zero16());
    n = mfma(kvf[1], phq[T.v][1], n);
    n = mfma(kvf[2], phq[T.v][2], n);
    n = mfma(kvf[3], phq[T.v][3], n);
    const float rd = __builtin_amdgcn_rcpf(swap_hi(n[12]) + 1e-6f);
#pragma unroll
    for (int i = 0; i < 16; ++i) n[i] *= rd;
    u32x4 o1 = pack8(n, 1);
    if (h) { o1[2] = 0u; o1[3] = 0u; }
    const unsigned so = OFF_O + ((4 * gq + T.v) * 32 + 2 * hd) * 1024;
    st16s(pack8(n, 0), sWS, lane * 16, so); st16s<1024>(o1, sWS, lane * 16, so);
  });
  wvm<0>();
  publish(sWS, tg, FL_O + gq * 16 + hd, lane == 0);
  ST(8);
  if (xc == 0 && rs == 0 && w == 0) {
    sfor<0, 4>([&](auto I) { poll(fl, 56, FL_Q + 56 * I.v + lane, tag); });
    publish(sWS, tg, FL_E, lane == 0);
  }
}

__global__ __launch_bounds__(512, 2) void fused(Params p) {
  u64 pc;
  asm volatile("s_getpc_b64 %0" : "=s"(pc));
  ST(0);
  int w = __builtin_amdgcn_readfirstlane(threadIdx.x >> 6);
  asm volatile("s_nop 4" : "+s"(w));
  const int xc = blockIdx.x & 7, r = blockIdx.x >> 3;
  unsigned dummy = 0;
  if (r < 28 && w >= 4) first_loads(p, w - 4, xc, r);
  if (r < 28 && w < 4) code_prefetch(dummy, pc, (((r & 1) * 4 + w) * 64 + (threadIdx.x & 63)) * 128);
  SB();
  if (r < 28) {
    if (w < 4) compute_path(p, dummy, w, xc, r STPASS);
    else loader_path(p, w - 4, xc, r STPASS);
  }
  else spare_path(p, w, xc, r STPASS);
}

extern "C" void kernel_launch(void* const* d_in, const int* in_sizes, int n_in, void* d_out, int out_size, void* d_ws, size_t ws_size,
                              hipStream_t stream) {
  Params p;
  p.x = (const uint16_t*)d_in[0]; p.wqkv = (const uint16_t*)d_in[1]; p.wphi = (const uint16_t*)d_in[2]; p.wo = (const uint16_t*)d_in[3];
  p.w1 = (const uint16_t*)d_in[5]; p.w2 = (const uint16_t*)d_in[6]; p.w3 = (const uint16_t*)d_in[7]; p.w4 = (const uint16_t*)d_in[8];
  p.out = (uint16_t*)d_out; p.ws = (char*)d_ws + ((ws_size - WS_USED) & ~(size_t)0xFFFFF);
  (void)hipFuncSetAttribute((const void*)fused, hipFuncAttributeMaxDynamicSharedMemorySize, LDS_BYTES);
  fused<<<256, 512, LDS_BYTES, stream>>>(p);
}
```
